```python
import torch

def _softmax(x, axis=-1):
    return torch.softmax(x, dim=axis)
def _gelu(x):
    return torch.nn.functional.gelu(x, approximate="tanh")
def _silu(x):
    return torch.nn.functional.silu(x)
def _sigmoid(x):
    return torch.sigmoid(x)
def _layernorm(x, axis=-1, eps=1e-5):
    m = x.mean(dim=axis, keepdim=True)
    v = x.var(dim=axis, keepdim=True, unbiased=False)
    return (x - m) / torch.sqrt(v + eps)
def _scatter_add(tgt, idx, src, axis=0):
    return tgt.clone().index_add_(axis, idx, src)
def _segment_sum(src, seg, n):
    out = torch.zeros((n,) + src.shape[1:], dtype=src.dtype)
    return out.index_add_(0, seg, src)
def _pad(x, pw):
    flat = tuple(v for pair in reversed(pw) for v in pair)
    return torch.nn.functional.pad(x, flat)

kernel_name = 'parallel_sum__spectral_gfnet__dense_activation_mish__prompt'

def setup_inputs():

    _w = (lambda a: (lambda t: t.to(torch.bfloat16) if t.is_floating_point() else t)(torch.as_tensor(a)))
    def _A_setup():
        B, S, D = 1, 2048, 576
        Df = D // 2 + 1
        return {
            "x": _w((lambda *s: torch.randn(*s, dtype=torch.bfloat16) * 0.05)(B, S, D)),
            "w_r": _w((lambda *s: torch.randn(*s, dtype=torch.bfloat16) * 0.8)(S, Df) * 0.02),
            "w_i": _w((lambda *s: torch.randn(*s, dtype=torch.bfloat16) * 0.8)(S, Df) * 0.02),
        }
    _A_in = _A_setup()

    def _B_setup():
        B, S, D, F = 1, 2048, 576, 4608
        return {
            "x": _w((lambda *s: torch.randn(*s, dtype=torch.bfloat16) * 0.05)(B, S, D)),
            "w1": _w((lambda *s: torch.randn(*s, dtype=torch.bfloat16) * 0.8)(D, F) * (D ** -0.5)),
            "b1": _w((lambda *s: torch.randn(*s, dtype=torch.bfloat16) * 0.2)(F)),
            "w2": _w((lambda *s: torch.randn(*s, dtype=torch.bfloat16) * 0.1)(F, D) * (F ** -0.5)),
        }
    _B_in = _B_setup()

    return {
        "A_x": _A_in["x"],
        "A_w_r": _A_in["w_r"],
        "A_w_i": _A_in["w_i"],
        "B_x": _B_in["x"],
        "B_w1": _B_in["w1"],
        "B_b1": _B_in["b1"],
        "B_w2": _B_in["w2"],
    }

def reference(A_x, A_w_r, A_w_i, B_x, B_w1, B_b1, B_w2):
    def _A_ref(x, w_r, w_i):
        B, S, D = x.shape
        X = (lambda x, axes=(-2,-1): torch.fft.rfft2(x.float(), dim=axes))(x, axes=(-2, -1))
        W = w_r + 1j * w_i
        X = X * W
        y = (lambda x, s, axes=(-2,-1): torch.fft.irfft2(x, s=s, dim=axes).to(torch.bfloat16))(X, (S, D), axes=(-2, -1))
        return y

    def _B_ref(x, w1, b1, w2):
        h = torch.einsum("bsd,df->bsf", x, w1) + b1
        sp = torch.log1p(torch.exp(-torch.abs(h))) + torch.where(h > 0, h, torch.zeros_like(h))
        act = h * torch.tanh(sp)
        return torch.einsum("bsf,fd->bsd", act, w2)

    x = A_x
    xn = _layernorm(x, axis=-1)
    return x + _A_ref(xn, A_w_r, A_w_i) + _B_ref(xn, B_w1, B_b1, B_w2)
```

```cpp
#include <hip/hip_runtime.h>

struct Args { const void* x; const void* wr; const void* wi; const void* w1; const void* b1; const void* w2; void* out; void* ws; };

__global__ __launch_bounds__(256, 1) void gfmlp_kernel(Args a) {
  asm volatile(
    "v_and_b32 v1, 63, %9\n\t"
    "v_lshrrev_b32 v13, 6, %9\n\t"
    "s_nop 0\n\t"
    "v_readfirstlane_b32 s64, v13\n\t"
    "s_bfe_u32 s65, %8, 262147\n\t"
    "s_and_b32 s70, %8, 1\n\t"
    "s_lshl_b32 s70, s70, 4\n\t"
    "s_add_u32 s65, s65, s70\n\t"
    "s_bfe_u32 s66, %8, 131073\n\t"
    "s_lshl_b32 s66, s66, 1\n\t"
    "s_lshr_b32 s70, %8, 7\n\t"
    "s_add_u32 s66, s66, s70\n\t"
    "s_mov_b64 s[36:37], %0\n\t"
    "s_and_b32 s37, s37, 0xffff\n\t"
    "s_mov_b32 s38, 2359296\n\t"
    "s_mov_b32 s39, 0x20000\n\t"
    "s_lshl_b32 s70, s64, 4\n\t"
    "v_lshrrev_b32 v13, 3, v1\n\t"
    "v_add_u32 v13, s70, v13\n\t"
    "v_lshlrev_b32 v14, 5, v13\n\t"
    "v_add_u32 v14, s65, v14\n\t"
    "v_mul_u32_u24 v14, 1152, v14\n\t"
    "v_and_b32 v15, 7, v1\n\t"
    "v_lshl_add_u32 v14, v15, 4, v14\n\t"
    "v_add_u32 v12, 294912, v14\n\t"
    "buffer_load_dwordx4 v[132:135], v14, s[36:39], 0 offen offset:0\n\t"
    "buffer_load_dwordx4 v[136:139], v14, s[36:39], 0 offen offset:128\n\t"
    "buffer_load_dwordx4 v[140:143], v14, s[36:39], 0 offen offset:256\n\t"
    "buffer_load_dwordx4 v[144:147], v14, s[36:39], 0 offen offset:384\n\t"
    "buffer_load_dwordx4 v[148:151], v14, s[36:39], 0 offen offset:512\n\t"
    "buffer_load_dwordx4 v[152:155], v14, s[36:39], 0 offen offset:640\n\t"
    "buffer_load_dwordx4 v[156:159], v14, s[36:39], 0 offen offset:768\n\t"
    "buffer_load_dwordx4 v[160:163], v14, s[36:39], 0 offen offset:896\n\t"
    "buffer_load_dwordx4 v[164:167], v14, s[36:39], 0 offen offset:1024\n\t"
    "v_mul_u32_u24 v7, 1168, v13\n\t"
    "v_lshl_add_u32 v7, v15, 4, v7\n\t"
    "s_mov_b64 s[40:41], %7\n\t"
    "s_and_b32 s41, s41, 0xffff\n\t"
    "s_mov_b32 s42, 25427968\n\t"
    "s_mov_b32 s43, 0x20000\n\t"
    "buffer_load_dword v11, off, s[40:43], 0 sc1\n\t"
    "s_mov_b64 s[60:61], %4\n\t"
    "s_and_b32 s61, s61, 0xffff\n\t"
    "s_mov_b32 s62, 9216\n\t"
    "s_mov_b32 s63, 0x20000\n\t"
    "s_lshl_b32 s70, s64, 6\n\t"
    "v_add_u32 v6, s70, v1\n\t"
    "s_mul_i32 s71, s66, 1152\n\t"
    "v_lshlrev_b32 v14, 1, v6\n\t"
    "v_mov_b32 v17, 2146435072\n\t"
    "v_cmp_gt_u32 vcc, 64, v6\n\t"
    "v_add_u32 v16, 1024, v14\n\t"
    "s_nop 0\n\t"
    "v_cndmask_b32 v16, v17, v16, vcc\n\t"
    "buffer_load_ushort v34, v14, s[60:63], s71 offen\n\t"
    "buffer_load_ushort v35, v14, s[60:63], s71 offen offset:512\n\t"
    "buffer_load_ushort v36, v16, s[60:63], s71 offen\n\t"
    "v_cmp_ne_u32 vcc, 0, v1\n\t"
    "v_mov_b32 v10, 0\n\t"
    "s_nop 0\n\t"
    "v_cndmask_b32 v10, v10, v17, vcc\n\t"
    "s_cmp_lg_u32 s64, 0\n\t"
    "s_cbranch_scc1 L_nocode_1%=\n\t"
    "s_getpc_b64 s[60:61]\n\t"
    "L_pc%=:\n\t"
    "s_and_b32 s61, s61, 0xffff\n\t"
    "s_mov_b32 s62, L_end%=-L_pc%=\n\t"
    "s_lshr_b32 s70, %8, 3\n\t"
    "v_lshlrev_b32 v14, 5, v1\n\t"
    "v_add_u32 v14, s70, v14\n\t"
    "v_lshlrev_b32 v14, 6, v14\n\t"
    "v_cmp_gt_u32 vcc, 34, v1\n\t"
    "s_nop 1\n\t"
    "v_cndmask_b32 v14, v17, v14, vcc\n\t"
    "s_mov_b32 m0, 145728\n\t"
    "s_nop 0\n\t"
    "buffer_load_dword v14, s[60:63], 0 offen lds\n\t"
    "v_mov_b32 v14, 145664\n\t"
    "v_mov_b32 v15, 0\n\t"
    "ds_write_b32 v14, v15 offset:0\n\t"
    "ds_write_b32 v14, v15 offset:4\n\t"
    "ds_write_b32 v14, v15 offset:8\n\t"
    "ds_write_b32 v14, v15 offset:12\n\t"
    "L_nocode_1%=:\n\t"
    "s_mov_b64 s[44:45], %3\n\t"
    "s_and_b32 s45, s45, 0xffff\n\t"
    "s_mov_b32 s46, 5308416\n\t"
    "s_mov_b32 s47, 0x20000\n\t"
    "s_mov_b64 s[48:49], %5\n\t"
    "s_and_b32 s49, s49, 0xffff\n\t"
    "s_mov_b32 s50, 5308416\n\t"
    "s_mov_b32 s51, 0x20000\n\t"
    "s_mov_b64 s[52:53], %1\n\t"
    "s_and_b32 s53, s53, 0xffff\n\t"
    "s_mov_b32 s54, 1183744\n\t"
    "s_mov_b32 s55, 0x20000\n\t"
    "s_mov_b64 s[56:57], %2\n\t"
    "s_and_b32 s57, s57, 0xffff\n\t"
    "s_mov_b32 s58, 1183744\n\t"
    "s_mov_b32 s59, 0x20000\n\t"
    "s_lshl_b32 s70, s64, 6\n\t"
    "v_add_u32 v13, s70, v1\n\t"
    "v_lshrrev_b32 v14, 2, v13\n\t"
    "v_and_b32 v15, 3, v13\n\t"
    "v_lshlrev_b32 v8, 4, v13\n\t"
    "v_add_u32 v8, 132352, v8\n\t"
    "v_mul_u32_u24 v9, 6144, v14\n\t"
    "v_lshl_add_u32 v9, v15, 4, v9\n\t"
    "v_mul_u32_u24 v75, 192, v14\n\t"
    "v_lshl_add_u32 v75, v15, 4, v75\n\t"
    "v_add_u32 v75, 151552, v75\n\t"
    "v_lshrrev_b32 v14, 3, v6\n\t"
    "v_and_b32 v15, 7, v6\n\t"
    "v_cvt_f32_u32 v38, v14\n\t"
    "v_mul_f32 v39, 0x3d000000, v38\n\t"
    "v_cvt_f32_u32 v40, v15\n\t"
    "v_mul_f32 v40, 4.0, v40\n\t"
    "v_mul_f32 v38, v40, v39\n\t"
    "v_cos_f32 v46, v38\n\t"
    "v_sin_f32 v48, v38\n\t"
    "v_add_f32 v38, v38, v39\n\t"
    "v_cos_f32 v47, v38\n\t"
    "v_sin_f32 v49, v38\n\t"
    "v_add_f32 v38, v38, v39\n\t"
    "v_cvt_pk_bf16_f32 v42, v46, v47\n\t"
    "v_cvt_pk_bf16_f32 v44, v48, v49\n\t"
    "v_cos_f32 v46, v38\n\t"
    "v_sin_f32 v48, v38\n\t"
    "v_add_f32 v38, v38, v39\n\t"
    "v_cos_f32 v47, v38\n\t"
    "v_sin_f32 v49, v38\n\t"
    "v_cvt_pk_bf16_f32 v43, v46, v47\n\t"
    "v_cvt_pk_bf16_f32 v45, v48, v49\n\t"
    "v_lshlrev_b32 v14, 6, v14\n\t"
    "v_lshl_add_u32 v14, v15, 3, v14\n\t"
    "v_xor_b32 v46, 0x80008000, v44\n\t"
    "v_xor_b32 v47, 0x80008000, v45\n\t"
    "v_add_u32 v14, 126208, v14\n\t"
    "ds_write_b64 v14, v[42:43]\n\t"
    "ds_write_b64 v14, v[44:45] offset:2048\n\t"
    "ds_write_b64 v14, v[46:47] offset:4096\n\t"
    "s_lshl_b32 s70, s64, 4\n\t"
    "v_and_b32 v14, 15, v1\n\t"
    "v_add_u32 v14, s70, v14\n\t"
    "v_lshrrev_b32 v15, 4, v1\n\t"
    "v_lshlrev_b32 v15, 8, v15\n\t"
    "v_add_u32 v15, s65, v15\n\t"
    "v_cvt_f32_u32 v39, v14\n\t"
    "v_mul_f32 v39, 0x3c800000, v39\n\t"
    "v_add_u32 v16, 0, v15\n\t"
    "v_mul_u32_u24 v16, v16, v14\n\t"
    "v_cvt_f32_u32 v38, v16\n\t"
    "v_mul_f32 v38, 0x3a000000, v38\n\t"
    "v_cos_f32 v46, v38\n\t"
    "v_sin_f32 v48, v38\n\t"
    "v_add_f32 v38, v38, v39\n\t"
    "v_cos_f32 v47, v38\n\t"
    "v_sin_f32 v49, v38\n\t"
    "v_add_f32 v38, v38, v39\n\t"
    "v_cvt_pk_bf16_f32 v18, v46, v47\n\t"
    "v_cvt_pk_bf16_f32 v26, v48, v49\n\t"
    "v_cos_f32 v46, v38\n\t"
    "v_sin_f32 v48, v38\n\t"
    "v_add_f32 v38, v38, v39\n\t"
    "v_cos_f32 v47, v38\n\t"
    "v_sin_f32 v49, v38\n\t"
    "v_add_f32 v38, v38, v39\n\t"
    "v_cvt_pk_bf16_f32 v19, v46, v47\n\t"
    "v_cvt_pk_bf16_f32 v27, v48, v49\n\t"
    "v_cos_f32 v46, v38\n\t"
    "v_sin_f32 v48, v38\n\t"
    "v_add_f32 v38, v38, v39\n\t"
    "v_cos_f32 v47, v38\n\t"
    "v_sin_f32 v49, v38\n\t"
    "v_add_f32 v38, v38, v39\n\t"
    "v_cvt_pk_bf16_f32 v20, v46, v47\n\t"
    "v_cvt_pk_bf16_f32 v28, v48, v49\n\t"
    "v_cos_f32 v46, v38\n\t"
    "v_sin_f32 v48, v38\n\t"
    "v_add_f32 v38, v38, v39\n\t"
    "v_cos_f32 v47, v38\n\t"
    "v_sin_f32 v49, v38\n\t"
    "v_cvt_pk_bf16_f32 v21, v46, v47\n\t"
    "v_cvt_pk_bf16_f32 v29, v48, v49\n\t"
    "v_add_u32 v16, 1024, v15\n\t"
    "v_mul_u32_u24 v16, v16, v14\n\t"
    "v_cvt_f32_u32 v38, v16\n\t"
    "v_mul_f32 v38, 0x3a000000, v38\n\t"
    "v_cos_f32 v46, v38\n\t"
    "v_sin_f32 v48, v38\n\t"
    "v_add_f32 v38, v38, v39\n\t"
    "v_cos_f32 v47, v38\n\t"
    "v_sin_f32 v49, v38\n\t"
    "v_add_f32 v38, v38, v39\n\t"
    "v_cvt_pk_bf16_f32 v22, v46, v47\n\t"
    "v_cvt_pk_bf16_f32 v30, v48, v49\n\t"
    "v_cos_f32 v46, v38\n\t"
    "v_sin_f32 v48, v38\n\t"
    "v_add_f32 v38, v38, v39\n\t"
    "v_cos_f32 v47, v38\n\t"
    "v_sin_f32 v49, v38\n\t"
    "v_add_f32 v38, v38, v39\n\t"
    "v_cvt_pk_bf16_f32 v23, v46, v47\n\t"
    "v_cvt_pk_bf16_f32 v31, v48, v49\n\t"
    "v_cos_f32 v46, v38\n\t"
    "v_sin_f32 v48, v38\n\t"
    "v_add_f32 v38, v38, v39\n\t"
    "v_cos_f32 v47, v38\n\t"
    "v_sin_f32 v49, v38\n\t"
    "v_add_f32 v38, v38, v39\n\t"
    "v_cvt_pk_bf16_f32 v24, v46, v47\n\t"
    "v_cvt_pk_bf16_f32 v32, v48, v49\n\t"
    "v_cos_f32 v46, v38\n\t"
    "v_sin_f32 v48, v38\n\t"
    "v_add_f32 v38, v38, v39\n\t"
    "v_cos_f32 v47, v38\n\t"
    "v_sin_f32 v49, v38\n\t"
    "v_cvt_pk_bf16_f32 v25, v46, v47\n\t"
    "v_cvt_pk_bf16_f32 v33, v48, v49\n\t"
    "v_and_b32 v14, 31, v1\n\t"
    "v_and_b32 v15, 15, v1\n\t"
    "v_add_u32 v15, 32, v15\n\t"
    "s_cmp_eq_u32 s64, 2\n\t"
    "s_cselect_b64 vcc, -1, 0\n\t"
    "v_cndmask_b32 v15, v14, v15, vcc\n\t"
    "s_mul_i32 s70, s66, 36\n\t"
    "v_add_u32 v15, s70, v15\n\t"
    "v_cvt_f32_u32 v38, v15\n\t"
    "v_mul_f32 v38, 0x3ae38e39, v38\n\t"
    "v_cmp_lt_u32 vcc, 15, v14\n\t"
    "s_cmp_eq_u32 s64, 2\n\t"
    "s_cselect_b64 s[74:75], vcc, 0\n\t"
    "s_cmp_eq_u32 s64, 1\n\t"
    "s_cselect_b64 vcc, -1, s[74:75]\n\t"
    "v_mov_b32 v39, 0\n\t"
    "v_mov_b32 v40, 0x3e800000\n\t"
    "v_cndmask_b32 v39, v39, v40, vcc\n\t"
    "v_lshrrev_b32 v14, 5, v1\n\t"
    "v_lshlrev_b32 v14, 3, v14\n\t"
    "v_cvt_f32_u32 v40, v14\n\t"
    "v_fma_f32 v41, v38, v40, v39\n\t"
    "v_mul_f32 v42, 0x41800000, v38\n\t"
    "v_sub_f32 v43, v41, v42\n\t"
    "v_cos_f32 v76, v41\n\t"
    "v_cos_f32 v84, v43\n\t"
    "v_add_f32 v41, v41, v38\n\t"
    "v_add_f32 v43, v43, v38\n\t"
    "v_cos_f32 v77, v41\n\t"
    "v_cos_f32 v85, v43\n\t"
    "v_add_f32 v41, v41, v38\n\t"
    "v_add_f32 v43, v43, v38\n\t"
    "v_cos_f32 v78, v41\n\t"
    "v_cos_f32 v86, v43\n\t"
    "v_add_f32 v41, v41, v38\n\t"
    "v_add_f32 v43, v43, v38\n\t"
    "v_cos_f32 v79, v41\n\t"
    "v_cos_f32 v87, v43\n\t"
    "v_add_f32 v41, v41, v38\n\t"
    "v_add_f32 v43, v43, v38\n\t"
    "v_cos_f32 v80, v41\n\t"
    "v_cos_f32 v88, v43\n\t"
    "v_add_f32 v41, v41, v38\n\t"
    "v_add_f32 v43, v43, v38\n\t"
    "v_cos_f32 v81, v41\n\t"
    "v_cos_f32 v89, v43\n\t"
    "v_add_f32 v41, v41, v38\n\t"
    "v_add_f32 v43, v43, v38\n\t"
    "v_cos_f32 v82, v41\n\t"
    "v_cos_f32 v90, v43\n\t"
    "v_add_f32 v41, v41, v38\n\t"
    "v_add_f32 v43, v43, v38\n\t"
    "v_cos_f32 v83, v41\n\t"
    "v_cos_f32 v91, v43\n\t"
    "v_cos_f32 v92, v42\n\t"
    "s_nop 0\n\t"
    "v_add_f32 v92, v92, v92\n\t"
    "v_mov_b32 v93, v92\n\t"
    "v_mul_f32 v76, 0x3f638e39, v76\n\t"
    "v_mul_f32 v84, 0x3f638e39, v84\n\t"
    "v_mul_f32 v77, 0x3f638e39, v77\n\t"
    "v_mul_f32 v85, 0x3f638e39, v85\n\t"
    "v_mul_f32 v78, 0x3f638e39, v78\n\t"
    "v_mul_f32 v86, 0x3f638e39, v86\n\t"
    "v_mul_f32 v79, 0x3f638e39, v79\n\t"
    "v_mul_f32 v87, 0x3f638e39, v87\n\t"
    "v_mul_f32 v80, 0x3f638e39, v80\n\t"
    "v_mul_f32 v88, 0x3f638e39, v88\n\t"
    "v_mul_f32 v81, 0x3f638e39, v81\n\t"
    "v_mul_f32 v89, 0x3f638e39, v89\n\t"
    "v_mul_f32 v82, 0x3f638e39, v82\n\t"
    "v_mul_f32 v90, 0x3f638e39, v90\n\t"
    "v_mul_f32 v83, 0x3f638e39, v83\n\t"
    "v_mul_f32 v91, 0x3f638e39, v91\n\t"
    "buffer_load_dwordx4 v[168:171], v12, s[36:39], 0 offen offset:0\n\t"
    "buffer_load_dwordx4 v[172:175], v12, s[36:39], 0 offen offset:128\n\t"
    "buffer_load_dwordx4 v[176:179], v12, s[36:39], 0 offen offset:256\n\t"
    "buffer_load_dwordx4 v[180:183], v12, s[36:39], 0 offen offset:384\n\t"
    "buffer_load_dwordx4 v[184:187], v12, s[36:39], 0 offen offset:512\n\t"
    "buffer_load_dwordx4 v[188:191], v12, s[36:39], 0 offen offset:640\n\t"
    "buffer_load_dwordx4 v[192:195], v12, s[36:39], 0 offen offset:768\n\t"
    "buffer_load_dwordx4 v[196:199], v12, s[36:39], 0 offen offset:896\n\t"
    "buffer_load_dwordx4 v[200:203], v12, s[36:39], 0 offen offset:1024\n\t"
    "s_lshl_b32 s70, s64, 6\n\t"
    "v_add_u32 v13, s70, v1\n\t"
    "v_mul_u32_u24 v14, 13108, v13\n\t"
    "v_lshrrev_b32 v14, 16, v14\n\t"
    "v_mul_u32_u24 v15, 5, v14\n\t"
    "v_sub_u32 v15, v13, v15\n\t"
    "v_and_b32 v16, 31, v14\n\t"
    "v_lshlrev_b32 v16, 6, v16\n\t"
    "v_lshrrev_b32 v17, 5, v14\n\t"
    "s_lshl_b32 s71, s65, 1\n\t"
    "v_add_u32 v16, s71, v16\n\t"
    "v_add_u32 v16, v16, v17\n\t"
    "v_mul_u32_u24 v16, 289, v16\n\t"
    "s_mul_i32 s71, s66, 36\n\t"
    "v_add_u32 v16, s71, v16\n\t"
    "v_sub_u32 v16, v16, v17\n\t"
    "v_lshlrev_b32 v16, 1, v16\n\t"
    "v_lshl_add_u32 v16, v15, 4, v16\n\t"
    "buffer_load_dwordx4 v[104:107], v16, s[52:55], 0 offen\n\t"
    "buffer_load_dwordx4 v[108:111], v16, s[56:59], 0 offen\n\t"
    "s_lshl_b32 s70, s64, 6\n\t"
    "v_add_u32 v13, s70, v1\n\t"
    "v_add_u32 v13, 256, v13\n\t"
    "v_mul_u32_u24 v14, 13108, v13\n\t"
    "v_lshrrev_b32 v14, 16, v14\n\t"
    "v_mul_u32_u24 v15, 5, v14\n\t"
    "v_sub_u32 v15, v13, v15\n\t"
    "v_and_b32 v16, 31, v14\n\t"
    "v_lshlrev_b32 v16, 6, v16\n\t"
    "v_lshrrev_b32 v17, 5, v14\n\t"
    "s_lshl_b32 s71, s65, 1\n\t"
    "v_add_u32 v16, s71, v16\n\t"
    "v_add_u32 v16, v16, v17\n\t"
    "v_mul_u32_u24 v16, 289, v16\n\t"
    "s_mul_i32 s71, s66, 36\n\t"
    "v_add_u32 v16, s71, v16\n\t"
    "v_sub_u32 v16, v16, v17\n\t"
    "v_lshlrev_b32 v16, 1, v16\n\t"
    "v_lshl_add_u32 v16, v15, 4, v16\n\t"
    "v_cmp_gt_u32 vcc, 320, v13\n\t"
    "v_mov_b32 v17, 2146435072\n\t"
    "s_nop 0\n\t"
    "v_cndmask_b32 v16, v17, v16, vcc\n\t"
    "buffer_load_dwordx4 v[112:115], v16, s[52:55], 0 offen\n\t"
    "buffer_load_dwordx4 v[116:119], v16, s[56:59], 0 offen\n\t"
    "v_mov_b32 v38, 0\n\t"
    "v_mov_b32 v39, 0\n\t"
    "v_mov_b32 v40, 0\n\t"
    "v_mov_b32 v41, 0\n\t"
    "s_waitcnt vmcnt(25)\n\t"
    "v_dot2c_f32_bf16 v38, 0x3f803f80, v132\n\t"
    "v_dot2c_f32_bf16 v40, v132, v132\n\t"
    "v_dot2c_f32_bf16 v39, 0x3f803f80, v133\n\t"
    "v_dot2c_f32_bf16 v41, v133, v133\n\t"
    "v_dot2c_f32_bf16 v38, 0x3f803f80, v134\n\t"
    "v_dot2c_f32_bf16 v40, v134, v134\n\t"
    "v_dot2c_f32_bf16 v39, 0x3f803f80, v135\n\t"
    "v_dot2c_f32_bf16 v41, v135, v135\n\t"
    "s_waitcnt vmcnt(24)\n\t"
    "v_dot2c_f32_bf16 v38, 0x3f803f80, v136\n\t"
    "v_dot2c_f32_bf16 v40, v136, v136\n\t"
    "v_dot2c_f32_bf16 v39, 0x3f803f80, v137\n\t"
    "v_dot2c_f32_bf16 v41, v137, v137\n\t"
    "v_dot2c_f32_bf16 v38, 0x3f803f80, v138\n\t"
    "v_dot2c_f32_bf16 v40, v138, v138\n\t"
    "v_dot2c_f32_bf16 v39, 0x3f803f80, v139\n\t"
    "v_dot2c_f32_bf16 v41, v139, v139\n\t"
    "s_waitcnt vmcnt(23)\n\t"
    "v_dot2c_f32_bf16 v38, 0x3f803f80, v140\n\t"
    "v_dot2c_f32_bf16 v40, v140, v140\n\t"
    "v_dot2c_f32_bf16 v39, 0x3f803f80, v141\n\t"
    "v_dot2c_f32_bf16 v41, v141, v141\n\t"
    "v_dot2c_f32_bf16 v38, 0x3f803f80, v142\n\t"
    "v_dot2c_f32_bf16 v40, v142, v142\n\t"
    "v_dot2c_f32_bf16 v39, 0x3f803f80, v143\n\t"
    "v_dot2c_f32_bf16 v41, v143, v143\n\t"
    "s_waitcnt vmcnt(22)\n\t"
    "v_dot2c_f32_bf16 v38, 0x3f803f80, v144\n\t"
    "v_dot2c_f32_bf16 v40, v144, v144\n\t"
    "v_dot2c_f32_bf16 v39, 0x3f803f80, v145\n\t"
    "v_dot2c_f32_bf16 v41, v145, v145\n\t"
    "v_dot2c_f32_bf16 v38, 0x3f803f80, v146\n\t"
    "v_dot2c_f32_bf16 v40, v146, v146\n\t"
    "v_dot2c_f32_bf16 v39, 0x3f803f80, v147\n\t"
    "v_dot2c_f32_bf16 v41, v147, v147\n\t"
    "s_waitcnt vmcnt(21)\n\t"
    "v_dot2c_f32_bf16 v38, 0x3f803f80, v148\n\t"
    "v_dot2c_f32_bf16 v40, v148, v148\n\t"
    "v_dot2c_f32_bf16 v39, 0x3f803f80, v149\n\t"
    "v_dot2c_f32_bf16 v41, v149, v149\n\t"
    "v_dot2c_f32_bf16 v38, 0x3f803f80, v150\n\t"
    "v_dot2c_f32_bf16 v40, v150, v150\n\t"
    "v_dot2c_f32_bf16 v39, 0x3f803f80, v151\n\t"
    "v_dot2c_f32_bf16 v41, v151, v151\n\t"
    "s_waitcnt vmcnt(20)\n\t"
    "v_dot2c_f32_bf16 v38, 0x3f803f80, v152\n\t"
    "v_dot2c_f32_bf16 v40, v152, v152\n\t"
    "v_dot2c_f32_bf16 v39, 0x3f803f80, v153\n\t"
    "v_dot2c_f32_bf16 v41, v153, v153\n\t"
    "v_dot2c_f32_bf16 v38, 0x3f803f80, v154\n\t"
    "v_dot2c_f32_bf16 v40, v154, v154\n\t"
    "v_dot2c_f32_bf16 v39, 0x3f803f80, v155\n\t"
    "v_dot2c_f32_bf16 v41, v155, v155\n\t"
    "s_waitcnt vmcnt(19)\n\t"
    "v_dot2c_f32_bf16 v38, 0x3f803f80, v156\n\t"
    "v_dot2c_f32_bf16 v40, v156, v156\n\t"
    "v_dot2c_f32_bf16 v39, 0x3f803f80, v157\n\t"
    "v_dot2c_f32_bf16 v41, v157, v157\n\t"
    "v_dot2c_f32_bf16 v38, 0x3f803f80, v158\n\t"
    "v_dot2c_f32_bf16 v40, v158, v158\n\t"
    "v_dot2c_f32_bf16 v39, 0x3f803f80, v159\n\t"
    "v_dot2c_f32_bf16 v41, v159, v159\n\t"
    "s_waitcnt vmcnt(18)\n\t"
    "v_dot2c_f32_bf16 v38, 0x3f803f80, v160\n\t"
    "v_dot2c_f32_bf16 v40, v160, v160\n\t"
    "v_dot2c_f32_bf16 v39, 0x3f803f80, v161\n\t"
    "v_dot2c_f32_bf16 v41, v161, v161\n\t"
    "v_dot2c_f32_bf16 v38, 0x3f803f80, v162\n\t"
    "v_dot2c_f32_bf16 v40, v162, v162\n\t"
    "v_dot2c_f32_bf16 v39, 0x3f803f80, v163\n\t"
    "v_dot2c_f32_bf16 v41, v163, v163\n\t"
    "s_waitcnt vmcnt(17)\n\t"
    "v_dot2c_f32_bf16 v38, 0x3f803f80, v164\n\t"
    "v_dot2c_f32_bf16 v40, v164, v164\n\t"
    "v_dot2c_f32_bf16 v39, 0x3f803f80, v165\n\t"
    "v_dot2c_f32_bf16 v41, v165, v165\n\t"
    "v_dot2c_f32_bf16 v38, 0x3f803f80, v166\n\t"
    "v_dot2c_f32_bf16 v40, v166, v166\n\t"
    "v_dot2c_f32_bf16 v39, 0x3f803f80, v167\n\t"
    "v_dot2c_f32_bf16 v41, v167, v167\n\t"
    "s_nop 1\n\t"
    "v_add_f32 v38, v38, v39\n\t"
    "v_add_f32 v40, v40, v41\n\t"
    "s_nop 0\n\t"
    "v_add_f32_dpp v38, v38, v38 quad_perm:[1,0,3,2] row_mask:0xf bank_mask:0xf\n\t"
    "v_add_f32_dpp v40, v40, v40 quad_perm:[1,0,3,2] row_mask:0xf bank_mask:0xf\n\t"
    "s_nop 0\n\t"
    "v_add_f32_dpp v38, v38, v38 quad_perm:[2,3,0,1] row_mask:0xf bank_mask:0xf\n\t"
    "v_add_f32_dpp v40, v40, v40 quad_perm:[2,3,0,1] row_mask:0xf bank_mask:0xf\n\t"
    "s_nop 0\n\t"
    "v_add_f32_dpp v38, v38, v38 row_half_mirror row_mask:0xf bank_mask:0xf\n\t"
    "v_add_f32_dpp v40, v40, v40 row_half_mirror row_mask:0xf bank_mask:0xf\n\t"
    "v_mul_f32 v42, 0x3ae38e39, v38\n\t"
    "v_mul_f32 v43, v42, v42\n\t"
    "s_mov_b32 s75, 0x3ae38e39\n\t"
    "v_fma_f32 v40, v40, s75, -v43\n\t"
    "v_max_f32 v40, 0, v40\n\t"
    "v_cvt_pk_bf16_f32 v42, v42, v40\n\t"
    "v_and_b32 v43, 0xffff0000, v42\n\t"
    "v_lshlrev_b32 v42, 16, v42\n\t"
    "v_add_f32 v43, 0x3727c5ac, v43\n\t"
    "v_cvt_pk_bf16_f32 v43, v43, v43\n\t"
    "v_lshlrev_b32 v43, 16, v43\n\t"
    "v_sqrt_f32 v43, v43\n\t"
    "s_nop 0\n\t"
    "v_cvt_pk_bf16_f32 v43, v43, v43\n\t"
    "v_lshlrev_b32 v43, 16, v43\n\t"
    "v_rcp_f32 v43, v43\n\t"
    "s_nop 0\n\t"
    "v_mul_f32 v43, 0x3fb8aa3b, v43\n\t"
    "v_cvt_pk_bf16_f32 v43, v43, v43\n\t"
    "v_and_b32 v38, 0xffff, v43\n\t"
    "v_and_b32 v39, 0xffff0000, v43\n\t"
    "v_mul_f32 v42, v42, v39\n\t"
    "v_sub_f32 v42, 0, v42\n\t"
    "v_dot2_f32_bf16 v54, v132, v38, v42\n\t"
    "v_dot2_f32_bf16 v55, v132, v39, v42\n\t"
    "v_dot2_f32_bf16 v56, v133, v38, v42\n\t"
    "v_dot2_f32_bf16 v57, v133, v39, v42\n\t"
    "v_dot2_f32_bf16 v58, v134, v38, v42\n\t"
    "v_dot2_f32_bf16 v59, v134, v39, v42\n\t"
    "v_dot2_f32_bf16 v60, v135, v38, v42\n\t"
    "v_dot2_f32_bf16 v61, v135, v39, v42\n\t"
    "v_cvt_pk_bf16_f32 v46, v54, v55\n\t"
    "v_cvt_pk_bf16_f32 v47, v56, v57\n\t"
    "v_cvt_pk_bf16_f32 v48, v58, v59\n\t"
    "v_cvt_pk_bf16_f32 v49, v60, v61\n\t"
    "ds_write_b128 v7, v[46:49] offset:0\n\t"
    "v_dot2_f32_bf16 v54, v136, v38, v42\n\t"
    "v_dot2_f32_bf16 v55, v136, v39, v42\n\t"
    "v_dot2_f32_bf16 v56, v137, v38, v42\n\t"
    "v_dot2_f32_bf16 v57, v137, v39, v42\n\t"
    "v_dot2_f32_bf16 v58, v138, v38, v42\n\t"
    "v_dot2_f32_bf16 v59, v138, v39, v42\n\t"
    "v_dot2_f32_bf16 v60, v139, v38, v42\n\t"
    "v_dot2_f32_bf16 v61, v139, v39, v42\n\t"
    "v_cvt_pk_bf16_f32 v50, v54, v55\n\t"
    "v_cvt_pk_bf16_f32 v51, v56, v57\n\t"
    "v_cvt_pk_bf16_f32 v52, v58, v59\n\t"
    "v_cvt_pk_bf16_f32 v53, v60, v61\n\t"
    "ds_write_b128 v7, v[50:53] offset:128\n\t"
    "v_dot2_f32_bf16 v54, v140, v38, v42\n\t"
    "v_dot2_f32_bf16 v55, v140, v39, v42\n\t"
    "v_dot2_f32_bf16 v56, v141, v38, v42\n\t"
    "v_dot2_f32_bf16 v57, v141, v39, v42\n\t"
    "v_dot2_f32_bf16 v58, v142, v38, v42\n\t"
    "v_dot2_f32_bf16 v59, v142, v39, v42\n\t"
    "v_dot2_f32_bf16 v60, v143, v38, v42\n\t"
    "v_dot2_f32_bf16 v61, v143, v39, v42\n\t"
    "v_cvt_pk_bf16_f32 v46, v54, v55\n\t"
    "v_cvt_pk_bf16_f32 v47, v56, v57\n\t"
    "v_cvt_pk_bf16_f32 v48, v58, v59\n\t"
    "v_cvt_pk_bf16_f32 v49, v60, v61\n\t"
    "ds_write_b128 v7, v[46:49] offset:256\n\t"
    "v_dot2_f32_bf16 v54, v144, v38, v42\n\t"
    "v_dot2_f32_bf16 v55, v144, v39, v42\n\t"
    "v_dot2_f32_bf16 v56, v145, v38, v42\n\t"
    "v_dot2_f32_bf16 v57, v145, v39, v42\n\t"
    "v_dot2_f32_bf16 v58, v146, v38, v42\n\t"
    "v_dot2_f32_bf16 v59, v146, v39, v42\n\t"
    "v_dot2_f32_bf16 v60, v147, v38, v42\n\t"
    "v_dot2_f32_bf16 v61, v147, v39, v42\n\t"
    "v_cvt_pk_bf16_f32 v50, v54, v55\n\t"
    "v_cvt_pk_bf16_f32 v51, v56, v57\n\t"
    "v_cvt_pk_bf16_f32 v52, v58, v59\n\t"
    "v_cvt_pk_bf16_f32 v53, v60, v61\n\t"
    "ds_write_b128 v7, v[50:53] offset:384\n\t"
    "v_dot2_f32_bf16 v54, v148, v38, v42\n\t"
    "v_dot2_f32_bf16 v55, v148, v39, v42\n\t"
    "v_dot2_f32_bf16 v56, v149, v38, v42\n\t"
    "v_dot2_f32_bf16 v57, v149, v39, v42\n\t"
    "v_dot2_f32_bf16 v58, v150, v38, v42\n\t"
    "v_dot2_f32_bf16 v59, v150, v39, v42\n\t"
    "v_dot2_f32_bf16 v60, v151, v38, v42\n\t"
    "v_dot2_f32_bf16 v61, v151, v39, v42\n\t"
    "v_cvt_pk_bf16_f32 v46, v54, v55\n\t"
    "v_cvt_pk_bf16_f32 v47, v56, v57\n\t"
    "v_cvt_pk_bf16_f32 v48, v58, v59\n\t"
    "v_cvt_pk_bf16_f32 v49, v60, v61\n\t"
    "ds_write_b128 v7, v[46:49] offset:512\n\t"
    "v_dot2_f32_bf16 v54, v152, v38, v42\n\t"
    "v_dot2_f32_bf16 v55, v152, v39, v42\n\t"
    "v_dot2_f32_bf16 v56, v153, v38, v42\n\t"
    "v_dot2_f32_bf16 v57, v153, v39, v42\n\t"
    "v_dot2_f32_bf16 v58, v154, v38, v42\n\t"
    "v_dot2_f32_bf16 v59, v154, v39, v42\n\t"
    "v_dot2_f32_bf16 v60, v155, v38, v42\n\t"
    "v_dot2_f32_bf16 v61, v155, v39, v42\n\t"
    "v_cvt_pk_bf16_f32 v50, v54, v55\n\t"
    "v_cvt_pk_bf16_f32 v51, v56, v57\n\t"
    "v_cvt_pk_bf16_f32 v52, v58, v59\n\t"
    "v_cvt_pk_bf16_f32 v53, v60, v61\n\t"
    "ds_write_b128 v7, v[50:53] offset:640\n\t"
    "v_dot2_f32_bf16 v54, v156, v38, v42\n\t"
    "v_dot2_f32_bf16 v55, v156, v39, v42\n\t"
    "v_dot2_f32_bf16 v56, v157, v38, v42\n\t"
    "v_dot2_f32_bf16 v57, v157, v39, v42\n\t"
    "v_dot2_f32_bf16 v58, v158, v38, v42\n\t"
    "v_dot2_f32_bf16 v59, v158, v39, v42\n\t"
    "v_dot2_f32_bf16 v60, v159, v38, v42\n\t"
    "v_dot2_f32_bf16 v61, v159, v39, v42\n\t"
    "v_cvt_pk_bf16_f32 v46, v54, v55\n\t"
    "v_cvt_pk_bf16_f32 v47, v56, v57\n\t"
    "v_cvt_pk_bf16_f32 v48, v58, v59\n\t"
    "v_cvt_pk_bf16_f32 v49, v60, v61\n\t"
    "ds_write_b128 v7, v[46:49] offset:768\n\t"
    "v_dot2_f32_bf16 v54, v160, v38, v42\n\t"
    "v_dot2_f32_bf16 v55, v160, v39, v42\n\t"
    "v_dot2_f32_bf16 v56, v161, v38, v42\n\t"
    "v_dot2_f32_bf16 v57, v161, v39, v42\n\t"
    "v_dot2_f32_bf16 v58, v162, v38, v42\n\t"
    "v_dot2_f32_bf16 v59, v162, v39, v42\n\t"
    "v_dot2_f32_bf16 v60, v163, v38, v42\n\t"
    "v_dot2_f32_bf16 v61, v163, v39, v42\n\t"
    "v_cvt_pk_bf16_f32 v50, v54, v55\n\t"
    "v_cvt_pk_bf16_f32 v51, v56, v57\n\t"
    "v_cvt_pk_bf16_f32 v52, v58, v59\n\t"
    "v_cvt_pk_bf16_f32 v53, v60, v61\n\t"
    "ds_write_b128 v7, v[50:53] offset:896\n\t"
    "v_dot2_f32_bf16 v54, v164, v38, v42\n\t"
    "v_dot2_f32_bf16 v55, v164, v39, v42\n\t"
    "v_dot2_f32_bf16 v56, v165, v38, v42\n\t"
    "v_dot2_f32_bf16 v57, v165, v39, v42\n\t"
    "v_dot2_f32_bf16 v58, v166, v38, v42\n\t"
    "v_dot2_f32_bf16 v59, v166, v39, v42\n\t"
    "v_dot2_f32_bf16 v60, v167, v38, v42\n\t"
    "v_dot2_f32_bf16 v61, v167, v39, v42\n\t"
    "v_cvt_pk_bf16_f32 v46, v54, v55\n\t"
    "v_cvt_pk_bf16_f32 v47, v56, v57\n\t"
    "v_cvt_pk_bf16_f32 v48, v58, v59\n\t"
    "v_cvt_pk_bf16_f32 v49, v60, v61\n\t"
    "ds_write_b128 v7, v[46:49] offset:1024\n\t"
    "v_mov_b32 v38, 0\n\t"
    "v_mov_b32 v39, 0\n\t"
    "v_mov_b32 v40, 0\n\t"
    "v_mov_b32 v41, 0\n\t"
    "s_waitcnt vmcnt(12)\n\t"
    "v_dot2c_f32_bf16 v38, 0x3f803f80, v168\n\t"
    "v_dot2c_f32_bf16 v40, v168, v168\n\t"
    "v_dot2c_f32_bf16 v39, 0x3f803f80, v169\n\t"
    "v_dot2c_f32_bf16 v41, v169, v169\n\t"
    "v_dot2c_f32_bf16 v38, 0x3f803f80, v170\n\t"
    "v_dot2c_f32_bf16 v40, v170, v170\n\t"
    "v_dot2c_f32_bf16 v39, 0x3f803f80, v171\n\t"
    "v_dot2c_f32_bf16 v41, v171, v171\n\t"
    "s_waitcnt vmcnt(11)\n\t"
    "v_dot2c_f32_bf16 v38, 0x3f803f80, v172\n\t"
    "v_dot2c_f32_bf16 v40, v172, v172\n\t"
    "v_dot2c_f32_bf16 v39, 0x3f803f80, v173\n\t"
    "v_dot2c_f32_bf16 v41, v173, v173\n\t"
    "v_dot2c_f32_bf16 v38, 0x3f803f80, v174\n\t"
    "v_dot2c_f32_bf16 v40, v174, v174\n\t"
    "v_dot2c_f32_bf16 v39, 0x3f803f80, v175\n\t"
    "v_dot2c_f32_bf16 v41, v175, v175\n\t"
    "s_waitcnt vmcnt(10)\n\t"
    "v_dot2c_f32_bf16 v38, 0x3f803f80, v176\n\t"
    "v_dot2c_f32_bf16 v40, v176, v176\n\t"
    "v_dot2c_f32_bf16 v39, 0x3f803f80, v177\n\t"
    "v_dot2c_f32_bf16 v41, v177, v177\n\t"
    "v_dot2c_f32_bf16 v38, 0x3f803f80, v178\n\t"
    "v_dot2c_f32_bf16 v40, v178, v178\n\t"
    "v_dot2c_f32_bf16 v39, 0x3f803f80, v179\n\t"
    "v_dot2c_f32_bf16 v41, v179, v179\n\t"
    "s_waitcnt vmcnt(9)\n\t"
    "v_dot2c_f32_bf16 v38, 0x3f803f80, v180\n\t"
    "v_dot2c_f32_bf16 v40, v180, v180\n\t"
    "v_dot2c_f32_bf16 v39, 0x3f803f80, v181\n\t"
    "v_dot2c_f32_bf16 v41, v181, v181\n\t"
    "v_dot2c_f32_bf16 v38, 0x3f803f80, v182\n\t"
    "v_dot2c_f32_bf16 v40, v182, v182\n\t"
    "v_dot2c_f32_bf16 v39, 0x3f803f80, v183\n\t"
    "v_dot2c_f32_bf16 v41, v183, v183\n\t"
    "s_waitcnt vmcnt(8)\n\t"
    "v_dot2c_f32_bf16 v38, 0x3f803f80, v184\n\t"
    "v_dot2c_f32_bf16 v40, v184, v184\n\t"
    "v_dot2c_f32_bf16 v39, 0x3f803f80, v185\n\t"
    "v_dot2c_f32_bf16 v41, v185, v185\n\t"
    "v_dot2c_f32_bf16 v38, 0x3f803f80, v186\n\t"
    "v_dot2c_f32_bf16 v40, v186, v186\n\t"
    "v_dot2c_f32_bf16 v39, 0x3f803f80, v187\n\t"
    "v_dot2c_f32_bf16 v41, v187, v187\n\t"
    "s_waitcnt vmcnt(7)\n\t"
    "v_dot2c_f32_bf16 v38, 0x3f803f80, v188\n\t"
    "v_dot2c_f32_bf16 v40, v188, v188\n\t"
    "v_dot2c_f32_bf16 v39, 0x3f803f80, v189\n\t"
    "v_dot2c_f32_bf16 v41, v189, v189\n\t"
    "v_dot2c_f32_bf16 v38, 0x3f803f80, v190\n\t"
    "v_dot2c_f32_bf16 v40, v190, v190\n\t"
    "v_dot2c_f32_bf16 v39, 0x3f803f80, v191\n\t"
    "v_dot2c_f32_bf16 v41, v191, v191\n\t"
    "s_waitcnt vmcnt(6)\n\t"
    "v_dot2c_f32_bf16 v38, 0x3f803f80, v192\n\t"
    "v_dot2c_f32_bf16 v40, v192, v192\n\t"
    "v_dot2c_f32_bf16 v39, 0x3f803f80, v193\n\t"
    "v_dot2c_f32_bf16 v41, v193, v193\n\t"
    "v_dot2c_f32_bf16 v38, 0x3f803f80, v194\n\t"
    "v_dot2c_f32_bf16 v40, v194, v194\n\t"
    "v_dot2c_f32_bf16 v39, 0x3f803f80, v195\n\t"
    "v_dot2c_f32_bf16 v41, v195, v195\n\t"
    "s_waitcnt vmcnt(5)\n\t"
    "v_dot2c_f32_bf16 v38, 0x3f803f80, v196\n\t"
    "v_dot2c_f32_bf16 v40, v196, v196\n\t"
    "v_dot2c_f32_bf16 v39, 0x3f803f80, v197\n\t"
    "v_dot2c_f32_bf16 v41, v197, v197\n\t"
    "v_dot2c_f32_bf16 v38, 0x3f803f80, v198\n\t"
    "v_dot2c_f32_bf16 v40, v198, v198\n\t"
    "v_dot2c_f32_bf16 v39, 0x3f803f80, v199\n\t"
    "v_dot2c_f32_bf16 v41, v199, v199\n\t"
    "s_waitcnt vmcnt(4)\n\t"
    "v_dot2c_f32_bf16 v38, 0x3f803f80, v200\n\t"
    "v_dot2c_f32_bf16 v40, v200, v200\n\t"
    "v_dot2c_f32_bf16 v39, 0x3f803f80, v201\n\t"
    "v_dot2c_f32_bf16 v41, v201, v201\n\t"
    "v_dot2c_f32_bf16 v38, 0x3f803f80, v202\n\t"
    "v_dot2c_f32_bf16 v40, v202, v202\n\t"
    "v_dot2c_f32_bf16 v39, 0x3f803f80, v203\n\t"
    "v_dot2c_f32_bf16 v41, v203, v203\n\t"
    "s_nop 1\n\t"
    "v_add_f32 v38, v38, v39\n\t"
    "v_add_f32 v40, v40, v41\n\t"
    "s_nop 0\n\t"
    "v_add_f32_dpp v38, v38, v38 quad_perm:[1,0,3,2] row_mask:0xf bank_mask:0xf\n\t"
    "v_add_f32_dpp v40, v40, v40 quad_perm:[1,0,3,2] row_mask:0xf bank_mask:0xf\n\t"
    "s_nop 0\n\t"
    "v_add_f32_dpp v38, v38, v38 quad_perm:[2,3,0,1] row_mask:0xf bank_mask:0xf\n\t"
    "v_add_f32_dpp v40, v40, v40 quad_perm:[2,3,0,1] row_mask:0xf bank_mask:0xf\n\t"
    "s_nop 0\n\t"
    "v_add_f32_dpp v38, v38, v38 row_half_mirror row_mask:0xf bank_mask:0xf\n\t"
    "v_add_f32_dpp v40, v40, v40 row_half_mirror row_mask:0xf bank_mask:0xf\n\t"
    "v_mul_f32 v42, 0x3ae38e39, v38\n\t"
    "v_mul_f32 v43, v42, v42\n\t"
    "s_mov_b32 s75, 0x3ae38e39\n\t"
    "v_fma_f32 v40, v40, s75, -v43\n\t"
    "v_max_f32 v40, 0, v40\n\t"
    "v_cvt_pk_bf16_f32 v42, v42, v40\n\t"
    "v_and_b32 v43, 0xffff0000, v42\n\t"
    "v_lshlrev_b32 v42, 16, v42\n\t"
    "v_add_f32 v43, 0x3727c5ac, v43\n\t"
    "v_cvt_pk_bf16_f32 v43, v43, v43\n\t"
    "v_lshlrev_b32 v43, 16, v43\n\t"
    "v_sqrt_f32 v43, v43\n\t"
    "s_nop 0\n\t"
    "v_cvt_pk_bf16_f32 v43, v43, v43\n\t"
    "v_lshlrev_b32 v43, 16, v43\n\t"
    "v_rcp_f32 v43, v43\n\t"
    "s_nop 0\n\t"
    "v_mul_f32 v43, 0x3fb8aa3b, v43\n\t"
    "v_cvt_pk_bf16_f32 v43, v43, v43\n\t"
    "v_and_b32 v38, 0xffff, v43\n\t"
    "v_and_b32 v39, 0xffff0000, v43\n\t"
    "v_mul_f32 v42, v42, v39\n\t"
    "v_sub_f32 v42, 0, v42\n\t"
    "v_dot2_f32_bf16 v54, v168, v38, v42\n\t"
    "v_dot2_f32_bf16 v55, v168, v39, v42\n\t"
    "v_dot2_f32_bf16 v56, v169, v38, v42\n\t"
    "v_dot2_f32_bf16 v57, v169, v39, v42\n\t"
    "v_dot2_f32_bf16 v58, v170, v38, v42\n\t"
    "v_dot2_f32_bf16 v59, v170, v39, v42\n\t"
    "v_dot2_f32_bf16 v60, v171, v38, v42\n\t"
    "v_dot2_f32_bf16 v61, v171, v39, v42\n\t"
    "v_cvt_pk_bf16_f32 v46, v54, v55\n\t"
    "v_cvt_pk_bf16_f32 v47, v56, v57\n\t"
    "v_cvt_pk_bf16_f32 v48, v58, v59\n\t"
    "v_cvt_pk_bf16_f32 v49, v60, v61\n\t"
    "ds_write_b128 v7, v[46:49] offset:9344\n\t"
    "v_dot2_f32_bf16 v54, v172, v38, v42\n\t"
    "v_dot2_f32_bf16 v55, v172, v39, v42\n\t"
    "v_dot2_f32_bf16 v56, v173, v38, v42\n\t"
    "v_dot2_f32_bf16 v57, v173, v39, v42\n\t"
    "v_dot2_f32_bf16 v58, v174, v38, v42\n\t"
    "v_dot2_f32_bf16 v59, v174, v39, v42\n\t"
    "v_dot2_f32_bf16 v60, v175, v38, v42\n\t"
    "v_dot2_f32_bf16 v61, v175, v39, v42\n\t"
    "v_cvt_pk_bf16_f32 v50, v54, v55\n\t"
    "v_cvt_pk_bf16_f32 v51, v56, v57\n\t"
    "v_cvt_pk_bf16_f32 v52, v58, v59\n\t"
    "v_cvt_pk_bf16_f32 v53, v60, v61\n\t"
    "ds_write_b128 v7, v[50:53] offset:9472\n\t"
    "v_dot2_f32_bf16 v54, v176, v38, v42\n\t"
    "v_dot2_f32_bf16 v55, v176, v39, v42\n\t"
    "v_dot2_f32_bf16 v56, v177, v38, v42\n\t"
    "v_dot2_f32_bf16 v57, v177, v39, v42\n\t"
    "v_dot2_f32_bf16 v58, v178, v38, v42\n\t"
    "v_dot2_f32_bf16 v59, v178, v39, v42\n\t"
    "v_dot2_f32_bf16 v60, v179, v38, v42\n\t"
    "v_dot2_f32_bf16 v61, v179, v39, v42\n\t"
    "v_cvt_pk_bf16_f32 v46, v54, v55\n\t"
    "v_cvt_pk_bf16_f32 v47, v56, v57\n\t"
    "v_cvt_pk_bf16_f32 v48, v58, v59\n\t"
    "v_cvt_pk_bf16_f32 v49, v60, v61\n\t"
    "ds_write_b128 v7, v[46:49] offset:9600\n\t"
    "v_dot2_f32_bf16 v54, v180, v38, v42\n\t"
    "v_dot2_f32_bf16 v55, v180, v39, v42\n\t"
    "v_dot2_f32_bf16 v56, v181, v38, v42\n\t"
    "v_dot2_f32_bf16 v57, v181, v39, v42\n\t"
    "v_dot2_f32_bf16 v58, v182, v38, v42\n\t"
    "v_dot2_f32_bf16 v59, v182, v39, v42\n\t"
    "v_dot2_f32_bf16 v60, v183, v38, v42\n\t"
    "v_dot2_f32_bf16 v61, v183, v39, v42\n\t"
    "v_cvt_pk_bf16_f32 v50, v54, v55\n\t"
    "v_cvt_pk_bf16_f32 v51, v56, v57\n\t"
    "v_cvt_pk_bf16_f32 v52, v58, v59\n\t"
    "v_cvt_pk_bf16_f32 v53, v60, v61\n\t"
    "ds_write_b128 v7, v[50:53] offset:9728\n\t"
    "v_dot2_f32_bf16 v54, v184, v38, v42\n\t"
    "v_dot2_f32_bf16 v55, v184, v39, v42\n\t"
    "v_dot2_f32_bf16 v56, v185, v38, v42\n\t"
    "v_dot2_f32_bf16 v57, v185, v39, v42\n\t"
    "v_dot2_f32_bf16 v58, v186, v38, v42\n\t"
    "v_dot2_f32_bf16 v59, v186, v39, v42\n\t"
    "v_dot2_f32_bf16 v60, v187, v38, v42\n\t"
    "v_dot2_f32_bf16 v61, v187, v39, v42\n\t"
    "v_cvt_pk_bf16_f32 v46, v54, v55\n\t"
    "v_cvt_pk_bf16_f32 v47, v56, v57\n\t"
    "v_cvt_pk_bf16_f32 v48, v58, v59\n\t"
    "v_cvt_pk_bf16_f32 v49, v60, v61\n\t"
    "ds_write_b128 v7, v[46:49] offset:9856\n\t"
    "v_dot2_f32_bf16 v54, v188, v38, v42\n\t"
    "v_dot2_f32_bf16 v55, v188, v39, v42\n\t"
    "v_dot2_f32_bf16 v56, v189, v38, v42\n\t"
    "v_dot2_f32_bf16 v57, v189, v39, v42\n\t"
    "v_dot2_f32_bf16 v58, v190, v38, v42\n\t"
    "v_dot2_f32_bf16 v59, v190, v39, v42\n\t"
    "v_dot2_f32_bf16 v60, v191, v38, v42\n\t"
    "v_dot2_f32_bf16 v61, v191, v39, v42\n\t"
    "v_cvt_pk_bf16_f32 v50, v54, v55\n\t"
    "v_cvt_pk_bf16_f32 v51, v56, v57\n\t"
    "v_cvt_pk_bf16_f32 v52, v58, v59\n\t"
    "v_cvt_pk_bf16_f32 v53, v60, v61\n\t"
    "ds_write_b128 v7, v[50:53] offset:9984\n\t"
    "v_dot2_f32_bf16 v54, v192, v38, v42\n\t"
    "v_dot2_f32_bf16 v55, v192, v39, v42\n\t"
    "v_dot2_f32_bf16 v56, v193, v38, v42\n\t"
    "v_dot2_f32_bf16 v57, v193, v39, v42\n\t"
    "v_dot2_f32_bf16 v58, v194, v38, v42\n\t"
    "v_dot2_f32_bf16 v59, v194, v39, v42\n\t"
    "v_dot2_f32_bf16 v60, v195, v38, v42\n\t"
    "v_dot2_f32_bf16 v61, v195, v39, v42\n\t"
    "v_cvt_pk_bf16_f32 v46, v54, v55\n\t"
    "v_cvt_pk_bf16_f32 v47, v56, v57\n\t"
    "v_cvt_pk_bf16_f32 v48, v58, v59\n\t"
    "v_cvt_pk_bf16_f32 v49, v60, v61\n\t"
    "ds_write_b128 v7, v[46:49] offset:10112\n\t"
    "v_dot2_f32_bf16 v54, v196, v38, v42\n\t"
    "v_dot2_f32_bf16 v55, v196, v39, v42\n\t"
    "v_dot2_f32_bf16 v56, v197, v38, v42\n\t"
    "v_dot2_f32_bf16 v57, v197, v39, v42\n\t"
    "v_dot2_f32_bf16 v58, v198, v38, v42\n\t"
    "v_dot2_f32_bf16 v59, v198, v39, v42\n\t"
    "v_dot2_f32_bf16 v60, v199, v38, v42\n\t"
    "v_dot2_f32_bf16 v61, v199, v39, v42\n\t"
    "v_cvt_pk_bf16_f32 v50, v54, v55\n\t"
    "v_cvt_pk_bf16_f32 v51, v56, v57\n\t"
    "v_cvt_pk_bf16_f32 v52, v58, v59\n\t"
    "v_cvt_pk_bf16_f32 v53, v60, v61\n\t"
    "ds_write_b128 v7, v[50:53] offset:10240\n\t"
    "v_dot2_f32_bf16 v54, v200, v38, v42\n\t"
    "v_dot2_f32_bf16 v55, v200, v39, v42\n\t"
    "v_dot2_f32_bf16 v56, v201, v38, v42\n\t"
    "v_dot2_f32_bf16 v57, v201, v39, v42\n\t"
    "v_dot2_f32_bf16 v58, v202, v38, v42\n\t"
    "v_dot2_f32_bf16 v59, v202, v39, v42\n\t"
    "v_dot2_f32_bf16 v60, v203, v38, v42\n\t"
    "v_dot2_f32_bf16 v61, v203, v39, v42\n\t"
    "v_cvt_pk_bf16_f32 v46, v54, v55\n\t"
    "v_cvt_pk_bf16_f32 v47, v56, v57\n\t"
    "v_cvt_pk_bf16_f32 v48, v58, v59\n\t"
    "v_cvt_pk_bf16_f32 v49, v60, v61\n\t"
    "ds_write_b128 v7, v[46:49] offset:10368\n\t"
    "s_mov_b32 s72, 0x05040100\n\t"
    "s_mov_b32 s73, 0x07060302\n\t"
    "s_lshl_b32 s70, s64, 6\n\t"
    "v_add_u32 v13, s70, v1\n\t"
    "v_lshlrev_b32 v14, 5, v13\n\t"
    "v_add_u32 v14, 151552, v14\n\t"
    "s_waitcnt vmcnt(2)\n\t"
    "v_perm_b32 v38, v108, v104, s72\n\t"
    "v_perm_b32 v39, v108, v104, s73\n\t"
    "v_perm_b32 v40, v109, v105, s72\n\t"
    "v_perm_b32 v41, v109, v105, s73\n\t"
    "v_perm_b32 v42, v110, v106, s72\n\t"
    "v_perm_b32 v43, v110, v106, s73\n\t"
    "v_perm_b32 v44, v111, v107, s72\n\t"
    "v_perm_b32 v45, v111, v107, s73\n\t"
    "ds_write_b128 v14, v[38:41]\n\t"
    "ds_write_b128 v14, v[42:45] offset:16\n\t"
    "s_lshl_b32 s70, s64, 6\n\t"
    "v_add_u32 v13, s70, v1\n\t"
    "v_add_u32 v13, 256, v13\n\t"
    "v_lshlrev_b32 v14, 5, v13\n\t"
    "v_add_u32 v14, 151552, v14\n\t"
    "v_cmp_gt_u32 vcc, 320, v13\n\t"
    "v_mov_b32 v17, 145728\n\t"
    "s_nop 0\n\t"
    "v_cndmask_b32 v14, v17, v14, vcc\n\t"
    "s_waitcnt vmcnt(0)\n\t"
    "v_perm_b32 v38, v116, v112, s72\n\t"
    "v_perm_b32 v39, v116, v112, s73\n\t"
    "v_perm_b32 v40, v117, v113, s72\n\t"
    "v_perm_b32 v41, v117, v113, s73\n\t"
    "v_perm_b32 v42, v118, v114, s72\n\t"
    "v_perm_b32 v43, v118, v114, s73\n\t"
    "v_perm_b32 v44, v119, v115, s72\n\t"
    "v_perm_b32 v45, v119, v115, s73\n\t"
    "ds_write_b128 v14, v[38:41]\n\t"
    "ds_write_b128 v14, v[42:45] offset:16\n\t"
    "v_lshlrev_b32 v14, 2, v6\n\t"
    "v_add_u32 v14, 123904, v14\n\t"
    "v_lshlrev_b32 v34, 16, v34\n\t"
    "v_mul_f32 v34, 0x3fb8aa3b, v34\n\t"
    "v_lshlrev_b32 v35, 16, v35\n\t"
    "v_mul_f32 v35, 0x3fb8aa3b, v35\n\t"
    "v_lshlrev_b32 v36, 16, v36\n\t"
    "v_mul_f32 v36, 0x3fb8aa3b, v36\n\t"
    "ds_write_b32 v14, v34\n\t"
    "ds_write_b32 v14, v35 offset:1024\n\t"
    "v_cmp_gt_u32 vcc, 64, v6\n\t"
    "v_mov_b32 v15, 145728\n\t"
    "v_add_u32 v16, 2048, v14\n\t"
    "v_cndmask_b32 v16, v15, v16, vcc\n\t"
    "ds_write_b32 v16, v36\n\t"
    "v_readfirstlane_b32 s67, v11\n\t"
    "s_add_u32 s67, s67, 1\n\t"
    "s_waitcnt lgkmcnt(0)\n\t"
    "s_barrier\n\t"
    "v_lshrrev_b32 v13, 2, v1\n\t"
    "v_mul_u32_u24 v2, 9216, v13\n\t"
    "v_and_b32 v14, 3, v1\n\t"
    "v_lshl_add_u32 v2, v14, 4, v2\n\t"
    "v_add_u32 v3, 146432, v2\n\t"
    "v_add_u32 v4, 292864, v2\n\t"
    "s_mul_i32 s69, s64, 12288\n\t"
    "s_add_u32 s69, s69, 24576\n\t"
    "v_lshrrev_b32 v15, 5, v1\n\t"
    "v_bfe_u32 v16, v1, 2, 2\n\t"
    "v_lshl_add_u32 v15, v15, 3, v16\n\t"
    "v_lshlrev_b32 v5, 6, v15\n\t"
    "v_bfe_u32 v16, v1, 4, 1\n\t"
    "v_lshl_add_u32 v5, v16, 5, v5\n\t"
    "v_lshl_add_u32 v5, v14, 3, v5\n\t"
    "v_add_u32 v5, s69, v5\n\t"
    "s_and_b32 s70, s65, 3\n\t"
    "s_lshl_b32 s70, s70, 2\n\t"
    "s_add_u32 s70, s70, s64\n\t"
    "s_mul_i32 s71, s66, 1152\n\t"
    "s_add_u32 s72, s70, 0\n\t"
    "s_and_b32 s72, s72, 15\n\t"
    "s_lshl_b32 s79, s72, 6\n\t"
    "s_add_u32 s68, s71, s79\n\t"
    "s_add_u32 s72, s70, 4\n\t"
    "s_and_b32 s72, s72, 15\n\t"
    "s_lshl_b32 s80, s72, 6\n\t"
    "s_add_u32 s84, s71, s80\n\t"
    "s_add_u32 s72, s70, 8\n\t"
    "s_and_b32 s72, s72, 15\n\t"
    "s_lshl_b32 s81, s72, 6\n\t"
    "s_add_u32 s85, s71, s81\n\t"
    "s_add_u32 s72, s70, 12\n\t"
    "s_and_b32 s72, s72, 15\n\t"
    "s_lshl_b32 s82, s72, 6\n\t"
    "s_add_u32 s86, s71, s82\n\t"
    "s_lshr_b32 s72, s64, 1\n\t"
    "s_add_u32 s72, s72, 16\n\t"
    "s_lshl_b32 s83, s72, 6\n\t"
    "s_and_b32 s88, s64, 1\n\t"
    "s_mul_i32 s72, s88, 2654208\n\t"
    "s_add_u32 s72, s72, s71\n\t"
    "s_add_u32 s87, s72, s83\n\t"
    "s_mov_b32 s78, 0x3f3504f3\n\t"
    "v_lshrrev_b32 v15, 5, v1\n\t"
    "v_and_b32 v16, 31, v1\n\t"
    "v_lshlrev_b32 v6, 4, v15\n\t"
    "v_add_u32 v6, 123904, v6\n\t"
    "v_mul_u32_u24 v7, 1168, v16\n\t"
    "v_lshl_add_u32 v74, v15, 4, v7\n\t"
    "v_lshl_add_u32 v7, v15, 3, v7\n\t"
    "s_lshl_b32 s70, s64, 5\n\t"
    "v_and_b32 v13, 31, v1\n\t"
    "v_add_u32 v13, s70, v13\n\t"
    "v_min_u32 v13, 95, v13\n\t"
    "v_cmp_lt_u32 vcc, 47, v13\n\t"
    "s_nop 1\n\t"
    "v_cndmask_b32 v14, 0, 1, vcc\n\t"
    "v_mul_u32_u24 v15, 47, v14\n\t"
    "v_sub_u32 v15, v13, v15\n\t"
    "v_min_u32 v15, 39, v15\n\t"
    "v_mul_u32_u24 v14, 5120, v14\n\t"
    "v_lshl_add_u32 v14, v15, 2, v14\n\t"
    "v_lshrrev_b32 v16, 5, v1\n\t"
    "v_mul_u32_u24 v16, 640, v16\n\t"
    "v_add_u32 v14, v14, v16\n\t"
    "v_add_u32 v14, 151552, v14\n\t"
    "ds_read_b32 v50, v14 offset:0\n\t"
    "ds_read_b32 v51, v14 offset:160\n\t"
    "ds_read_b32 v52, v14 offset:320\n\t"
    "ds_read_b32 v53, v14 offset:480\n\t"
    "ds_read_b32 v54, v14 offset:1280\n\t"
    "ds_read_b32 v55, v14 offset:1440\n\t"
    "ds_read_b32 v56, v14 offset:1600\n\t"
    "ds_read_b32 v57, v14 offset:1760\n\t"
    "ds_read_b32 v58, v14 offset:2560\n\t"
    "ds_read_b32 v59, v14 offset:2720\n\t"
    "ds_read_b32 v60, v14 offset:2880\n\t"
    "ds_read_b32 v61, v14 offset:3040\n\t"
    "ds_read_b32 v62, v14 offset:3840\n\t"
    "ds_read_b32 v63, v14 offset:4000\n\t"
    "ds_read_b32 v64, v14 offset:4160\n\t"
    "ds_read_b32 v65, v14 offset:4320\n\t"
    "s_add_u32 m0, s69, 50176\n\t"
    "s_nop 0\n\t"
    "buffer_load_dwordx4 v2, s[44:47], s68 offen offset:0 lds\n\t"
    "buffer_load_dwordx4 v3, s[44:47], s68 offen offset:1024 lds\n\t"
    "buffer_load_dwordx4 v4, s[44:47], s68 offen offset:2048 lds\n\t"
    "s_add_u32 s68, s68, 442368\n\t"
    "s_add_u32 m0, s69, 53248\n\t"
    "s_nop 0\n\t"
    "buffer_load_dwordx4 v2, s[44:47], s68 offen offset:0 lds\n\t"
    "s_cmp_eq_u32 s64, 3\n\t"
    "s_cbranch_scc1 L_w3_2%=\n\t"
    "ds_read_b128 a[0:3], v74 offset:0\n\t"
    "ds_read_b128 a[4:7], v74 offset:37376\n\t"
    "ds_read_b128 a[8:11], v74 offset:32\n\t"
    "ds_read_b128 a[12:15], v74 offset:37408\n\t"
    "ds_read_b128 a[16:19], v74 offset:64\n\t"
    "ds_read_b128 a[20:23], v74 offset:37440\n\t"
    "ds_read_b128 a[24:27], v74 offset:96\n\t"
    "ds_read_b128 a[28:31], v74 offset:37472\n\t"
    "v_cvt_pk_bf16_f32 v94, v76, v77\n\t"
    "v_cvt_pk_bf16_f32 v95, v78, v79\n\t"
    "v_cvt_pk_bf16_f32 v96, v80, v81\n\t"
    "v_cvt_pk_bf16_f32 v97, v82, v83\n\t"
    "ds_read_b128 a[32:35], v74 offset:128\n\t"
    "ds_read_b128 a[36:39], v74 offset:37504\n\t"
    "s_waitcnt lgkmcnt(9)\n\t"
    "v_mfma_f32_32x32x16_bf16 v[192:207], v[94:97], a[0:3], 0\n\t"
    "v_fma_f32 v84, v92, v76, -v84\n\t"
    "v_fma_f32 v85, v92, v77, -v85\n\t"
    "v_fma_f32 v86, v92, v78, -v86\n\t"
    "v_fma_f32 v87, v92, v79, -v87\n\t"
    "v_fma_f32 v88, v92, v80, -v88\n\t"
    "v_fma_f32 v89, v92, v81, -v89\n\t"
    "v_fma_f32 v90, v92, v82, -v90\n\t"
    "v_fma_f32 v91, v92, v83, -v91\n\t"
    "v_cvt_pk_bf16_f32 v98, v84, v85\n\t"
    "v_cvt_pk_bf16_f32 v99, v86, v87\n\t"
    "v_cvt_pk_bf16_f32 v100, v88, v89\n\t"
    "s_waitcnt lgkmcnt(8)\n\t"
    "v_mfma_f32_32x32x16_bf16 v[208:223], v[94:97], a[4:7], 0\n\t"
    "v_cvt_pk_bf16_f32 v101, v90, v91\n\t"
    "ds_read_b128 a[40:43], v74 offset:160\n\t"
    "ds_read_b128 a[44:47], v74 offset:37536\n\t"
    "s_waitcnt lgkmcnt(9)\n\t"
    "v_mfma_f32_32x32x16_bf16 v[192:207], v[98:101], a[8:11], v[192:207]\n\t"
    "v_fma_f32 v76, v92, v84, -v76\n\t"
    "v_fma_f32 v77, v92, v85, -v77\n\t"
    "v_fma_f32 v78, v92, v86, -v78\n\t"
    "v_fma_f32 v79, v92, v87, -v79\n\t"
    "v_fma_f32 v80, v92, v88, -v80\n\t"
    "v_fma_f32 v81, v92, v89, -v81\n\t"
    "v_fma_f32 v82, v92, v90, -v82\n\t"
    "v_fma_f32 v83, v92, v91, -v83\n\t"
    "v_cvt_pk_bf16_f32 v94, v76, v77\n\t"
    "v_cvt_pk_bf16_f32 v95, v78, v79\n\t"
    "v_cvt_pk_bf16_f32 v96, v80, v81\n\t"
    "s_waitcnt lgkmcnt(8)\n\t"
    "v_mfma_f32_32x32x16_bf16 v[208:223], v[98:101], a[12:15], v[208:223]\n\t"
    "v_cvt_pk_bf16_f32 v97, v82, v83\n\t"
    "buffer_load_dwordx4 v3, s[44:47], s68 offen offset:1024 lds\n\t"
    "ds_read_b128 a[48:51], v74 offset:192\n\t"
    "ds_read_b128 a[52:55], v74 offset:37568\n\t"
    "s_waitcnt lgkmcnt(9)\n\t"
    "v_mfma_f32_32x32x16_bf16 v[192:207], v[94:97], a[16:19], v[192:207]\n\t"
    "v_fma_f32 v84, v92, v76, -v84\n\t"
    "v_fma_f32 v85, v92, v77, -v85\n\t"
    "v_fma_f32 v86, v92, v78, -v86\n\t"
    "v_fma_f32 v87, v92, v79, -v87\n\t"
    "v_fma_f32 v88, v92, v80, -v88\n\t"
    "v_fma_f32 v89, v92, v81, -v89\n\t"
    "v_fma_f32 v90, v92, v82, -v90\n\t"
    "v_fma_f32 v91, v92, v83, -v91\n\t"
    "v_cvt_pk_bf16_f32 v98, v84, v85\n\t"
    "v_cvt_pk_bf16_f32 v99, v86, v87\n\t"
    "v_cvt_pk_bf16_f32 v100, v88, v89\n\t"
    "s_waitcnt lgkmcnt(8)\n\t"
    "v_mfma_f32_32x32x16_bf16 v[208:223], v[94:97], a[20:23], v[208:223]\n\t"
    "v_cvt_pk_bf16_f32 v101, v90, v91\n\t"
    "ds_read_b128 a[56:59], v74 offset:224\n\t"
    "ds_read_b128 a[60:63], v74 offset:37600\n\t"
    "s_waitcnt lgkmcnt(9)\n\t"
    "v_mfma_f32_32x32x16_bf16 v[192:207], v[98:101], a[24:27], v[192:207]\n\t"
    "v_fma_f32 v76, v92, v84, -v76\n\t"
    "v_fma_f32 v77, v92, v85, -v77\n\t"
    "v_fma_f32 v78, v92, v86, -v78\n\t"
    "v_fma_f32 v79, v92, v87, -v79\n\t"
    "v_fma_f32 v80, v92, v88, -v80\n\t"
    "v_fma_f32 v81, v92, v89, -v81\n\t"
    "v_fma_f32 v82, v92, v90, -v82\n\t"
    "v_fma_f32 v83, v92, v91, -v83\n\t"
    "v_cvt_pk_bf16_f32 v94, v76, v77\n\t"
    "v_cvt_pk_bf16_f32 v95, v78, v79\n\t"
    "v_cvt_pk_bf16_f32 v96, v80, v81\n\t"
    "s_waitcnt lgkmcnt(8)\n\t"
    "v_mfma_f32_32x32x16_bf16 v[208:223], v[98:101], a[28:31], v[208:223]\n\t"
    "v_cvt_pk_bf16_f32 v97, v82, v83\n\t"
    "ds_read_b128 a[64:67], v74 offset:256\n\t"
    "ds_read_b128 a[68:71], v74 offset:37632\n\t"
    "s_waitcnt lgkmcnt(9)\n\t"
    "v_mfma_f32_32x32x16_bf16 v[192:207], v[94:97], a[32:35], v[192:207]\n\t"
    "v_fma_f32 v84, v92, v76, -v84\n\t"
    "v_fma_f32 v85, v92, v77, -v85\n\t"
    "v_fma_f32 v86, v92, v78, -v86\n\t"
    "v_fma_f32 v87, v92, v79, -v87\n\t"
    "v_fma_f32 v88, v92, v80, -v88\n\t"
    "v_fma_f32 v89, v92, v81, -v89\n\t"
    "v_fma_f32 v90, v92, v82, -v90\n\t"
    "v_fma_f32 v91, v92, v83, -v91\n\t"
    "v_cvt_pk_bf16_f32 v98, v84, v85\n\t"
    "v_cvt_pk_bf16_f32 v99, v86, v87\n\t"
    "v_cvt_pk_bf16_f32 v100, v88, v89\n\t"
    "s_waitcnt lgkmcnt(8)\n\t"
    "v_mfma_f32_32x32x16_bf16 v[208:223], v[94:97], a[36:39], v[208:223]\n\t"
    "v_cvt_pk_bf16_f32 v101, v90, v91\n\t"
    "ds_read_b128 a[72:75], v74 offset:288\n\t"
    "ds_read_b128 a[76:79], v74 offset:37664\n\t"
    "s_waitcnt lgkmcnt(9)\n\t"
    "v_mfma_f32_32x32x16_bf16 v[192:207], v[98:101], a[40:43], v[192:207]\n\t"
    "v_fma_f32 v76, v92, v84, -v76\n\t"
    "v_fma_f32 v77, v92, v85, -v77\n\t"
    "v_fma_f32 v78, v92, v86, -v78\n\t"
    "v_fma_f32 v79, v92, v87, -v79\n\t"
    "v_fma_f32 v80, v92, v88, -v80\n\t"
    "v_fma_f32 v81, v92, v89, -v81\n\t"
    "v_fma_f32 v82, v92, v90, -v82\n\t"
    "v_fma_f32 v83, v92, v91, -v83\n\t"
    "v_cvt_pk_bf16_f32 v94, v76, v77\n\t"
    "v_cvt_pk_bf16_f32 v95, v78, v79\n\t"
    "v_cvt_pk_bf16_f32 v96, v80, v81\n\t"
    "s_waitcnt lgkmcnt(8)\n\t"
    "v_mfma_f32_32x32x16_bf16 v[208:223], v[98:101], a[44:47], v[208:223]\n\t"
    "v_cvt_pk_bf16_f32 v97, v82, v83\n\t"
    "buffer_load_dwordx4 v4, s[44:47], s68 offen offset:2048 lds\n\t"
    "s_add_u32 s68, s68, 442368\n\t"
    "ds_read_b128 a[80:83], v74 offset:320\n\t"
    "ds_read_b128 a[84:87], v74 offset:37696\n\t"
    "s_waitcnt lgkmcnt(9)\n\t"
    "v_mfma_f32_32x32x16_bf16 v[192:207], v[94:97], a[48:51], v[192:207]\n\t"
    "v_fma_f32 v84, v92, v76, -v84\n\t"
    "v_fma_f32 v85, v92, v77, -v85\n\t"
    "v_fma_f32 v86, v92, v78, -v86\n\t"
    "v_fma_f32 v87, v92, v79, -v87\n\t"
    "v_fma_f32 v88, v92, v80, -v88\n\t"
    "v_fma_f32 v89, v92, v81, -v89\n\t"
    "v_fma_f32 v90, v92, v82, -v90\n\t"
    "v_fma_f32 v91, v92, v83, -v91\n\t"
    "v_cvt_pk_bf16_f32 v98, v84, v85\n\t"
    "v_cvt_pk_bf16_f32 v99, v86, v87\n\t"
    "v_cvt_pk_bf16_f32 v100, v88, v89\n\t"
    "s_waitcnt lgkmcnt(8)\n\t"
    "v_mfma_f32_32x32x16_bf16 v[208:223], v[94:97], a[52:55], v[208:223]\n\t"
    "v_cvt_pk_bf16_f32 v101, v90, v91\n\t"
    "ds_read_b128 a[88:91], v74 offset:352\n\t"
    "ds_read_b128 a[92:95], v74 offset:37728\n\t"
    "s_waitcnt lgkmcnt(9)\n\t"
    "v_mfma_f32_32x32x16_bf16 v[192:207], v[98:101], a[56:59], v[192:207]\n\t"
    "v_fma_f32 v76, v92, v84, -v76\n\t"
    "v_fma_f32 v77, v92, v85, -v77\n\t"
    "v_fma_f32 v78, v92, v86, -v78\n\t"
    "v_fma_f32 v79, v92, v87, -v79\n\t"
    "v_fma_f32 v80, v92, v88, -v80\n\t"
    "v_fma_f32 v81, v92, v89, -v81\n\t"
    "v_fma_f32 v82, v92, v90, -v82\n\t"
    "v_fma_f32 v83, v92, v91, -v83\n\t"
    "v_cvt_pk_bf16_f32 v94, v76, v77\n\t"
    "v_cvt_pk_bf16_f32 v95, v78, v79\n\t"
    "v_cvt_pk_bf16_f32 v96, v80, v81\n\t"
    "s_waitcnt lgkmcnt(8)\n\t"
    "v_mfma_f32_32x32x16_bf16 v[208:223], v[98:101], a[60:63], v[208:223]\n\t"
    "v_cvt_pk_bf16_f32 v97, v82, v83\n\t"
    "ds_read_b128 a[96:99], v74 offset:384\n\t"
    "ds_read_b128 a[100:103], v74 offset:37760\n\t"
    "s_waitcnt lgkmcnt(9)\n\t"
    "v_mfma_f32_32x32x16_bf16 v[192:207], v[94:97], a[64:67], v[192:207]\n\t"
    "v_fma_f32 v84, v92, v76, -v84\n\t"
    "v_fma_f32 v85, v92, v77, -v85\n\t"
    "v_fma_f32 v86, v92, v78, -v86\n\t"
    "v_fma_f32 v87, v92, v79, -v87\n\t"
    "v_fma_f32 v88, v92, v80, -v88\n\t"
    "v_fma_f32 v89, v92, v81, -v89\n\t"
    "v_fma_f32 v90, v92, v82, -v90\n\t"
    "v_fma_f32 v91, v92, v83, -v91\n\t"
    "v_cvt_pk_bf16_f32 v98, v84, v85\n\t"
    "v_cvt_pk_bf16_f32 v99, v86, v87\n\t"
    "v_cvt_pk_bf16_f32 v100, v88, v89\n\t"
    "s_waitcnt lgkmcnt(8)\n\t"
    "v_mfma_f32_32x32x16_bf16 v[208:223], v[94:97], a[68:71], v[208:223]\n\t"
    "v_cvt_pk_bf16_f32 v101, v90, v91\n\t"
    "ds_read_b128 a[104:107], v74 offset:416\n\t"
    "ds_read_b128 a[108:111], v74 offset:37792\n\t"
    "s_waitcnt lgkmcnt(9)\n\t"
    "v_mfma_f32_32x32x16_bf16 v[192:207], v[98:101], a[72:75], v[192:207]\n\t"
    "v_fma_f32 v76, v92, v84, -v76\n\t"
    "v_fma_f32 v77, v92, v85, -v77\n\t"
    "v_fma_f32 v78, v92, v86, -v78\n\t"
    "v_fma_f32 v79, v92, v87, -v79\n\t"
    "v_fma_f32 v80, v92, v88, -v80\n\t"
    "v_fma_f32 v81, v92, v89, -v81\n\t"
    "v_fma_f32 v82, v92, v90, -v82\n\t"
    "v_fma_f32 v83, v92, v91, -v83\n\t"
    "v_cvt_pk_bf16_f32 v94, v76, v77\n\t"
    "v_cvt_pk_bf16_f32 v95, v78, v79\n\t"
    "v_cvt_pk_bf16_f32 v96, v80, v81\n\t"
    "s_waitcnt lgkmcnt(8)\n\t"
    "v_mfma_f32_32x32x16_bf16 v[208:223], v[98:101], a[76:79], v[208:223]\n\t"
    "v_cvt_pk_bf16_f32 v97, v82, v83\n\t"
    "s_add_u32 m0, s69, 56320\n\t"
    "s_nop 0\n\t"
    "buffer_load_dwordx4 v2, s[44:47], s68 offen offset:0 lds\n\t"
    "ds_read_b128 a[112:115], v74 offset:448\n\t"
    "ds_read_b128 a[116:119], v74 offset:37824\n\t"
    "s_waitcnt lgkmcnt(9)\n\t"
    "v_mfma_f32_32x32x16_bf16 v[192:207], v[94:97], a[80:83], v[192:207]\n\t"
    "v_fma_f32 v84, v92, v76, -v84\n\t"
    "v_fma_f32 v85, v92, v77, -v85\n\t"
    "v_fma_f32 v86, v92, v78, -v86\n\t"
    "v_fma_f32 v87, v92, v79, -v87\n\t"
    "v_fma_f32 v88, v92, v80, -v88\n\t"
    "v_fma_f32 v89, v92, v81, -v89\n\t"
    "v_fma_f32 v90, v92, v82, -v90\n\t"
    "v_fma_f32 v91, v92, v83, -v91\n\t"
    "v_cvt_pk_bf16_f32 v98, v84, v85\n\t"
    "v_cvt_pk_bf16_f32 v99, v86, v87\n\t"
    "v_cvt_pk_bf16_f32 v100, v88, v89\n\t"
    "s_waitcnt lgkmcnt(8)\n\t"
    "v_mfma_f32_32x32x16_bf16 v[208:223], v[94:97], a[84:87], v[208:223]\n\t"
    "v_cvt_pk_bf16_f32 v101, v90, v91\n\t"
    "ds_read_b128 a[120:123], v74 offset:480\n\t"
    "ds_read_b128 a[124:127], v74 offset:37856\n\t"
    "s_waitcnt lgkmcnt(9)\n\t"
    "v_mfma_f32_32x32x16_bf16 v[192:207], v[98:101], a[88:91], v[192:207]\n\t"
    "v_fma_f32 v76, v92, v84, -v76\n\t"
    "v_fma_f32 v77, v92, v85, -v77\n\t"
    "v_fma_f32 v78, v92, v86, -v78\n\t"
    "v_fma_f32 v79, v92, v87, -v79\n\t"
    "v_fma_f32 v80, v92, v88, -v80\n\t"
    "v_fma_f32 v81, v92, v89, -v81\n\t"
    "v_fma_f32 v82, v92, v90, -v82\n\t"
    "v_fma_f32 v83, v92, v91, -v83\n\t"
    "v_cvt_pk_bf16_f32 v94, v76, v77\n\t"
    "v_cvt_pk_bf16_f32 v95, v78, v79\n\t"
    "v_cvt_pk_bf16_f32 v96, v80, v81\n\t"
    "s_waitcnt lgkmcnt(8)\n\t"
    "v_mfma_f32_32x32x16_bf16 v[208:223], v[98:101], a[92:95], v[208:223]\n\t"
    "v_cvt_pk_bf16_f32 v97, v82, v83\n\t"
    "ds_read_b128 a[128:131], v74 offset:512\n\t"
    "ds_read_b128 a[132:135], v74 offset:37888\n\t"
    "s_waitcnt lgkmcnt(9)\n\t"
    "v_mfma_f32_32x32x16_bf16 v[192:207], v[94:97], a[96:99], v[192:207]\n\t"
    "v_fma_f32 v84, v92, v76, -v84\n\t"
    "v_fma_f32 v85, v92, v77, -v85\n\t"
    "v_fma_f32 v86, v92, v78, -v86\n\t"
    "v_fma_f32 v87, v92, v79, -v87\n\t"
    "v_fma_f32 v88, v92, v80, -v88\n\t"
    "v_fma_f32 v89, v92, v81, -v89\n\t"
    "v_fma_f32 v90, v92, v82, -v90\n\t"
    "v_fma_f32 v91, v92, v83, -v91\n\t"
    "v_cvt_pk_bf16_f32 v98, v84, v85\n\t"
    "v_cvt_pk_bf16_f32 v99, v86, v87\n\t"
    "v_cvt_pk_bf16_f32 v100, v88, v89\n\t"
    "s_waitcnt lgkmcnt(8)\n\t"
    "v_mfma_f32_32x32x16_bf16 v[208:223], v[94:97], a[100:103], v[208:223]\n\t"
    "v_cvt_pk_bf16_f32 v101, v90, v91\n\t"
    "ds_read_b128 a[136:139], v74 offset:544\n\t"
    "ds_read_b128 a[140:143], v74 offset:37920\n\t"
    "s_waitcnt lgkmcnt(9)\n\t"
    "v_mfma_f32_32x32x16_bf16 v[192:207], v[98:101], a[104:107], v[192:207]\n\t"
    "v_fma_f32 v76, v92, v84, -v76\n\t"
    "v_fma_f32 v77, v92, v85, -v77\n\t"
    "v_fma_f32 v78, v92, v86, -v78\n\t"
    "v_fma_f32 v79, v92, v87, -v79\n\t"
    "v_fma_f32 v80, v92, v88, -v80\n\t"
    "v_fma_f32 v81, v92, v89, -v81\n\t"
    "v_fma_f32 v82, v92, v90, -v82\n\t"
    "v_fma_f32 v83, v92, v91, -v83\n\t"
    "v_cvt_pk_bf16_f32 v94, v76, v77\n\t"
    "v_cvt_pk_bf16_f32 v95, v78, v79\n\t"
    "v_cvt_pk_bf16_f32 v96, v80, v81\n\t"
    "s_waitcnt lgkmcnt(8)\n\t"
    "v_mfma_f32_32x32x16_bf16 v[208:223], v[98:101], a[108:111], v[208:223]\n\t"
    "v_cvt_pk_bf16_f32 v97, v82, v83\n\t"
    "buffer_load_dwordx4 v3, s[44:47], s68 offen offset:1024 lds\n\t"
    "ds_read_b128 a[144:147], v74 offset:576\n\t"
    "ds_read_b128 a[148:151], v74 offset:37952\n\t"
    "s_waitcnt lgkmcnt(9)\n\t"
    "v_mfma_f32_32x32x16_bf16 v[192:207], v[94:97], a[112:115], v[192:207]\n\t"
    "v_fma_f32 v84, v92, v76, -v84\n\t"
    "v_fma_f32 v85, v92, v77, -v85\n\t"
    "v_fma_f32 v86, v92, v78, -v86\n\t"
    "v_fma_f32 v87, v92, v79, -v87\n\t"
    "v_fma_f32 v88, v92, v80, -v88\n\t"
    "v_fma_f32 v89, v92, v81, -v89\n\t"
    "v_fma_f32 v90, v92, v82, -v90\n\t"
    "v_fma_f32 v91, v92, v83, -v91\n\t"
    "v_cvt_pk_bf16_f32 v98, v84, v85\n\t"
    "v_cvt_pk_bf16_f32 v99, v86, v87\n\t"
    "v_cvt_pk_bf16_f32 v100, v88, v89\n\t"
    "s_waitcnt lgkmcnt(8)\n\t"
    "v_mfma_f32_32x32x16_bf16 v[208:223], v[94:97], a[116:119], v[208:223]\n\t"
    "v_cvt_pk_bf16_f32 v101, v90, v91\n\t"
    "ds_read_b128 a[152:155], v74 offset:608\n\t"
    "ds_read_b128 a[156:159], v74 offset:37984\n\t"
    "s_waitcnt lgkmcnt(9)\n\t"
    "v_mfma_f32_32x32x16_bf16 v[192:207], v[98:101], a[120:123], v[192:207]\n\t"
    "v_fma_f32 v76, v92, v84, -v76\n\t"
    "v_fma_f32 v77, v92, v85, -v77\n\t"
    "v_fma_f32 v78, v92, v86, -v78\n\t"
    "v_fma_f32 v79, v92, v87, -v79\n\t"
    "v_fma_f32 v80, v92, v88, -v80\n\t"
    "v_fma_f32 v81, v92, v89, -v81\n\t"
    "v_fma_f32 v82, v92, v90, -v82\n\t"
    "v_fma_f32 v83, v92, v91, -v83\n\t"
    "v_cvt_pk_bf16_f32 v94, v76, v77\n\t"
    "v_cvt_pk_bf16_f32 v95, v78, v79\n\t"
    "v_cvt_pk_bf16_f32 v96, v80, v81\n\t"
    "s_waitcnt lgkmcnt(8)\n\t"
    "v_mfma_f32_32x32x16_bf16 v[208:223], v[98:101], a[124:127], v[208:223]\n\t"
    "v_cvt_pk_bf16_f32 v97, v82, v83\n\t"
    "ds_read_b128 a[160:163], v74 offset:640\n\t"
    "ds_read_b128 a[164:167], v74 offset:38016\n\t"
    "s_waitcnt lgkmcnt(9)\n\t"
    "v_mfma_f32_32x32x16_bf16 v[192:207], v[94:97], a[128:131], v[192:207]\n\t"
    "v_fma_f32 v84, v92, v76, -v84\n\t"
    "v_fma_f32 v85, v92, v77, -v85\n\t"
    "v_fma_f32 v86, v92, v78, -v86\n\t"
    "v_fma_f32 v87, v92, v79, -v87\n\t"
    "v_fma_f32 v88, v92, v80, -v88\n\t"
    "v_fma_f32 v89, v92, v81, -v89\n\t"
    "v_fma_f32 v90, v92, v82, -v90\n\t"
    "v_fma_f32 v91, v92, v83, -v91\n\t"
    "v_cvt_pk_bf16_f32 v98, v84, v85\n\t"
    "v_cvt_pk_bf16_f32 v99, v86, v87\n\t"
    "v_cvt_pk_bf16_f32 v100, v88, v89\n\t"
    "s_waitcnt lgkmcnt(8)\n\t"
    "v_mfma_f32_32x32x16_bf16 v[208:223], v[94:97], a[132:135], v[208:223]\n\t"
    "v_cvt_pk_bf16_f32 v101, v90, v91\n\t"
    "ds_read_b128 a[168:171], v74 offset:672\n\t"
    "ds_read_b128 a[172:175], v74 offset:38048\n\t"
    "s_waitcnt lgkmcnt(9)\n\t"
    "v_mfma_f32_32x32x16_bf16 v[192:207], v[98:101], a[136:139], v[192:207]\n\t"
    "v_fma_f32 v76, v92, v84, -v76\n\t"
    "v_fma_f32 v77, v92, v85, -v77\n\t"
    "v_fma_f32 v78, v92, v86, -v78\n\t"
    "v_fma_f32 v79, v92, v87, -v79\n\t"
    "v_fma_f32 v80, v92, v88, -v80\n\t"
    "v_fma_f32 v81, v92, v89, -v81\n\t"
    "v_fma_f32 v82, v92, v90, -v82\n\t"
    "v_fma_f32 v83, v92, v91, -v83\n\t"
    "v_cvt_pk_bf16_f32 v94, v76, v77\n\t"
    "v_cvt_pk_bf16_f32 v95, v78, v79\n\t"
    "v_cvt_pk_bf16_f32 v96, v80, v81\n\t"
    "s_waitcnt lgkmcnt(8)\n\t"
    "v_mfma_f32_32x32x16_bf16 v[208:223], v[98:101], a[140:143], v[208:223]\n\t"
    "v_cvt_pk_bf16_f32 v97, v82, v83\n\t"
    "buffer_load_dwordx4 v4, s[44:47], s68 offen offset:2048 lds\n\t"
    "s_add_u32 s68, s68, 442368\n\t"
    "ds_read_b128 a[176:179], v74 offset:704\n\t"
    "ds_read_b128 a[180:183], v74 offset:38080\n\t"
    "s_waitcnt lgkmcnt(9)\n\t"
    "v_mfma_f32_32x32x16_bf16 v[192:207], v[94:97], a[144:147], v[192:207]\n\t"
    "v_fma_f32 v84, v92, v76, -v84\n\t"
    "v_fma_f32 v85, v92, v77, -v85\n\t"
    "v_fma_f32 v86, v92, v78, -v86\n\t"
    "v_fma_f32 v87, v92, v79, -v87\n\t"
    "v_fma_f32 v88, v92, v80, -v88\n\t"
    "v_fma_f32 v89, v92, v81, -v89\n\t"
    "v_fma_f32 v90, v92, v82, -v90\n\t"
    "v_fma_f32 v91, v92, v83, -v91\n\t"
    "v_cvt_pk_bf16_f32 v98, v84, v85\n\t"
    "v_cvt_pk_bf16_f32 v99, v86, v87\n\t"
    "v_cvt_pk_bf16_f32 v100, v88, v89\n\t"
    "s_waitcnt lgkmcnt(8)\n\t"
    "v_mfma_f32_32x32x16_bf16 v[208:223], v[94:97], a[148:151], v[208:223]\n\t"
    "v_cvt_pk_bf16_f32 v101, v90, v91\n\t"
    "ds_read_b128 a[184:187], v74 offset:736\n\t"
    "ds_read_b128 a[188:191], v74 offset:38112\n\t"
    "s_waitcnt lgkmcnt(9)\n\t"
    "v_mfma_f32_32x32x16_bf16 v[192:207], v[98:101], a[152:155], v[192:207]\n\t"
    "v_fma_f32 v76, v92, v84, -v76\n\t"
    "v_fma_f32 v77, v92, v85, -v77\n\t"
    "v_fma_f32 v78, v92, v86, -v78\n\t"
    "v_fma_f32 v79, v92, v87, -v79\n\t"
    "v_fma_f32 v80, v92, v88, -v80\n\t"
    "v_fma_f32 v81, v92, v89, -v81\n\t"
    "v_fma_f32 v82, v92, v90, -v82\n\t"
    "v_fma_f32 v83, v92, v91, -v83\n\t"
    "v_cvt_pk_bf16_f32 v94, v76, v77\n\t"
    "v_cvt_pk_bf16_f32 v95, v78, v79\n\t"
    "v_cvt_pk_bf16_f32 v96, v80, v81\n\t"
    "s_waitcnt lgkmcnt(8)\n\t"
    "v_mfma_f32_32x32x16_bf16 v[208:223], v[98:101], a[156:159], v[208:223]\n\t"
    "v_cvt_pk_bf16_f32 v97, v82, v83\n\t"
    "ds_read_b128 a[192:195], v74 offset:768\n\t"
    "ds_read_b128 a[196:199], v74 offset:38144\n\t"
    "s_waitcnt lgkmcnt(9)\n\t"
    "v_mfma_f32_32x32x16_bf16 v[192:207], v[94:97], a[160:163], v[192:207]\n\t"
    "v_fma_f32 v84, v92, v76, -v84\n\t"
    "v_fma_f32 v85, v92, v77, -v85\n\t"
    "v_fma_f32 v86, v92, v78, -v86\n\t"
    "v_fma_f32 v87, v92, v79, -v87\n\t"
    "v_fma_f32 v88, v92, v80, -v88\n\t"
    "v_fma_f32 v89, v92, v81, -v89\n\t"
    "v_fma_f32 v90, v92, v82, -v90\n\t"
    "v_fma_f32 v91, v92, v83, -v91\n\t"
    "v_cvt_pk_bf16_f32 v98, v84, v85\n\t"
    "v_cvt_pk_bf16_f32 v99, v86, v87\n\t"
    "v_cvt_pk_bf16_f32 v100, v88, v89\n\t"
    "s_waitcnt lgkmcnt(8)\n\t"
    "v_mfma_f32_32x32x16_bf16 v[208:223], v[94:97], a[164:167], v[208:223]\n\t"
    "v_cvt_pk_bf16_f32 v101, v90, v91\n\t"
    "ds_read_b128 a[200:203], v74 offset:800\n\t"
    "ds_read_b128 a[204:207], v74 offset:38176\n\t"
    "s_waitcnt lgkmcnt(9)\n\t"
    "v_mfma_f32_32x32x16_bf16 v[192:207], v[98:101], a[168:171], v[192:207]\n\t"
    "v_fma_f32 v76, v92, v84, -v76\n\t"
    "v_fma_f32 v77, v92, v85, -v77\n\t"
    "v_fma_f32 v78, v92, v86, -v78\n\t"
    "v_fma_f32 v79, v92, v87, -v79\n\t"
    "v_fma_f32 v80, v92, v88, -v80\n\t"
    "v_fma_f32 v81, v92, v89, -v81\n\t"
    "v_fma_f32 v82, v92, v90, -v82\n\t"
    "v_fma_f32 v83, v92, v91, -v83\n\t"
    "v_cvt_pk_bf16_f32 v94, v76, v77\n\t"
    "v_cvt_pk_bf16_f32 v95, v78, v79\n\t"
    "v_cvt_pk_bf16_f32 v96, v80, v81\n\t"
    "s_waitcnt lgkmcnt(8)\n\t"
    "v_mfma_f32_32x32x16_bf16 v[208:223], v[98:101], a[172:175], v[208:223]\n\t"
    "v_cvt_pk_bf16_f32 v97, v82, v83\n\t"
    "s_add_u32 m0, s69, 59392\n\t"
    "s_nop 0\n\t"
    "buffer_load_dwordx4 v2, s[44:47], s68 offen offset:0 lds\n\t"
    "ds_read_b128 a[208:211], v74 offset:832\n\t"
    "ds_read_b128 a[212:215], v74 offset:38208\n\t"
    "s_waitcnt lgkmcnt(9)\n\t"
    "v_mfma_f32_32x32x16_bf16 v[192:207], v[94:97], a[176:179], v[192:207]\n\t"
    "v_fma_f32 v84, v92, v76, -v84\n\t"
    "v_fma_f32 v85, v92, v77, -v85\n\t"
    "v_fma_f32 v86, v92, v78, -v86\n\t"
    "v_fma_f32 v87, v92, v79, -v87\n\t"
    "v_fma_f32 v88, v92, v80, -v88\n\t"
    "v_fma_f32 v89, v92, v81, -v89\n\t"
    "v_fma_f32 v90, v92, v82, -v90\n\t"
    "v_fma_f32 v91, v92, v83, -v91\n\t"
    "v_cvt_pk_bf16_f32 v98, v84, v85\n\t"
    "v_cvt_pk_bf16_f32 v99, v86, v87\n\t"
    "v_cvt_pk_bf16_f32 v100, v88, v89\n\t"
    "s_waitcnt lgkmcnt(8)\n\t"
    "v_mfma_f32_32x32x16_bf16 v[208:223], v[94:97], a[180:183], v[208:223]\n\t"
    "v_cvt_pk_bf16_f32 v101, v90, v91\n\t"
    "ds_read_b128 a[216:219], v74 offset:864\n\t"
    "ds_read_b128 a[220:223], v74 offset:38240\n\t"
    "s_waitcnt lgkmcnt(9)\n\t"
    "v_mfma_f32_32x32x16_bf16 v[192:207], v[98:101], a[184:187], v[192:207]\n\t"
    "v_fma_f32 v76, v92, v84, -v76\n\t"
    "v_fma_f32 v77, v92, v85, -v77\n\t"
    "v_fma_f32 v78, v92, v86, -v78\n\t"
    "v_fma_f32 v79, v92, v87, -v79\n\t"
    "v_fma_f32 v80, v92, v88, -v80\n\t"
    "v_fma_f32 v81, v92, v89, -v81\n\t"
    "v_fma_f32 v82, v92, v90, -v82\n\t"
    "v_fma_f32 v83, v92, v91, -v83\n\t"
    "v_cvt_pk_bf16_f32 v94, v76, v77\n\t"
    "v_cvt_pk_bf16_f32 v95, v78, v79\n\t"
    "v_cvt_pk_bf16_f32 v96, v80, v81\n\t"
    "s_waitcnt lgkmcnt(8)\n\t"
    "v_mfma_f32_32x32x16_bf16 v[208:223], v[98:101], a[188:191], v[208:223]\n\t"
    "v_cvt_pk_bf16_f32 v97, v82, v83\n\t"
    "ds_read_b128 a[224:227], v74 offset:896\n\t"
    "ds_read_b128 a[228:231], v74 offset:38272\n\t"
    "s_waitcnt lgkmcnt(9)\n\t"
    "v_mfma_f32_32x32x16_bf16 v[192:207], v[94:97], a[192:195], v[192:207]\n\t"
    "v_fma_f32 v84, v92, v76, -v84\n\t"
    "v_fma_f32 v85, v92, v77, -v85\n\t"
    "v_fma_f32 v86, v92, v78, -v86\n\t"
    "v_fma_f32 v87, v92, v79, -v87\n\t"
    "v_fma_f32 v88, v92, v80, -v88\n\t"
    "v_fma_f32 v89, v92, v81, -v89\n\t"
    "v_fma_f32 v90, v92, v82, -v90\n\t"
    "v_fma_f32 v91, v92, v83, -v91\n\t"
    "v_cvt_pk_bf16_f32 v98, v84, v85\n\t"
    "v_cvt_pk_bf16_f32 v99, v86, v87\n\t"
    "v_cvt_pk_bf16_f32 v100, v88, v89\n\t"
    "s_waitcnt lgkmcnt(8)\n\t"
    "v_mfma_f32_32x32x16_bf16 v[208:223], v[94:97], a[196:199], v[208:223]\n\t"
    "v_cvt_pk_bf16_f32 v101, v90, v91\n\t"
    "ds_read_b128 a[232:235], v74 offset:928\n\t"
    "ds_read_b128 a[236:239], v74 offset:38304\n\t"
    "s_waitcnt lgkmcnt(9)\n\t"
    "v_mfma_f32_32x32x16_bf16 v[192:207], v[98:101], a[200:203], v[192:207]\n\t"
    "v_fma_f32 v76, v92, v84, -v76\n\t"
    "v_fma_f32 v77, v92, v85, -v77\n\t"
    "v_fma_f32 v78, v92, v86, -v78\n\t"
    "v_fma_f32 v79, v92, v87, -v79\n\t"
    "v_fma_f32 v80, v92, v88, -v80\n\t"
    "v_fma_f32 v81, v92, v89, -v81\n\t"
    "v_fma_f32 v82, v92, v90, -v82\n\t"
    "v_fma_f32 v83, v92, v91, -v83\n\t"
    "v_cvt_pk_bf16_f32 v94, v76, v77\n\t"
    "v_cvt_pk_bf16_f32 v95, v78, v79\n\t"
    "v_cvt_pk_bf16_f32 v96, v80, v81\n\t"
    "s_waitcnt lgkmcnt(8)\n\t"
    "v_mfma_f32_32x32x16_bf16 v[208:223], v[98:101], a[204:207], v[208:223]\n\t"
    "v_cvt_pk_bf16_f32 v97, v82, v83\n\t"
    "buffer_load_dwordx4 v3, s[44:47], s68 offen offset:1024 lds\n\t"
    "ds_read_b128 a[240:243], v74 offset:960\n\t"
    "ds_read_b128 a[244:247], v74 offset:38336\n\t"
    "s_waitcnt lgkmcnt(9)\n\t"
    "v_mfma_f32_32x32x16_bf16 v[192:207], v[94:97], a[208:211], v[192:207]\n\t"
    "v_fma_f32 v84, v92, v76, -v84\n\t"
    "v_fma_f32 v85, v92, v77, -v85\n\t"
    "v_fma_f32 v86, v92, v78, -v86\n\t"
    "v_fma_f32 v87, v92, v79, -v87\n\t"
    "v_fma_f32 v88, v92, v80, -v88\n\t"
    "v_fma_f32 v89, v92, v81, -v89\n\t"
    "v_fma_f32 v90, v92, v82, -v90\n\t"
    "v_fma_f32 v91, v92, v83, -v91\n\t"
    "v_cvt_pk_bf16_f32 v98, v84, v85\n\t"
    "v_cvt_pk_bf16_f32 v99, v86, v87\n\t"
    "v_cvt_pk_bf16_f32 v100, v88, v89\n\t"
    "s_waitcnt lgkmcnt(8)\n\t"
    "v_mfma_f32_32x32x16_bf16 v[208:223], v[94:97], a[212:215], v[208:223]\n\t"
    "v_cvt_pk_bf16_f32 v101, v90, v91\n\t"
    "ds_read_b128 a[248:251], v74 offset:992\n\t"
    "ds_read_b128 a[252:255], v74 offset:38368\n\t"
    "s_waitcnt lgkmcnt(9)\n\t"
    "v_mfma_f32_32x32x16_bf16 v[192:207], v[98:101], a[216:219], v[192:207]\n\t"
    "v_fma_f32 v76, v92, v84, -v76\n\t"
    "v_fma_f32 v77, v92, v85, -v77\n\t"
    "v_fma_f32 v78, v92, v86, -v78\n\t"
    "v_fma_f32 v79, v92, v87, -v79\n\t"
    "v_fma_f32 v80, v92, v88, -v80\n\t"
    "v_fma_f32 v81, v92, v89, -v81\n\t"
    "v_fma_f32 v82, v92, v90, -v82\n\t"
    "v_fma_f32 v83, v92, v91, -v83\n\t"
    "v_cvt_pk_bf16_f32 v94, v76, v77\n\t"
    "v_cvt_pk_bf16_f32 v95, v78, v79\n\t"
    "v_cvt_pk_bf16_f32 v96, v80, v81\n\t"
    "s_waitcnt lgkmcnt(8)\n\t"
    "v_mfma_f32_32x32x16_bf16 v[208:223], v[98:101], a[220:223], v[208:223]\n\t"
    "v_cvt_pk_bf16_f32 v97, v82, v83\n\t"
    "ds_read_b128 v[224:227], v74 offset:1024\n\t"
    "ds_read_b128 v[228:231], v74 offset:38400\n\t"
    "s_waitcnt lgkmcnt(9)\n\t"
    "v_mfma_f32_32x32x16_bf16 v[192:207], v[94:97], a[224:227], v[192:207]\n\t"
    "v_fma_f32 v84, v92, v76, -v84\n\t"
    "v_fma_f32 v85, v92, v77, -v85\n\t"
    "v_fma_f32 v86, v92, v78, -v86\n\t"
    "v_fma_f32 v87, v92, v79, -v87\n\t"
    "v_fma_f32 v88, v92, v80, -v88\n\t"
    "v_fma_f32 v89, v92, v81, -v89\n\t"
    "v_fma_f32 v90, v92, v82, -v90\n\t"
    "v_fma_f32 v91, v92, v83, -v91\n\t"
    "v_cvt_pk_bf16_f32 v98, v84, v85\n\t"
    "v_cvt_pk_bf16_f32 v99, v86, v87\n\t"
    "v_cvt_pk_bf16_f32 v100, v88, v89\n\t"
    "s_waitcnt lgkmcnt(8)\n\t"
    "v_mfma_f32_32x32x16_bf16 v[208:223], v[94:97], a[228:231], v[208:223]\n\t"
    "v_cvt_pk_bf16_f32 v101, v90, v91\n\t"
    "ds_read_b128 v[232:235], v74 offset:1056\n\t"
    "ds_read_b128 v[236:239], v74 offset:38432\n\t"
    "s_waitcnt lgkmcnt(9)\n\t"
    "v_mfma_f32_32x32x16_bf16 v[192:207], v[98:101], a[232:235], v[192:207]\n\t"
    "v_fma_f32 v76, v92, v84, -v76\n\t"
    "v_fma_f32 v77, v92, v85, -v77\n\t"
    "v_fma_f32 v78, v92, v86, -v78\n\t"
    "v_fma_f32 v79, v92, v87, -v79\n\t"
    "v_fma_f32 v80, v92, v88, -v80\n\t"
    "v_fma_f32 v81, v92, v89, -v81\n\t"
    "v_fma_f32 v82, v92, v90, -v82\n\t"
    "v_fma_f32 v83, v92, v91, -v83\n\t"
    "v_cvt_pk_bf16_f32 v94, v76, v77\n\t"
    "v_cvt_pk_bf16_f32 v95, v78, v79\n\t"
    "v_cvt_pk_bf16_f32 v96, v80, v81\n\t"
    "s_waitcnt lgkmcnt(8)\n\t"
    "v_mfma_f32_32x32x16_bf16 v[208:223], v[98:101], a[236:239], v[208:223]\n\t"
    "v_cvt_pk_bf16_f32 v97, v82, v83\n\t"
    "buffer_load_dwordx4 v4, s[44:47], s68 offen offset:2048 lds\n\t"
    "s_add_u32 s68, s68, 442368\n\t"
    "ds_read_b128 v[240:243], v74 offset:1088\n\t"
    "ds_read_b128 v[244:247], v74 offset:38464\n\t"
    "s_waitcnt lgkmcnt(9)\n\t"
    "v_mfma_f32_32x32x16_bf16 v[192:207], v[94:97], a[240:243], v[192:207]\n\t"
    "v_fma_f32 v84, v92, v76, -v84\n\t"
    "v_fma_f32 v85, v92, v77, -v85\n\t"
    "v_fma_f32 v86, v92, v78, -v86\n\t"
    "v_fma_f32 v87, v92, v79, -v87\n\t"
    "v_fma_f32 v88, v92, v80, -v88\n\t"
    "v_fma_f32 v89, v92, v81, -v89\n\t"
    "v_fma_f32 v90, v92, v82, -v90\n\t"
    "v_fma_f32 v91, v92, v83, -v91\n\t"
    "v_cvt_pk_bf16_f32 v98, v84, v85\n\t"
    "v_cvt_pk_bf16_f32 v99, v86, v87\n\t"
    "v_cvt_pk_bf16_f32 v100, v88, v89\n\t"
    "s_waitcnt lgkmcnt(8)\n\t"
    "v_mfma_f32_32x32x16_bf16 v[208:223], v[94:97], a[244:247], v[208:223]\n\t"
    "v_cvt_pk_bf16_f32 v101, v90, v91\n\t"
    "ds_read_b128 v[248:251], v74 offset:1120\n\t"
    "ds_read_b128 v[252:255], v74 offset:38496\n\t"
    "s_waitcnt lgkmcnt(9)\n\t"
    "v_mfma_f32_32x32x16_bf16 v[192:207], v[98:101], a[248:251], v[192:207]\n\t"
    "v_fma_f32 v76, v92, v84, -v76\n\t"
    "v_fma_f32 v77, v92, v85, -v77\n\t"
    "v_fma_f32 v78, v92, v86, -v78\n\t"
    "v_fma_f32 v79, v92, v87, -v79\n\t"
    "v_fma_f32 v80, v92, v88, -v80\n\t"
    "v_fma_f32 v81, v92, v89, -v81\n\t"
    "v_fma_f32 v82, v92, v90, -v82\n\t"
    "v_fma_f32 v83, v92, v91, -v83\n\t"
    "v_cvt_pk_bf16_f32 v94, v76, v77\n\t"
    "v_cvt_pk_bf16_f32 v95, v78, v79\n\t"
    "v_cvt_pk_bf16_f32 v96, v80, v81\n\t"
    "s_waitcnt lgkmcnt(8)\n\t"
    "v_mfma_f32_32x32x16_bf16 v[208:223], v[98:101], a[252:255], v[208:223]\n\t"
    "v_cvt_pk_bf16_f32 v97, v82, v83\n\t"
    "s_waitcnt lgkmcnt(7)\n\t"
    "s_nop 0\n\t"
    "v_mfma_f32_32x32x16_bf16 v[192:207], v[94:97], v[224:227], v[192:207]\n\t"
    "v_fma_f32 v84, v92, v76, -v84\n\t"
    "v_fma_f32 v85, v92, v77, -v85\n\t"
    "v_fma_f32 v86, v92, v78, -v86\n\t"
    "v_fma_f32 v87, v92, v79, -v87\n\t"
    "v_fma_f32 v88, v92, v80, -v88\n\t"
    "v_fma_f32 v89, v92, v81, -v89\n\t"
    "v_fma_f32 v90, v92, v82, -v90\n\t"
    "v_fma_f32 v91, v92, v83, -v91\n\t"
    "v_cvt_pk_bf16_f32 v98, v84, v85\n\t"
    "v_cvt_pk_bf16_f32 v99, v86, v87\n\t"
    "v_cvt_pk_bf16_f32 v100, v88, v89\n\t"
    "s_waitcnt lgkmcnt(6)\n\t"
    "v_mfma_f32_32x32x16_bf16 v[208:223], v[94:97], v[228:231], v[208:223]\n\t"
    "v_cvt_pk_bf16_f32 v101, v90, v91\n\t"
    "s_waitcnt lgkmcnt(5)\n\t"
    "s_nop 0\n\t"
    "v_mfma_f32_32x32x16_bf16 v[192:207], v[98:101], v[232:235], v[192:207]\n\t"
    "v_fma_f32 v76, v92, v84, -v76\n\t"
    "v_fma_f32 v77, v92, v85, -v77\n\t"
    "v_fma_f32 v78, v92, v86, -v78\n\t"
    "v_fma_f32 v79, v92, v87, -v79\n\t"
    "v_fma_f32 v80, v92, v88, -v80\n\t"
    "v_fma_f32 v81, v92, v89, -v81\n\t"
    "v_fma_f32 v82, v92, v90, -v82\n\t"
    "v_fma_f32 v83, v92, v91, -v83\n\t"
    "v_cvt_pk_bf16_f32 v94, v76, v77\n\t"
    "v_cvt_pk_bf16_f32 v95, v78, v79\n\t"
    "v_cvt_pk_bf16_f32 v96, v80, v81\n\t"
    "s_waitcnt lgkmcnt(4)\n\t"
    "v_mfma_f32_32x32x16_bf16 v[208:223], v[98:101], v[236:239], v[208:223]\n\t"
    "v_cvt_pk_bf16_f32 v97, v82, v83\n\t"
    "s_waitcnt lgkmcnt(3)\n\t"
    "s_nop 0\n\t"
    "v_mfma_f32_32x32x16_bf16 v[192:207], v[94:97], v[240:243], v[192:207]\n\t"
    "v_fma_f32 v84, v92, v76, -v84\n\t"
    "v_fma_f32 v85, v92, v77, -v85\n\t"
    "v_fma_f32 v86, v92, v78, -v86\n\t"
    "v_fma_f32 v87, v92, v79, -v87\n\t"
    "v_fma_f32 v88, v92, v80, -v88\n\t"
    "v_fma_f32 v89, v92, v81, -v89\n\t"
    "v_fma_f32 v90, v92, v82, -v90\n\t"
    "v_fma_f32 v91, v92, v83, -v91\n\t"
    "v_cvt_pk_bf16_f32 v98, v84, v85\n\t"
    "v_cvt_pk_bf16_f32 v99, v86, v87\n\t"
    "v_cvt_pk_bf16_f32 v100, v88, v89\n\t"
    "s_waitcnt lgkmcnt(2)\n\t"
    "v_mfma_f32_32x32x16_bf16 v[208:223], v[94:97], v[244:247], v[208:223]\n\t"
    "v_cvt_pk_bf16_f32 v101, v90, v91\n\t"
    "s_waitcnt lgkmcnt(1)\n\t"
    "s_nop 0\n\t"
    "v_mfma_f32_32x32x16_bf16 v[192:207], v[98:101], v[248:251], v[192:207]\n\t"
    "s_waitcnt lgkmcnt(0)\n\t"
    "v_mfma_f32_32x32x16_bf16 v[208:223], v[98:101], v[252:255], v[208:223]\n\t"
    "v_and_b32 v13, 31, v1\n\t"
    "v_mul_u32_u24 v13, 200, v13\n\t"
    "v_lshrrev_b32 v14, 5, v1\n\t"
    "v_lshl_add_u32 v13, v14, 3, v13\n\t"
    "v_add_u32 v13, 132352, v13\n\t"
    "s_cmp_eq_u32 s64, 1\n\t"
    "s_cselect_b32 s70, 96, 0\n\t"
    "s_cselect_b32 s71, 128, 32\n\t"
    "s_cmp_eq_u32 s64, 2\n\t"
    "s_cselect_b32 s70, 64, s70\n\t"
    "s_cselect_b32 s71, 160, s71\n\t"
    "v_add_u32 v14, s71, v13\n\t"
    "v_add_u32 v13, s70, v13\n\t"
    "v_cvt_pk_bf16_f32 v94, v192, v193\n\t"
    "v_cvt_pk_bf16_f32 v95, v194, v195\n\t"
    "ds_write_b64 v13, v[94:95] offset:0\n\t"
    "v_cvt_pk_bf16_f32 v96, v196, v197\n\t"
    "v_cvt_pk_bf16_f32 v97, v198, v199\n\t"
    "ds_write_b64 v13, v[96:97] offset:16\n\t"
    "v_cvt_pk_bf16_f32 v94, v200, v201\n\t"
    "v_cvt_pk_bf16_f32 v95, v202, v203\n\t"
    "ds_write_b64 v14, v[94:95] offset:0\n\t"
    "v_cvt_pk_bf16_f32 v96, v204, v205\n\t"
    "v_cvt_pk_bf16_f32 v97, v206, v207\n\t"
    "ds_write_b64 v14, v[96:97] offset:16\n\t"
    "v_cvt_pk_bf16_f32 v94, v208, v209\n\t"
    "v_cvt_pk_bf16_f32 v95, v210, v211\n\t"
    "ds_write_b64 v13, v[94:95] offset:6400\n\t"
    "v_cvt_pk_bf16_f32 v96, v212, v213\n\t"
    "v_cvt_pk_bf16_f32 v97, v214, v215\n\t"
    "ds_write_b64 v13, v[96:97] offset:6416\n\t"
    "v_cvt_pk_bf16_f32 v94, v216, v217\n\t"
    "v_cvt_pk_bf16_f32 v95, v218, v219\n\t"
    "ds_write_b64 v14, v[94:95] offset:6400\n\t"
    "v_cvt_pk_bf16_f32 v96, v220, v221\n\t"
    "v_cvt_pk_bf16_f32 v97, v222, v223\n\t"
    "ds_write_b64 v14, v[96:97] offset:6416\n\t"
    "s_branch L_a1end_3%=\n\t"
    "L_w3_2%=:\n\t"
    "ds_read_b128 a[0:3], v74 offset:0\n\t"
    "ds_read_b128 a[4:7], v74 offset:37376\n\t"
    "ds_read_b128 a[8:11], v74 offset:32\n\t"
    "ds_read_b128 a[12:15], v74 offset:37408\n\t"
    "ds_read_b128 a[16:19], v74 offset:64\n\t"
    "ds_read_b128 a[20:23], v74 offset:37440\n\t"
    "ds_read_b128 a[24:27], v74 offset:96\n\t"
    "ds_read_b128 a[28:31], v74 offset:37472\n\t"
    "ds_read_b128 a[32:35], v74 offset:128\n\t"
    "ds_read_b128 a[36:39], v74 offset:37504\n\t"
    "ds_read_b128 a[40:43], v74 offset:160\n\t"
    "ds_read_b128 a[44:47], v74 offset:37536\n\t"
    "ds_read_b128 a[48:51], v74 offset:192\n\t"
    "ds_read_b128 a[52:55], v74 offset:37568\n\t"
    "ds_read_b128 a[56:59], v74 offset:224\n\t"
    "ds_read_b128 a[60:63], v74 offset:37600\n\t"
    "ds_read_b128 a[64:67], v74 offset:256\n\t"
    "ds_read_b128 a[68:71], v74 offset:37632\n\t"
    "ds_read_b128 a[72:75], v74 offset:288\n\t"
    "ds_read_b128 a[76:79], v74 offset:37664\n\t"
    "ds_read_b128 a[80:83], v74 offset:320\n\t"
    "ds_read_b128 a[84:87], v74 offset:37696\n\t"
    "ds_read_b128 a[88:91], v74 offset:352\n\t"
    "ds_read_b128 a[92:95], v74 offset:37728\n\t"
    "ds_read_b128 a[96:99], v74 offset:384\n\t"
    "ds_read_b128 a[100:103], v74 offset:37760\n\t"
    "ds_read_b128 a[104:107], v74 offset:416\n\t"
    "ds_read_b128 a[108:111], v74 offset:37792\n\t"
    "ds_read_b128 a[112:115], v74 offset:448\n\t"
    "ds_read_b128 a[116:119], v74 offset:37824\n\t"
    "ds_read_b128 a[120:123], v74 offset:480\n\t"
    "ds_read_b128 a[124:127], v74 offset:37856\n\t"
    "ds_read_b128 a[128:131], v74 offset:512\n\t"
    "ds_read_b128 a[132:135], v74 offset:37888\n\t"
    "ds_read_b128 a[136:139], v74 offset:544\n\t"
    "ds_read_b128 a[140:143], v74 offset:37920\n\t"
    "ds_read_b128 a[144:147], v74 offset:576\n\t"
    "ds_read_b128 a[148:151], v74 offset:37952\n\t"
    "ds_read_b128 a[152:155], v74 offset:608\n\t"
    "ds_read_b128 a[156:159], v74 offset:37984\n\t"
    "ds_read_b128 a[160:163], v74 offset:640\n\t"
    "ds_read_b128 a[164:167], v74 offset:38016\n\t"
    "ds_read_b128 a[168:171], v74 offset:672\n\t"
    "ds_read_b128 a[172:175], v74 offset:38048\n\t"
    "ds_read_b128 a[176:179], v74 offset:704\n\t"
    "ds_read_b128 a[180:183], v74 offset:38080\n\t"
    "ds_read_b128 a[184:187], v74 offset:736\n\t"
    "ds_read_b128 a[188:191], v74 offset:38112\n\t"
    "ds_read_b128 a[192:195], v74 offset:768\n\t"
    "ds_read_b128 a[196:199], v74 offset:38144\n\t"
    "ds_read_b128 a[200:203], v74 offset:800\n\t"
    "ds_read_b128 a[204:207], v74 offset:38176\n\t"
    "ds_read_b128 a[208:211], v74 offset:832\n\t"
    "ds_read_b128 a[212:215], v74 offset:38208\n\t"
    "ds_read_b128 a[216:219], v74 offset:864\n\t"
    "ds_read_b128 a[220:223], v74 offset:38240\n\t"
    "ds_read_b128 a[224:227], v74 offset:896\n\t"
    "ds_read_b128 a[228:231], v74 offset:38272\n\t"
    "ds_read_b128 a[232:235], v74 offset:928\n\t"
    "ds_read_b128 a[236:239], v74 offset:38304\n\t"
    "ds_read_b128 a[240:243], v74 offset:960\n\t"
    "ds_read_b128 a[244:247], v74 offset:38336\n\t"
    "ds_read_b128 a[248:251], v74 offset:992\n\t"
    "ds_read_b128 a[252:255], v74 offset:38368\n\t"
    "ds_read_b128 v[224:227], v74 offset:1024\n\t"
    "ds_read_b128 v[228:231], v74 offset:38400\n\t"
    "ds_read_b128 v[232:235], v74 offset:1056\n\t"
    "ds_read_b128 v[236:239], v74 offset:38432\n\t"
    "ds_read_b128 v[240:243], v74 offset:1088\n\t"
    "ds_read_b128 v[244:247], v74 offset:38464\n\t"
    "ds_read_b128 v[248:251], v74 offset:1120\n\t"
    "ds_read_b128 v[252:255], v74 offset:38496\n\t"
    "buffer_load_dwordx4 v3, s[44:47], s68 offen offset:1024 lds\n\t"
    "buffer_load_dwordx4 v4, s[44:47], s68 offen offset:2048 lds\n\t"
    "s_add_u32 s68, s68, 442368\n\t"
    "s_add_u32 m0, s69, 56320\n\t"
    "s_nop 0\n\t"
    "buffer_load_dwordx4 v2, s[44:47], s68 offen offset:0 lds\n\t"
    "buffer_load_dwordx4 v3, s[44:47], s68 offen offset:1024 lds\n\t"
    "buffer_load_dwordx4 v4, s[44:47], s68 offen offset:2048 lds\n\t"
    "s_add_u32 s68, s68, 442368\n\t"
    "s_add_u32 m0, s69, 59392\n\t"
    "s_nop 0\n\t"
    "buffer_load_dwordx4 v2, s[44:47], s68 offen offset:0 lds\n\t"
    "buffer_load_dwordx4 v3, s[44:47], s68 offen offset:1024 lds\n\t"
    "buffer_load_dwordx4 v4, s[44:47], s68 offen offset:2048 lds\n\t"
    "s_add_u32 s68, s68, 442368\n\t"
    "s_mov_b32 m0, 145728\n\t"
    "s_and_b32 s72, s65, 15\n\t"
    "v_mov_b32 v16, v1\n\t"
    "s_mul_i32 s70, s72, 36\n\t"
    "s_mul_i32 s71, s66, 1152\n\t"
    "v_add_u32 v13, 0, v16\n\t"
    "v_mul_u32_u24 v14, 3641, v13\n\t"
    "v_lshrrev_b32 v14, 16, v14\n\t"
    "v_mul_u32_u24 v15, 18, v14\n\t"
    "v_sub_u32 v15, v13, v15\n\t"
    "v_add_u32 v14, s70, v14\n\t"
    "v_mul_u32_u24 v14, 9216, v14\n\t"
    "v_lshl_add_u32 v14, v15, 6, v14\n\t"
    "v_add_u32 v14, s71, v14\n\t"
    "buffer_load_dword v14, s[44:47], 0 offen lds\n\t"
    "v_add_u32 v13, 64, v16\n\t"
    "v_mul_u32_u24 v14, 3641, v13\n\t"
    "v_lshrrev_b32 v14, 16, v14\n\t"
    "v_mul_u32_u24 v15, 18, v14\n\t"
    "v_sub_u32 v15, v13, v15\n\t"
    "v_add_u32 v14, s70, v14\n\t"
    "v_mul_u32_u24 v14, 9216, v14\n\t"
    "v_lshl_add_u32 v14, v15, 6, v14\n\t"
    "v_add_u32 v14, s71, v14\n\t"
    "buffer_load_dword v14, s[44:47], 0 offen lds\n\t"
    "v_add_u32 v13, 128, v16\n\t"
    "v_mul_u32_u24 v14, 3641, v13\n\t"
    "v_lshrrev_b32 v14, 16, v14\n\t"
    "v_mul_u32_u24 v15, 18, v14\n\t"
    "v_sub_u32 v15, v13, v15\n\t"
    "v_add_u32 v14, s70, v14\n\t"
    "v_mul_u32_u24 v14, 9216, v14\n\t"
    "v_lshl_add_u32 v14, v15, 6, v14\n\t"
    "v_add_u32 v14, s71, v14\n\t"
    "buffer_load_dword v14, s[44:47], 0 offen lds\n\t"
    "v_add_u32 v13, 192, v16\n\t"
    "v_mul_u32_u24 v14, 3641, v13\n\t"
    "v_lshrrev_b32 v14, 16, v14\n\t"
    "v_mul_u32_u24 v15, 18, v14\n\t"
    "v_sub_u32 v15, v13, v15\n\t"
    "v_add_u32 v14, s70, v14\n\t"
    "v_mul_u32_u24 v14, 9216, v14\n\t"
    "v_lshl_add_u32 v14, v15, 6, v14\n\t"
    "v_add_u32 v14, s71, v14\n\t"
    "buffer_load_dword v14, s[44:47], 0 offen lds\n\t"
    "v_add_u32 v13, 256, v16\n\t"
    "v_mul_u32_u24 v14, 3641, v13\n\t"
    "v_lshrrev_b32 v14, 16, v14\n\t"
    "v_mul_u32_u24 v15, 18, v14\n\t"
    "v_sub_u32 v15, v13, v15\n\t"
    "v_add_u32 v14, s70, v14\n\t"
    "v_mul_u32_u24 v14, 9216, v14\n\t"
    "v_lshl_add_u32 v14, v15, 6, v14\n\t"
    "v_add_u32 v14, s71, v14\n\t"
    "buffer_load_dword v14, s[44:47], 0 offen lds\n\t"
    "v_add_u32 v13, 320, v16\n\t"
    "v_mul_u32_u24 v14, 3641, v13\n\t"
    "v_lshrrev_b32 v14, 16, v14\n\t"
    "v_mul_u32_u24 v15, 18, v14\n\t"
    "v_sub_u32 v15, v13, v15\n\t"
    "v_add_u32 v14, s70, v14\n\t"
    "v_mul_u32_u24 v14, 9216, v14\n\t"
    "v_lshl_add_u32 v14, v15, 6, v14\n\t"
    "v_add_u32 v14, s71, v14\n\t"
    "buffer_load_dword v14, s[44:47], 0 offen lds\n\t"
    "v_add_u32 v13, 384, v16\n\t"
    "v_mul_u32_u24 v14, 3641, v13\n\t"
    "v_lshrrev_b32 v14, 16, v14\n\t"
    "v_mul_u32_u24 v15, 18, v14\n\t"
    "v_sub_u32 v15, v13, v15\n\t"
    "v_add_u32 v14, s70, v14\n\t"
    "v_mul_u32_u24 v14, 9216, v14\n\t"
    "v_lshl_add_u32 v14, v15, 6, v14\n\t"
    "v_add_u32 v14, s71, v14\n\t"
    "buffer_load_dword v14, s[44:47], 0 offen lds\n\t"
    "v_add_u32 v13, 448, v16\n\t"
    "v_mul_u32_u24 v14, 3641, v13\n\t"
    "v_lshrrev_b32 v14, 16, v14\n\t"
    "v_mul_u32_u24 v15, 18, v14\n\t"
    "v_sub_u32 v15, v13, v15\n\t"
    "v_add_u32 v14, s70, v14\n\t"
    "v_mul_u32_u24 v14, 9216, v14\n\t"
    "v_lshl_add_u32 v14, v15, 6, v14\n\t"
    "v_add_u32 v14, s71, v14\n\t"
    "buffer_load_dword v14, s[44:47], 0 offen lds\n\t"
    "v_add_u32 v13, 512, v16\n\t"
    "v_mul_u32_u24 v14, 3641, v13\n\t"
    "v_lshrrev_b32 v14, 16, v14\n\t"
    "v_mul_u32_u24 v15, 18, v14\n\t"
    "v_sub_u32 v15, v13, v15\n\t"
    "v_add_u32 v14, s70, v14\n\t"
    "v_mul_u32_u24 v14, 9216, v14\n\t"
    "v_lshl_add_u32 v14, v15, 6, v14\n\t"
    "v_add_u32 v14, s71, v14\n\t"
    "buffer_load_dword v14, s[44:47], 0 offen lds\n\t"
    "v_add_u32 v13, 576, v16\n\t"
    "v_mul_u32_u24 v14, 3641, v13\n\t"
    "v_lshrrev_b32 v14, 16, v14\n\t"
    "v_mul_u32_u24 v15, 18, v14\n\t"
    "v_sub_u32 v15, v13, v15\n\t"
    "v_add_u32 v14, s70, v14\n\t"
    "v_mul_u32_u24 v14, 9216, v14\n\t"
    "v_lshl_add_u32 v14, v15, 6, v14\n\t"
    "v_add_u32 v14, s71, v14\n\t"
    "buffer_load_dword v14, s[44:47], 0 offen lds\n\t"
    "v_add_u32 v13, 640, v16\n\t"
    "v_mul_u32_u24 v14, 3641, v13\n\t"
    "v_lshrrev_b32 v14, 16, v14\n\t"
    "v_mul_u32_u24 v15, 18, v14\n\t"
    "v_sub_u32 v15, v13, v15\n\t"
    "v_add_u32 v14, s70, v14\n\t"
    "v_mul_u32_u24 v14, 9216, v14\n\t"
    "v_lshl_add_u32 v14, v15, 6, v14\n\t"
    "v_add_u32 v14, s71, v14\n\t"
    "v_cmp_gt_u32 vcc, 648, v13\n\t"
    "v_mov_b32 v15, 2146435072\n\t"
    "s_nop 0\n\t"
    "v_cndmask_b32 v14, v15, v14, vcc\n\t"
    "buffer_load_dword v14, s[44:47], 0 offen lds\n\t"
    "s_mul_i32 s70, s66, 576\n\t"
    "s_mul_i32 s71, s72, 36\n\t"
    "s_add_u32 s70, s70, s71\n\t"
    "s_mul_i32 s70, s70, 1152\n\t"
    "s_waitcnt lgkmcnt(0)\n\t"
    "L_a1end_3%=:\n\t"
    "v_xor_b32 v34, 0x80008000, v26\n\t"
    "v_xor_b32 v35, 0x80008000, v27\n\t"
    "v_xor_b32 v36, 0x80008000, v28\n\t"
    "v_xor_b32 v37, 0x80008000, v29\n\t"
    "v_xor_b32 v38, 0x80008000, v30\n\t"
    "v_xor_b32 v39, 0x80008000, v31\n\t"
    "v_xor_b32 v40, 0x80008000, v32\n\t"
    "v_xor_b32 v41, 0x80008000, v33\n\t"
    "v_lshrrev_b32 v13, 4, v1\n\t"
    "v_bfe_u32 v14, v1, 2, 2\n\t"
    "v_lshl_add_u32 v14, v13, 3, v14\n\t"
    "v_mul_u32_u24 v14, 200, v14\n\t"
    "v_and_b32 v15, 3, v1\n\t"
    "v_lshl_add_u32 v14, v15, 3, v14\n\t"
    "v_add_u32 v14, 132352, v14\n\t"
    "s_lshl_b32 s70, s64, 4\n\t"
    "v_and_b32 v15, 15, v1\n\t"
    "v_add_u32 v15, s70, v15\n\t"
    "v_lshrrev_b32 v16, 1, v15\n\t"
    "v_mul_u32_u24 v16, 384, v16\n\t"
    "v_and_b32 v15, 1, v15\n\t"
    "v_mul_u32_u24 v15, 96, v15\n\t"
    "v_add_u32 v16, v16, v15\n\t"
    "v_lshl_add_u32 v16, v13, 3, v16\n\t"
    "v_add_u32 v16, 151552, v16\n\t"
    "s_waitcnt lgkmcnt(0)\n\t"
    "s_barrier\n\t"
    "ds_read_b64_tr_b16 v[76:77], v14 offset:0\n\t"
    "ds_read_b64_tr_b16 v[78:79], v14 offset:800\n\t"
    "ds_read_b64_tr_b16 v[92:93], v14 offset:32\n\t"
    "ds_read_b64_tr_b16 v[94:95], v14 offset:832\n\t"
    "ds_read_b64_tr_b16 v[108:109], v14 offset:64\n\t"
    "ds_read_b64_tr_b16 v[110:111], v14 offset:864\n\t"
    "ds_read_b64_tr_b16 v[80:81], v14 offset:96\n\t"
    "ds_read_b64_tr_b16 v[82:83], v14 offset:896\n\t"
    "ds_read_b64_tr_b16 v[96:97], v14 offset:128\n\t"
    "ds_read_b64_tr_b16 v[98:99], v14 offset:928\n\t"
    "ds_read_b64_tr_b16 v[112:113], v14 offset:160\n\t"
    "ds_read_b64_tr_b16 v[114:115], v14 offset:960\n\t"
    "ds_read_b64_tr_b16 v[84:85], v14 offset:6400\n\t"
    "ds_read_b64_tr_b16 v[86:87], v14 offset:7200\n\t"
    "ds_read_b64_tr_b16 v[100:101], v14 offset:6432\n\t"
    "ds_read_b64_tr_b16 v[102:103], v14 offset:7232\n\t"
    "ds_read_b64_tr_b16 v[116:117], v14 offset:6464\n\t"
    "ds_read_b64_tr_b16 v[118:119], v14 offset:7264\n\t"
    "ds_read_b64_tr_b16 v[88:89], v14 offset:6496\n\t"
    "ds_read_b64_tr_b16 v[90:91], v14 offset:7296\n\t"
    "ds_read_b64_tr_b16 v[104:105], v14 offset:6528\n\t"
    "ds_read_b64_tr_b16 v[106:107], v14 offset:7328\n\t"
    "ds_read_b64_tr_b16 v[120:121], v14 offset:6560\n\t"
    "ds_read_b64_tr_b16 v[122:123], v14 offset:7360\n\t"
    "s_waitcnt lgkmcnt(15)\n\t"
    "v_mfma_f32_16x16x32_bf16 v[160:163], v[76:79], v[18:21], 0\n\t"
    "v_mfma_f32_16x16x32_bf16 v[164:167], v[76:79], v[34:37], 0\n\t"
    "v_mfma_f32_16x16x32_bf16 v[168:171], v[92:95], v[18:21], 0\n\t"
    "v_mfma_f32_16x16x32_bf16 v[172:175], v[92:95], v[34:37], 0\n\t"
    "v_mfma_f32_16x16x32_bf16 v[176:179], v[108:111], v[18:21], 0\n\t"
    "v_mfma_f32_16x16x32_bf16 v[180:183], v[108:111], v[34:37], 0\n\t"
    "v_mfma_f32_16x16x32_bf16 v[160:163], v[80:83], v[26:29], v[160:163]\n\t"
    "v_mfma_f32_16x16x32_bf16 v[164:167], v[80:83], v[18:21], v[164:167]\n\t"
    "s_waitcnt lgkmcnt(14)\n\t"
    "v_mfma_f32_16x16x32_bf16 v[168:171], v[96:99], v[26:29], v[168:171]\n\t"
    "v_mfma_f32_16x16x32_bf16 v[172:175], v[96:99], v[18:21], v[172:175]\n\t"
    "s_waitcnt lgkmcnt(12)\n\t"
    "v_mfma_f32_16x16x32_bf16 v[176:179], v[112:115], v[26:29], v[176:179]\n\t"
    "v_mfma_f32_16x16x32_bf16 v[180:183], v[112:115], v[18:21], v[180:183]\n\t"
    "s_waitcnt lgkmcnt(10)\n\t"
    "v_mfma_f32_16x16x32_bf16 v[160:163], v[84:87], v[22:25], v[160:163]\n\t"
    "v_mfma_f32_16x16x32_bf16 v[164:167], v[84:87], v[38:41], v[164:167]\n\t"
    "s_waitcnt lgkmcnt(8)\n\t"
    "v_mfma_f32_16x16x32_bf16 v[168:171], v[100:103], v[22:25], v[168:171]\n\t"
    "v_mfma_f32_16x16x32_bf16 v[172:175], v[100:103], v[38:41], v[172:175]\n\t"
    "s_waitcnt lgkmcnt(6)\n\t"
    "v_mfma_f32_16x16x32_bf16 v[176:179], v[116:119], v[22:25], v[176:179]\n\t"
    "v_mfma_f32_16x16x32_bf16 v[180:183], v[116:119], v[38:41], v[180:183]\n\t"
    "s_waitcnt lgkmcnt(4)\n\t"
    "v_mfma_f32_16x16x32_bf16 v[160:163], v[88:91], v[30:33], v[160:163]\n\t"
    "v_mfma_f32_16x16x32_bf16 v[164:167], v[88:91], v[22:25], v[164:167]\n\t"
    "s_waitcnt lgkmcnt(2)\n\t"
    "v_mfma_f32_16x16x32_bf16 v[168:171], v[104:107], v[30:33], v[168:171]\n\t"
    "v_mfma_f32_16x16x32_bf16 v[172:175], v[104:107], v[22:25], v[172:175]\n\t"
    "s_waitcnt lgkmcnt(0)\n\t"
    "v_mfma_f32_16x16x32_bf16 v[176:179], v[120:123], v[30:33], v[176:179]\n\t"
    "v_mfma_f32_16x16x32_bf16 v[180:183], v[120:123], v[22:25], v[180:183]\n\t"
    "s_nop 0\n\t"
    "v_cvt_pk_bf16_f32 v184, v160, v161\n\t"
    "v_cvt_pk_bf16_f32 v185, v162, v163\n\t"
    "ds_write_b64 v16, v[184:185] offset:0\n\t"
    "v_cvt_pk_bf16_f32 v186, v164, v165\n\t"
    "v_cvt_pk_bf16_f32 v187, v166, v167\n\t"
    "ds_write_b64 v16, v[186:187] offset:192\n\t"
    "v_cvt_pk_bf16_f32 v188, v168, v169\n\t"
    "v_cvt_pk_bf16_f32 v189, v170, v171\n\t"
    "ds_write_b64 v16, v[188:189] offset:32\n\t"
    "v_cvt_pk_bf16_f32 v190, v172, v173\n\t"
    "v_cvt_pk_bf16_f32 v191, v174, v175\n\t"
    "ds_write_b64 v16, v[190:191] offset:224\n\t"
    "v_cvt_pk_bf16_f32 v192, v176, v177\n\t"
    "v_cvt_pk_bf16_f32 v193, v178, v179\n\t"
    "ds_write_b64 v16, v[192:193] offset:64\n\t"
    "v_cvt_pk_bf16_f32 v194, v180, v181\n\t"
    "v_cvt_pk_bf16_f32 v195, v182, v183\n\t"
    "ds_write_b64 v16, v[194:195] offset:256\n\t"
    "s_mul_i32 s70, s66, 393216\n\t"
    "s_mul_i32 s71, s65, 192\n\t"
    "s_add_u32 s70, s70, s71\n\t"
    "s_add_u32 s70, s70, 262144\n\t"
    "s_waitcnt lgkmcnt(0)\n\t"
    "s_barrier\n\t"
    "ds_read_b128 v[18:21], v75 offset:0\n\t"
    "ds_read_b128 v[22:25], v75 offset:64\n\t"
    "ds_read_b128 v[26:29], v75 offset:128\n\t"
    "s_waitcnt lgkmcnt(2)\n\t"
    "buffer_store_dwordx4 v[18:21], v9, s[40:43], s70 offen offset:0 sc1\n\t"
    "s_waitcnt lgkmcnt(1)\n\t"
    "buffer_store_dwordx4 v[22:25], v9, s[40:43], s70 offen offset:64 sc1\n\t"
    "s_waitcnt lgkmcnt(0)\n\t"
    "buffer_store_dwordx4 v[26:29], v9, s[40:43], s70 offen offset:128 sc1\n\t"
    "s_lshl_b32 s70, s79, 1\n\t"
    "v_add_u32 v74, s70, v6\n\t"
    "ds_read_b128 v[144:147], v74 offset:0\n\t"
    "ds_read_b128 v[148:151], v74 offset:32\n\t"
    "ds_read_b128 v[152:155], v74 offset:64\n\t"
    "ds_read_b128 v[156:159], v74 offset:96\n\t"
    "s_waitcnt vmcnt(13)\n\t"
    "ds_read_b64_tr_b16 v[132:133], v5 offset:50176\n\t"
    "ds_read_b64_tr_b16 v[134:135], v5 offset:50432\n\t"
    "ds_read_b64_tr_b16 v[136:137], v5 offset:51200\n\t"
    "ds_read_b64_tr_b16 v[138:139], v5 offset:51456\n\t"
    "s_waitcnt vmcnt(12) lgkmcnt(2)\n\t"
    "v_mfma_f32_32x32x16_bf16 v[160:175], v[132:135], a[0:3], v[144:159]\n\t"
    "ds_read_b64_tr_b16 v[140:141], v5 offset:52224\n\t"
    "ds_read_b64_tr_b16 v[142:143], v5 offset:52480\n\t"
    "v_mfma_f32_32x32x16_bf16 v[176:191], v[132:135], a[4:7], v[144:159]\n\t"
    "s_waitcnt vmcnt(11) lgkmcnt(2)\n\t"
    "v_mfma_f32_32x32x16_bf16 v[160:175], v[136:139], a[8:11], v[160:175]\n\t"
    "s_add_u32 m0, s69, 50176\n\t"
    "ds_read_b64_tr_b16 v[132:133], v5 offset:53248\n\t"
    "buffer_load_dwordx4 v2, s[44:47], s68 offen offset:0 lds\n\t"
    "ds_read_b64_tr_b16 v[134:135], v5 offset:53504\n\t"
    "v_mfma_f32_32x32x16_bf16 v[176:191], v[136:139], a[12:15], v[176:191]\n\t"
    "s_waitcnt vmcnt(11) lgkmcnt(2)\n\t"
    "v_mfma_f32_32x32x16_bf16 v[160:175], v[140:143], a[16:19], v[160:175]\n\t"
    "ds_read_b64_tr_b16 v[136:137], v5 offset:54272\n\t"
    "buffer_load_dwordx4 v3, s[44:47], s68 offen offset:1024 lds\n\t"
    "ds_read_b64_tr_b16 v[138:139], v5 offset:54528\n\t"
    "v_mfma_f32_32x32x16_bf16 v[176:191], v[140:143], a[20:23], v[176:191]\n\t"
    "s_waitcnt vmcnt(11) lgkmcnt(2)\n\t"
    "v_mfma_f32_32x32x16_bf16 v[160:175], v[132:135], a[24:27], v[160:175]\n\t"
    "ds_read_b64_tr_b16 v[140:141], v5 offset:55296\n\t"
    "buffer_load_dwordx4 v4, s[44:47], s68 offen offset:2048 lds\n\t"
    "s_add_u32 s68, s68, 442368\n\t"
    "ds_read_b64_tr_b16 v[142:143], v5 offset:55552\n\t"
    "v_mfma_f32_32x32x16_bf16 v[176:191], v[132:135], a[28:31], v[176:191]\n\t"
    "s_waitcnt vmcnt(11) lgkmcnt(2)\n\t"
    "v_mfma_f32_32x32x16_bf16 v[160:175], v[136:139], a[32:35], v[160:175]\n\t"
    "s_add_u32 m0, s69, 53248\n\t"
    "ds_read_b64_tr_b16 v[132:133], v5 offset:56320\n\t"
    "buffer_load_dwordx4 v2, s[44:47], s68 offen offset:0 lds\n\t"
    "ds_read_b64_tr_b16 v[134:135], v5 offset:56576\n\t"
    "v_mfma_f32_32x32x16_bf16 v[176:191], v[136:139], a[36:39], v[176:191]\n\t"
    "s_waitcnt vmcnt(11) lgkmcnt(2)\n\t"
    "v_mfma_f32_32x32x16_bf16 v[160:175], v[140:143], a[40:43], v[160:175]\n\t"
    "ds_read_b64_tr_b16 v[136:137], v5 offset:57344\n\t"
    "buffer_load_dwordx4 v3, s[44:47], s68 offen offset:1024 lds\n\t"
    "ds_read_b64_tr_b16 v[138:139], v5 offset:57600\n\t"
    "v_mfma_f32_32x32x16_bf16 v[176:191], v[140:143], a[44:47], v[176:191]\n\t"
    "s_waitcnt vmcnt(11) lgkmcnt(2)\n\t"
    "v_mfma_f32_32x32x16_bf16 v[160:175], v[132:135], a[48:51], v[160:175]\n\t"
    "ds_read_b64_tr_b16 v[140:141], v5 offset:58368\n\t"
    "buffer_load_dwordx4 v4, s[44:47], s68 offen offset:2048 lds\n\t"
    "s_add_u32 s68, s68, 442368\n\t"
    "ds_read_b64_tr_b16 v[142:143], v5 offset:58624\n\t"
    "v_mfma_f32_32x32x16_bf16 v[176:191], v[132:135], a[52:55], v[176:191]\n\t"
    "s_waitcnt vmcnt(11) lgkmcnt(2)\n\t"
    "v_mfma_f32_32x32x16_bf16 v[160:175], v[136:139], a[56:59], v[160:175]\n\t"
    "s_add_u32 m0, s69, 56320\n\t"
    "ds_read_b64_tr_b16 v[132:133], v5 offset:59392\n\t"
    "buffer_load_dwordx4 v2, s[44:47], s68 offen offset:0 lds\n\t"
    "ds_read_b64_tr_b16 v[134:135], v5 offset:59648\n\t"
    "v_mfma_f32_32x32x16_bf16 v[176:191], v[136:139], a[60:63], v[176:191]\n\t"
    "s_waitcnt vmcnt(11) lgkmcnt(2)\n\t"
    "v_mfma_f32_32x32x16_bf16 v[160:175], v[140:143], a[64:67], v[160:175]\n\t"
    "ds_read_b64_tr_b16 v[136:137], v5 offset:60416\n\t"
    "buffer_load_dwordx4 v3, s[44:47], s68 offen offset:1024 lds\n\t"
    "ds_read_b64_tr_b16 v[138:139], v5 offset:60672\n\t"
    "v_mfma_f32_32x32x16_bf16 v[176:191], v[140:143], a[68:71], v[176:191]\n\t"
    "s_waitcnt vmcnt(11) lgkmcnt(2)\n\t"
    "v_mfma_f32_32x32x16_bf16 v[160:175], v[132:135], a[72:75], v[160:175]\n\t"
    "ds_read_b64_tr_b16 v[140:141], v5 offset:61440\n\t"
    "buffer_load_dwordx4 v4, s[44:47], s68 offen offset:2048 lds\n\t"
    "s_add_u32 s68, s68, 442368\n\t"
    "ds_read_b64_tr_b16 v[142:143], v5 offset:61696\n\t"
    "v_mfma_f32_32x32x16_bf16 v[176:191], v[132:135], a[76:79], v[176:191]\n\t"
    "s_waitcnt vmcnt(8) lgkmcnt(2)\n\t"
    "v_mfma_f32_32x32x16_bf16 v[160:175], v[136:139], a[80:83], v[160:175]\n\t"
    "s_add_u32 m0, s69, 59392\n\t"
    "ds_read_b64_tr_b16 v[132:133], v5 offset:50176\n\t"
    "buffer_load_dwordx4 v2, s[44:47], s68 offen offset:0 lds\n\t"
    "ds_read_b64_tr_b16 v[134:135], v5 offset:50432\n\t"
    "v_mfma_f32_32x32x16_bf16 v[176:191], v[136:139], a[84:87], v[176:191]\n\t"
    "s_lshl_b32 s70, s80, 1\n\t"
    "v_add_u32 v74, s70, v6\n\t"
    "ds_read_b128 v[144:147], v74 offset:0\n\t"
    "ds_read_b128 v[148:151], v74 offset:32\n\t"
    "ds_read_b128 v[152:155], v74 offset:64\n\t"
    "ds_read_b128 v[156:159], v74 offset:96\n\t"
    "s_waitcnt vmcnt(8) lgkmcnt(6)\n\t"
    "v_mfma_f32_32x32x16_bf16 v[160:175], v[140:143], a[88:91], v[160:175]\n\t"
    "ds_read_b64_tr_b16 v[136:137], v5 offset:51200\n\t"
    "buffer_load_dwordx4 v3, s[44:47], s68 offen offset:1024 lds\n\t"
    "ds_read_b64_tr_b16 v[138:139], v5 offset:51456\n\t"
    "v_mfma_f32_32x32x16_bf16 v[176:191], v[140:143], a[92:95], v[176:191]\n\t"
    "s_waitcnt vmcnt(8) lgkmcnt(6)\n\t"
    "v_mfma_f32_32x32x16_bf16 v[160:175], v[132:135], a[96:99], v[160:175]\n\t"
    "ds_read_b64_tr_b16 v[140:141], v5 offset:52224\n\t"
    "buffer_load_dwordx4 v4, s[44:47], s68 offen offset:2048 lds\n\t"
    "s_add_u32 s68, s68, 442368\n\t"
    "ds_read_b64_tr_b16 v[142:143], v5 offset:52480\n\t"
    "v_mfma_f32_32x32x16_bf16 v[176:191], v[132:135], a[100:103], v[176:191]\n\t"
    "s_waitcnt vmcnt(8) lgkmcnt(2)\n\t"
    "v_mfma_f32_32x32x16_bf16 v[160:175], v[136:139], a[104:107], v[160:175]\n\t"
    "s_add_u32 m0, s69, 50176\n\t"
    "ds_read_b64_tr_b16 v[132:133], v5 offset:53248\n\t"
    "buffer_load_dwordx4 v2, s[44:47], s68 offen offset:0 lds\n\t"
    "ds_read_b64_tr_b16 v[134:135], v5 offset:53504\n\t"
    "v_mfma_f32_32x32x16_bf16 v[176:191], v[136:139], a[108:111], v[176:191]\n\t"
    "s_waitcnt vmcnt(8) lgkmcnt(2)\n\t"
    "v_mfma_f32_32x32x16_bf16 v[160:175], v[140:143], a[112:115], v[160:175]\n\t"
    "ds_read_b64_tr_b16 v[136:137], v5 offset:54272\n\t"
    "buffer_load_dwordx4 v3, s[44:47], s68 offen offset:1024 lds\n\t"
    "ds_read_b64_tr_b16 v[138:139], v5 offset:54528\n\t"
    "v_mfma_f32_32x32x16_bf16 v[176:191], v[140:143], a[116:119], v[176:191]\n\t"
    "v_mov_b32 v13, 145664\n\t"
    "v_cmp_eq_u32 vcc, 0, v1\n\t"
    "s_nop 1\n\t"
    "v_cndmask_b32 v14, 0, 1, vcc\n\t"
    "ds_add_rtn_u32 v12, v13, v14\n\t"
    "s_waitcnt vmcnt(8) lgkmcnt(3)\n\t"
    "v_mfma_f32_32x32x16_bf16 v[160:175], v[132:135], a[120:123], v[160:175]\n\t"
    "ds_read_b64_tr_b16 v[140:141], v5 offset:55296\n\t"
    "buffer_load_dwordx4 v4, s[44:47], s68 offen offset:2048 lds\n\t"
    "s_add_u32 s68, s68, 442368\n\t"
    "ds_read_b64_tr_b16 v[142:143], v5 offset:55552\n\t"
    "v_mfma_f32_32x32x16_bf16 v[176:191], v[132:135], a[124:127], v[176:191]\n\t"
    "s_waitcnt vmcnt(8) lgkmcnt(3)\n\t"
    "v_mfma_f32_32x32x16_bf16 v[160:175], v[136:139], a[128:131], v[160:175]\n\t"
    "s_add_u32 m0, s69, 53248\n\t"
    "ds_read_b64_tr_b16 v[132:133], v5 offset:56320\n\t"
    "buffer_load_dwordx4 v2, s[44:47], s68 offen offset:0 lds\n\t"
    "ds_read_b64_tr_b16 v[134:135], v5 offset:56576\n\t"
    "v_mfma_f32_32x32x16_bf16 v[176:191], v[136:139], a[132:135], v[176:191]\n\t"
    "s_waitcnt vmcnt(8) lgkmcnt(2)\n\t"
    "v_mfma_f32_32x32x16_bf16 v[160:175], v[140:143], a[136:139], v[160:175]\n\t"
    "ds_read_b64_tr_b16 v[136:137], v5 offset:57344\n\t"
    "buffer_load_dwordx4 v3, s[44:47], s68 offen offset:1024 lds\n\t"
    "ds_read_b64_tr_b16 v[138:139], v5 offset:57600\n\t"
    "v_mfma_f32_32x32x16_bf16 v[176:191], v[140:143], a[140:143], v[176:191]\n\t"
    "s_waitcnt vmcnt(8) lgkmcnt(2)\n\t"
    "v_mfma_f32_32x32x16_bf16 v[160:175], v[132:135], a[144:147], v[160:175]\n\t"
    "ds_read_b64_tr_b16 v[140:141], v5 offset:58368\n\t"
    "buffer_load_dwordx4 v4, s[44:47], s68 offen offset:2048 lds\n\t"
    "s_add_u32 s68, s68, 442368\n\t"
    "ds_read_b64_tr_b16 v[142:143], v5 offset:58624\n\t"
    "v_mfma_f32_32x32x16_bf16 v[176:191], v[132:135], a[148:151], v[176:191]\n\t"
    "s_lshl_b32 s71, s66, 7\n\t"
    "s_add_u32 s71, s71, 256\n\t"
    "s_lshl_b32 s76, s65, 2\n\t"
    "s_add_u32 s71, s71, s76\n\t"
    "v_cmp_eq_u32 vcc, 3, v12\n\t"
    "v_mov_b32 v13, 2146435072\n\t"
    "s_nop 0\n\t"
    "v_cndmask_b32 v13, v13, v10, vcc\n\t"
    "v_mov_b32 v14, s67\n\t"
    "buffer_store_dword v14, v13, s[40:43], s71 offen sc1\n\t"
    "s_waitcnt vmcnt(9) lgkmcnt(2)\n\t"
    "v_mfma_f32_32x32x16_bf16 v[160:175], v[136:139], a[152:155], v[160:175]\n\t"
    "s_add_u32 m0, s69, 56320\n\t"
    "ds_read_b64_tr_b16 v[132:133], v5 offset:59392\n\t"
    "buffer_load_dwordx4 v2, s[44:47], s68 offen offset:0 lds\n\t"
    "ds_read_b64_tr_b16 v[134:135], v5 offset:59648\n\t"
    "v_mfma_f32_32x32x16_bf16 v[176:191], v[136:139], a[156:159], v[176:191]\n\t"
    "s_waitcnt vmcnt(9) lgkmcnt(2)\n\t"
    "v_mfma_f32_32x32x16_bf16 v[160:175], v[140:143], a[160:163], v[160:175]\n\t"
    "ds_read_b64_tr_b16 v[136:137], v5 offset:60416\n\t"
    "buffer_load_dwordx4 v3, s[44:47], s68 offen offset:1024 lds\n\t"
    "ds_read_b64_tr_b16 v[138:139], v5 offset:60672\n\t"
    "v_mfma_f32_32x32x16_bf16 v[176:191], v[140:143], a[164:167], v[176:191]\n\t"
    "s_waitcnt vmcnt(9) lgkmcnt(2)\n\t"
    "v_mfma_f32_32x32x16_bf16 v[160:175], v[132:135], a[168:171], v[160:175]\n\t"
    "ds_read_b64_tr_b16 v[140:141], v5 offset:61440\n\t"
    "buffer_load_dwordx4 v4, s[44:47], s68 offen offset:2048 lds\n\t"
    "s_add_u32 s68, s68, 442368\n\t"
    "ds_read_b64_tr_b16 v[142:143], v5 offset:61696\n\t"
    "v_mfma_f32_32x32x16_bf16 v[176:191], v[132:135], a[172:175], v[176:191]\n\t"
    "s_waitcnt vmcnt(9) lgkmcnt(2)\n\t"
    "v_mfma_f32_32x32x16_bf16 v[160:175], v[136:139], a[176:179], v[160:175]\n\t"
    "s_add_u32 m0, s69, 59392\n\t"
    "ds_read_b64_tr_b16 v[132:133], v5 offset:50176\n\t"
    "buffer_load_dwordx4 v2, s[44:47], s68 offen offset:0 lds\n\t"
    "ds_read_b64_tr_b16 v[134:135], v5 offset:50432\n\t"
    "v_mfma_f32_32x32x16_bf16 v[176:191], v[136:139], a[180:183], v[176:191]\n\t"
    "s_waitcnt vmcnt(9) lgkmcnt(2)\n\t"
    "v_mfma_f32_32x32x16_bf16 v[160:175], v[140:143], a[184:187], v[160:175]\n\t"
    "ds_read_b64_tr_b16 v[136:137], v5 offset:51200\n\t"
    "buffer_load_dwordx4 v3, s[44:47], s68 offen offset:1024 lds\n\t"
    "ds_read_b64_tr_b16 v[138:139], v5 offset:51456\n\t"
    "v_mfma_f32_32x32x16_bf16 v[176:191], v[140:143], a[188:191], v[176:191]\n\t"
    "s_waitcnt vmcnt(9) lgkmcnt(2)\n\t"
    "v_mfma_f32_32x32x16_bf16 v[160:175], v[132:135], a[192:195], v[160:175]\n\t"
    "ds_read_b64_tr_b16 v[140:141], v5 offset:52224\n\t"
    "buffer_load_dwordx4 v4, s[44:47], s68 offen offset:2048 lds\n\t"
    "s_mov_b32 s68, s84\n\t"
    "ds_read_b64_tr_b16 v[142:143], v5 offset:52480\n\t"
    "v_mfma_f32_32x32x16_bf16 v[176:191], v[132:135], a[196:199], v[176:191]\n\t"
    "s_waitcnt vmcnt(9) lgkmcnt(2)\n\t"
    "v_mfma_f32_32x32x16_bf16 v[160:175], v[136:139], a[200:203], v[160:175]\n\t"
    "s_add_u32 m0, s69, 50176\n\t"
    "ds_read_b64_tr_b16 v[132:133], v5 offset:53248\n\t"
    "buffer_load_dwordx4 v2, s[44:47], s68 offen offset:0 lds\n\t"
    "ds_read_b64_tr_b16 v[134:135], v5 offset:53504\n\t"
    "v_mfma_f32_32x32x16_bf16 v[176:191], v[136:139], a[204:207], v[176:191]\n\t"
    "s_waitcnt vmcnt(9) lgkmcnt(2)\n\t"
    "v_mfma_f32_32x32x16_bf16 v[160:175], v[140:143], a[208:211], v[160:175]\n\t"
    "ds_read_b64_tr_b16 v[136:137], v5 offset:54272\n\t"
    "buffer_load_dwordx4 v3, s[44:47], s68 offen offset:1024 lds\n\t"
    "ds_read_b64_tr_b16 v[138:139], v5 offset:54528\n\t"
    "v_mfma_f32_32x32x16_bf16 v[176:191], v[140:143], a[212:215], v[176:191]\n\t"
    "s_waitcnt vmcnt(9) lgkmcnt(2)\n\t"
    "v_mfma_f32_32x32x16_bf16 v[160:175], v[132:135], a[216:219], v[160:175]\n\t"
    "ds_read_b64_tr_b16 v[140:141], v5 offset:55296\n\t"
    "buffer_load_dwordx4 v4, s[44:47], s68 offen offset:2048 lds\n\t"
    "s_add_u32 s68, s68, 442368\n\t"
    "ds_read_b64_tr_b16 v[142:143], v5 offset:55552\n\t"
    "v_mfma_f32_32x32x16_bf16 v[176:191], v[132:135], a[220:223], v[176:191]\n\t"
    "s_waitcnt vmcnt(8) lgkmcnt(2)\n\t"
    "v_mfma_f32_32x32x16_bf16 v[160:175], v[136:139], a[224:227], v[160:175]\n\t"
    "s_add_u32 m0, s69, 53248\n\t"
    "ds_read_b64_tr_b16 v[132:133], v5 offset:56320\n\t"
    "buffer_load_dwordx4 v2, s[44:47], s68 offen offset:0 lds\n\t"
    "ds_read_b64_tr_b16 v[134:135], v5 offset:56576\n\t"
    "v_mfma_f32_32x32x16_bf16 v[176:191], v[136:139], a[228:231], v[176:191]\n\t"
    "s_waitcnt vmcnt(8) lgkmcnt(2)\n\t"
    "v_mfma_f32_32x32x16_bf16 v[160:175], v[140:143], a[232:235], v[160:175]\n\t"
    "ds_read_b64_tr_b16 v[136:137], v5 offset:57344\n\t"
    "buffer_load_dwordx4 v3, s[44:47], s68 offen offset:1024 lds\n\t"
    "ds_read_b64_tr_b16 v[138:139], v5 offset:57600\n\t"
    "v_mfma_f32_32x32x16_bf16 v[176:191], v[140:143], a[236:239], v[176:191]\n\t"
    "s_waitcnt vmcnt(8) lgkmcnt(2)\n\t"
    "v_mfma_f32_32x32x16_bf16 v[160:175], v[132:135], a[240:243], v[160:175]\n\t"
    "ds_read_b64_tr_b16 v[140:141], v5 offset:58368\n\t"
    "buffer_load_dwordx4 v4, s[44:47], s68 offen offset:2048 lds\n\t"
    "s_add_u32 s68, s68, 442368\n\t"
    "ds_read_b64_tr_b16 v[142:143], v5 offset:58624\n\t"
    "v_mfma_f32_32x32x16_bf16 v[176:191], v[132:135], a[244:247], v[176:191]\n\t"
    "s_waitcnt vmcnt(8) lgkmcnt(2)\n\t"
    "v_mfma_f32_32x32x16_bf16 v[160:175], v[136:139], a[248:251], v[160:175]\n\t"
    "s_add_u32 m0, s69, 56320\n\t"
    "ds_read_b64_tr_b16 v[132:133], v5 offset:59392\n\t"
    "buffer_load_dwordx4 v2, s[44:47], s68 offen offset:0 lds\n\t"
    "ds_read_b64_tr_b16 v[134:135], v5 offset:59648\n\t"
    "v_mfma_f32_32x32x16_bf16 v[176:191], v[136:139], a[252:255], v[176:191]\n\t"
    "s_waitcnt vmcnt(8) lgkmcnt(2)\n\t"
    "v_mfma_f32_32x32x16_bf16 v[160:175], v[140:143], v[224:227], v[160:175]\n\t"
    "ds_read_b64_tr_b16 v[136:137], v5 offset:60416\n\t"
    "buffer_load_dwordx4 v3, s[44:47], s68 offen offset:1024 lds\n\t"
    "ds_read_b64_tr_b16 v[138:139], v5 offset:60672\n\t"
    "v_mfma_f32_32x32x16_bf16 v[176:191], v[140:143], v[228:231], v[176:191]\n\t"
    "s_waitcnt vmcnt(8) lgkmcnt(2)\n\t"
    "v_mfma_f32_32x32x16_bf16 v[160:175], v[132:135], v[232:235], v[160:175]\n\t"
    "ds_read_b64_tr_b16 v[140:141], v5 offset:61440\n\t"
    "buffer_load_dwordx4 v4, s[44:47], s68 offen offset:2048 lds\n\t"
    "s_add_u32 s68, s68, 442368\n\t"
    "ds_read_b64_tr_b16 v[142:143], v5 offset:61696\n\t"
    "v_mfma_f32_32x32x16_bf16 v[176:191], v[132:135], v[236:239], v[176:191]\n\t"
    "s_waitcnt vmcnt(8) lgkmcnt(2)\n\t"
    "v_mfma_f32_32x32x16_bf16 v[160:175], v[136:139], v[240:243], v[160:175]\n\t"
    "s_add_u32 m0, s69, 59392\n\t"
    "ds_read_b64_tr_b16 v[132:133], v5 offset:50176\n\t"
    "buffer_load_dwordx4 v2, s[44:47], s68 offen offset:0 lds\n\t"
    "ds_read_b64_tr_b16 v[134:135], v5 offset:50432\n\t"
    "v_mfma_f32_32x32x16_bf16 v[176:191], v[136:139], v[244:247], v[176:191]\n\t"
    "s_waitcnt vmcnt(8) lgkmcnt(2)\n\t"
    "v_mfma_f32_32x32x16_bf16 v[160:175], v[140:143], v[248:251], v[160:175]\n\t"
    "ds_read_b64_tr_b16 v[136:137], v5 offset:51200\n\t"
    "buffer_load_dwordx4 v3, s[44:47], s68 offen offset:1024 lds\n\t"
    "ds_read_b64_tr_b16 v[138:139], v5 offset:51456\n\t"
    "v_mfma_f32_32x32x16_bf16 v[176:191], v[140:143], v[252:255], v[176:191]\n\t"
    "s_waitcnt vmcnt(8) lgkmcnt(2)\n\t"
    "v_mfma_f32_32x32x16_bf16 v[192:207], v[132:135], a[0:3], v[144:159]\n\t"
    "ds_read_b64_tr_b16 v[140:141], v5 offset:52224\n\t"
    "buffer_load_dwordx4 v4, s[44:47], s68 offen offset:2048 lds\n\t"
    "s_add_u32 s68, s68, 442368\n\t"
    "ds_read_b64_tr_b16 v[142:143], v5 offset:52480\n\t"
    "v_mfma_f32_32x32x16_bf16 v[208:223], v[132:135], a[4:7], v[144:159]\n\t"
    "s_waitcnt vmcnt(8) lgkmcnt(2)\n\t"
    "v_mfma_f32_32x32x16_bf16 v[192:207], v[136:139], a[8:11], v[192:207]\n\t"
    "s_add_u32 m0, s69, 50176\n\t"
    "ds_read_b64_tr_b16 v[132:133], v5 offset:53248\n\t"
    "buffer_load_dwordx4 v2, s[44:47], s68 offen offset:0 lds\n\t"
    "ds_read_b64_tr_b16 v[134:135], v5 offset:53504\n\t"
    "v_exp_f32 v66, v160\n\t"
    "v_mfma_f32_32x32x16_bf16 v[208:223], v[136:139], a[12:15], v[208:223]\n\t"
    "v_exp_f32 v67, v161\n\t"
    "v_fma_f32 v66, v66, s78, s78\n\t"
    "v_exp_f32 v68, v162\n\t"
    "s_waitcnt vmcnt(8) lgkmcnt(2)\n\t"
    "v_mfma_f32_32x32x16_bf16 v[192:207], v[140:143], a[16:19], v[192:207]\n\t"
    "ds_read_b64_tr_b16 v[136:137], v5 offset:54272\n\t"
    "buffer_load_dwordx4 v3, s[44:47], s68 offen offset:1024 lds\n\t"
    "ds_read_b64_tr_b16 v[138:139], v5 offset:54528\n\t"
    "v_fma_f32 v67, v67, s78, s78\n\t"
    "v_fma_f32 v66, v66, v66, 0.5\n\t"
    "v_mfma_f32_32x32x16_bf16 v[208:223], v[140:143], a[20:23], v[208:223]\n\t"
    "v_exp_f32 v69, v163\n\t"
    "v_fma_f32 v68, v68, s78, s78\n\t"
    "v_fma_f32 v67, v67, v67, 0.5\n\t"
    "v_and_b32 v15, 31, v1\n\t"
    "v_lshlrev_b32 v15, 2, v15\n\t"
    "s_lshl_b32 s71, s66, 7\n\t"
    "s_add_u32 s71, s71, 256\n\t"
    "s_cmp_eq_u32 s64, 0\n\t"
    "s_cselect_b32 s71, s71, 2146435072\n\t"
    "buffer_load_dword v11, v15, s[40:43], s71 offen sc1\n\t"
    "s_waitcnt vmcnt(9) lgkmcnt(2)\n\t"
    "v_mfma_f32_32x32x16_bf16 v[192:207], v[132:135], a[24:27], v[192:207]\n\t"
    "ds_read_b64_tr_b16 v[140:141], v5 offset:55296\n\t"
    "buffer_load_dwordx4 v4, s[44:47], s68 offen offset:2048 lds\n\t"
    "s_add_u32 s68, s68, 442368\n\t"
    "ds_read_b64_tr_b16 v[142:143], v5 offset:55552\n\t"
    "v_rcp_f32 v66, v66\n\t"
    "v_mfma_f32_32x32x16_bf16 v[208:223], v[132:135], a[28:31], v[208:223]\n\t"
    "v_exp_f32 v70, v164\n\t"
    "v_fma_f32 v69, v69, s78, s78\n\t"
    "v_fma_f32 v68, v68, v68, 0.5\n\t"
    "v_rcp_f32 v67, v67\n\t"
    "s_waitcnt vmcnt(9) lgkmcnt(2)\n\t"
    "v_mfma_f32_32x32x16_bf16 v[192:207], v[136:139], a[32:35], v[192:207]\n\t"
    "s_add_u32 m0, s69, 53248\n\t"
    "ds_read_b64_tr_b16 v[132:133], v5 offset:56320\n\t"
    "buffer_load_dwordx4 v2, s[44:47], s68 offen offset:0 lds\n\t"
    "ds_read_b64_tr_b16 v[134:135], v5 offset:56576\n\t"
    "v_fma_f32 v66, -v160, v66, v160\n\t"
    "v_mfma_f32_32x32x16_bf16 v[208:223], v[136:139], a[36:39], v[208:223]\n\t"
    "v_exp_f32 v71, v165\n\t"
    "v_fma_f32 v70, v70, s78, s78\n\t"
    "v_fma_f32 v69, v69, v69, 0.5\n\t"
    "s_waitcnt vmcnt(9) lgkmcnt(2)\n\t"
    "v_mfma_f32_32x32x16_bf16 v[192:207], v[140:143], a[40:43], v[192:207]\n\t"
    "ds_read_b64_tr_b16 v[136:137], v5 offset:57344\n\t"
    "buffer_load_dwordx4 v3, s[44:47], s68 offen offset:1024 lds\n\t"
    "ds_read_b64_tr_b16 v[138:139], v5 offset:57600\n\t"
    "v_rcp_f32 v68, v68\n\t"
    "v_fma_f32 v67, -v161, v67, v161\n\t"
    "v_mfma_f32_32x32x16_bf16 v[208:223], v[140:143], a[44:47], v[208:223]\n\t"
    "v_cvt_pk_bf16_f32 v34, v66, v67\n\t"
    "v_exp_f32 v72, v166\n\t"
    "v_fma_f32 v71, v71, s78, s78\n\t"
    "v_fma_f32 v70, v70, v70, 0.5\n\t"
    "s_waitcnt vmcnt(9) lgkmcnt(2)\n\t"
    "v_mfma_f32_32x32x16_bf16 v[192:207], v[132:135], a[48:51], v[192:207]\n\t"
    "ds_read_b64_tr_b16 v[140:141], v5 offset:58368\n\t"
    "buffer_load_dwordx4 v4, s[44:47], s68 offen offset:2048 lds\n\t"
    "s_add_u32 s68, s68, 442368\n\t"
    "ds_read_b64_tr_b16 v[142:143], v5 offset:58624\n\t"
    "v_rcp_f32 v69, v69\n\t"
    "v_mfma_f32_32x32x16_bf16 v[208:223], v[132:135], a[52:55], v[208:223]\n\t"
    "v_fma_f32 v68, -v162, v68, v162\n\t"
    "v_exp_f32 v73, v167\n\t"
    "v_fma_f32 v72, v72, s78, s78\n\t"
    "v_fma_f32 v71, v71, v71, 0.5\n\t"
    "s_waitcnt vmcnt(9) lgkmcnt(2)\n\t"
    "v_mfma_f32_32x32x16_bf16 v[192:207], v[136:139], a[56:59], v[192:207]\n\t"
    "s_add_u32 m0, s69, 56320\n\t"
    "ds_read_b64_tr_b16 v[132:133], v5 offset:59392\n\t"
    "buffer_load_dwordx4 v2, s[44:47], s68 offen offset:0 lds\n\t"
    "ds_read_b64_tr_b16 v[134:135], v5 offset:59648\n\t"
    "v_rcp_f32 v70, v70\n\t"
    "v_mfma_f32_32x32x16_bf16 v[208:223], v[136:139], a[60:63], v[208:223]\n\t"
    "v_fma_f32 v69, -v163, v69, v163\n\t"
    "v_cvt_pk_bf16_f32 v35, v68, v69\n\t"
    "v_exp_f32 v66, v168\n\t"
    "v_fma_f32 v73, v73, s78, s78\n\t"
    "s_waitcnt vmcnt(9) lgkmcnt(2)\n\t"
    "v_mfma_f32_32x32x16_bf16 v[192:207], v[140:143], a[64:67], v[192:207]\n\t"
    "ds_read_b64_tr_b16 v[136:137], v5 offset:60416\n\t"
    "buffer_load_dwordx4 v3, s[44:47], s68 offen offset:1024 lds\n\t"
    "ds_read_b64_tr_b16 v[138:139], v5 offset:60672\n\t"
    "v_fma_f32 v72, v72, v72, 0.5\n\t"
    "v_mfma_f32_32x32x16_bf16 v[208:223], v[140:143], a[68:71], v[208:223]\n\t"
    "v_rcp_f32 v71, v71\n\t"
    "v_fma_f32 v70, -v164, v70, v164\n\t"
    "v_exp_f32 v67, v169\n\t"
    "v_fma_f32 v66, v66, s78, s78\n\t"
    "s_waitcnt vmcnt(9) lgkmcnt(2)\n\t"
    "v_mfma_f32_32x32x16_bf16 v[192:207], v[132:135], a[72:75], v[192:207]\n\t"
    "ds_read_b64_tr_b16 v[140:141], v5 offset:61440\n\t"
    "buffer_load_dwordx4 v4, s[44:47], s68 offen offset:2048 lds\n\t"
    "s_add_u32 s68, s68, 442368\n\t"
    "ds_read_b64_tr_b16 v[142:143], v5 offset:61696\n\t"
    "v_fma_f32 v73, v73, v73, 0.5\n\t"
    "v_mfma_f32_32x32x16_bf16 v[208:223], v[132:135], a[76:79], v[208:223]\n\t"
    "v_rcp_f32 v72, v72\n\t"
    "v_fma_f32 v71, -v165, v71, v165\n\t"
    "v_cvt_pk_bf16_f32 v36, v70, v71\n\t"
    "v_exp_f32 v68, v170\n\t"
    "s_waitcnt vmcnt(9) lgkmcnt(2)\n\t"
    "v_mfma_f32_32x32x16_bf16 v[192:207], v[136:139], a[80:83], v[192:207]\n\t"
    "s_add_u32 m0, s69, 59392\n\t"
    "ds_read_b64_tr_b16 v[132:133], v5 offset:50176\n\t"
    "buffer_load_dwordx4 v2, s[44:47], s68 offen offset:0 lds\n\t"
    "ds_read_b64_tr_b16 v[134:135], v5 offset:50432\n\t"
    "v_fma_f32 v67, v67, s78, s78\n\t"
    "v_fma_f32 v66, v66, v66, 0.5\n\t"
    "v_mfma_f32_32x32x16_bf16 v[208:223], v[136:139], a[84:87], v[208:223]\n\t"
    "v_rcp_f32 v73, v73\n\t"
    "v_fma_f32 v72, -v166, v72, v166\n\t"
    "v_exp_f32 v69, v171\n\t"
    "s_waitcnt vmcnt(9) lgkmcnt(2)\n\t"
    "v_mfma_f32_32x32x16_bf16 v[192:207], v[140:143], a[88:91], v[192:207]\n\t"
    "ds_read_b64_tr_b16 v[136:137], v5 offset:51200\n\t"
    "buffer_load_dwordx4 v3, s[44:47], s68 offen offset:1024 lds\n\t"
    "ds_read_b64_tr_b16 v[138:139], v5 offset:51456\n\t"
    "v_fma_f32 v68, v68, s78, s78\n\t"
    "v_fma_f32 v67, v67, v67, 0.5\n\t"
    "v_mfma_f32_32x32x16_bf16 v[208:223], v[140:143], a[92:95], v[208:223]\n\t"
    "v_rcp_f32 v66, v66\n\t"
    "v_fma_f32 v73, -v167, v73, v167\n\t"
    "v_cvt_pk_bf16_f32 v37, v72, v73\n\t"
    "s_waitcnt vmcnt(8) lgkmcnt(2)\n\t"
    "v_mfma_f32_32x32x16_bf16 v[192:207], v[132:135], a[96:99], v[192:207]\n\t"
    "ds_read_b64_tr_b16 v[140:141], v5 offset:52224\n\t"
    "buffer_load_dwordx4 v4, s[44:47], s68 offen offset:2048 lds\n\t"
    "s_add_u32 s68, s68, 442368\n\t"
    "ds_read_b64_tr_b16 v[142:143], v5 offset:52480\n\t"
    "v_exp_f32 v70, v172\n\t"
    "v_mfma_f32_32x32x16_bf16 v[208:223], v[132:135], a[100:103], v[208:223]\n\t"
    "v_fma_f32 v69, v69, s78, s78\n\t"
    "v_fma_f32 v68, v68, v68, 0.5\n\t"
    "v_rcp_f32 v67, v67\n\t"
    "v_fma_f32 v66, -v168, v66, v168\n\t"
    "s_waitcnt vmcnt(8) lgkmcnt(2)\n\t"
    "v_mfma_f32_32x32x16_bf16 v[192:207], v[136:139], a[104:107], v[192:207]\n\t"
    "s_add_u32 m0, s69, 50176\n\t"
    "ds_read_b64_tr_b16 v[132:133], v5 offset:53248\n\t"
    "buffer_load_dwordx4 v2, s[44:47], s68 offen offset:0 lds\n\t"
    "ds_read_b64_tr_b16 v[134:135], v5 offset:53504\n\t"
    "v_exp_f32 v71, v173\n\t"
    "v_mfma_f32_32x32x16_bf16 v[208:223], v[136:139], a[108:111], v[208:223]\n\t"
    "v_fma_f32 v70, v70, s78, s78\n\t"
    "v_fma_f32 v69, v69, v69, 0.5\n\t"
    "v_rcp_f32 v68, v68\n\t"
    "v_fma_f32 v67, -v169, v67, v169\n\t"
    "s_waitcnt vmcnt(8) lgkmcnt(2)\n\t"
    "v_mfma_f32_32x32x16_bf16 v[192:207], v[140:143], a[112:115], v[192:207]\n\t"
    "ds_read_b64_tr_b16 v[136:137], v5 offset:54272\n\t"
    "buffer_load_dwordx4 v3, s[44:47], s68 offen offset:1024 lds\n\t"
    "ds_read_b64_tr_b16 v[138:139], v5 offset:54528\n\t"
    "v_cvt_pk_bf16_f32 v38, v66, v67\n\t"
    "v_mfma_f32_32x32x16_bf16 v[208:223], v[140:143], a[116:119], v[208:223]\n\t"
    "v_exp_f32 v72, v174\n\t"
    "v_fma_f32 v71, v71, s78, s78\n\t"
    "v_fma_f32 v70, v70, v70, 0.5\n\t"
    "v_rcp_f32 v69, v69\n\t"
    "s_waitcnt vmcnt(8) lgkmcnt(2)\n\t"
    "v_mfma_f32_32x32x16_bf16 v[192:207], v[132:135], a[120:123], v[192:207]\n\t"
    "ds_read_b64_tr_b16 v[140:141], v5 offset:55296\n\t"
    "buffer_load_dwordx4 v4, s[44:47], s68 offen offset:2048 lds\n\t"
    "s_add_u32 s68, s68, 442368\n\t"
    "ds_read_b64_tr_b16 v[142:143], v5 offset:55552\n\t"
    "v_fma_f32 v68, -v170, v68, v170\n\t"
    "v_mfma_f32_32x32x16_bf16 v[208:223], v[132:135], a[124:127], v[208:223]\n\t"
    "v_exp_f32 v73, v175\n\t"
    "v_fma_f32 v72, v72, s78, s78\n\t"
    "v_fma_f32 v71, v71, v71, 0.5\n\t"
    "v_rcp_f32 v70, v70\n\t"
    "s_waitcnt vmcnt(8) lgkmcnt(2)\n\t"
    "v_mfma_f32_32x32x16_bf16 v[192:207], v[136:139], a[128:131], v[192:207]\n\t"
    "s_add_u32 m0, s69, 53248\n\t"
    "ds_read_b64_tr_b16 v[132:133], v5 offset:56320\n\t"
    "buffer_load_dwordx4 v2, s[44:47], s68 offen offset:0 lds\n\t"
    "ds_read_b64_tr_b16 v[134:135], v5 offset:56576\n\t"
    "v_fma_f32 v69, -v171, v69, v171\n\t"
    "v_cvt_pk_bf16_f32 v39, v68, v69\n\t"
    "v_mfma_f32_32x32x16_bf16 v[208:223], v[136:139], a[132:135], v[208:223]\n\t"
    "v_exp_f32 v66, v176\n\t"
    "v_fma_f32 v73, v73, s78, s78\n\t"
    "v_fma_f32 v72, v72, v72, 0.5\n\t"
    "s_waitcnt vmcnt(8) lgkmcnt(2)\n\t"
    "v_mfma_f32_32x32x16_bf16 v[192:207], v[140:143], a[136:139], v[192:207]\n\t"
    "ds_read_b64_tr_b16 v[136:137], v5 offset:57344\n\t"
    "buffer_load_dwordx4 v3, s[44:47], s68 offen offset:1024 lds\n\t"
    "ds_read_b64_tr_b16 v[138:139], v5 offset:57600\n\t"
    "v_rcp_f32 v71, v71\n\t"
    "v_fma_f32 v70, -v172, v70, v172\n\t"
    "v_mfma_f32_32x32x16_bf16 v[208:223], v[140:143], a[140:143], v[208:223]\n\t"
    "v_exp_f32 v67, v177\n\t"
    "v_fma_f32 v66, v66, s78, s78\n\t"
    "v_fma_f32 v73, v73, v73, 0.5\n\t"
    "s_waitcnt vmcnt(8) lgkmcnt(2)\n\t"
    "v_mfma_f32_32x32x16_bf16 v[192:207], v[132:135], a[144:147], v[192:207]\n\t"
    "ds_read_b64_tr_b16 v[140:141], v5 offset:58368\n\t"
    "buffer_load_dwordx4 v4, s[44:47], s68 offen offset:2048 lds\n\t"
    "s_add_u32 s68, s68, 442368\n\t"
    "ds_read_b64_tr_b16 v[142:143], v5 offset:58624\n\t"
    "v_rcp_f32 v72, v72\n\t"
    "v_fma_f32 v71, -v173, v71, v173\n\t"
    "v_mfma_f32_32x32x16_bf16 v[208:223], v[132:135], a[148:151], v[208:223]\n\t"
    "v_cvt_pk_bf16_f32 v40, v70, v71\n\t"
    "v_exp_f32 v68, v178\n\t"
    "v_fma_f32 v67, v67, s78, s78\n\t"
    "s_waitcnt vmcnt(8) lgkmcnt(2)\n\t"
    "v_mfma_f32_32x32x16_bf16 v[192:207], v[136:139], a[152:155], v[192:207]\n\t"
    "s_add_u32 m0, s69, 56320\n\t"
    "ds_read_b64_tr_b16 v[132:133], v5 offset:59392\n\t"
    "buffer_load_dwordx4 v2, s[44:47], s68 offen offset:0 lds\n\t"
    "ds_read_b64_tr_b16 v[134:135], v5 offset:59648\n\t"
    "v_fma_f32 v66, v66, v66, 0.5\n\t"
    "v_mfma_f32_32x32x16_bf16 v[208:223], v[136:139], a[156:159], v[208:223]\n\t"
    "v_rcp_f32 v73, v73\n\t"
    "v_fma_f32 v72, -v174, v72, v174\n\t"
    "v_exp_f32 v69, v179\n\t"
    "v_fma_f32 v68, v68, s78, s78\n\t"
    "s_waitcnt vmcnt(8) lgkmcnt(2)\n\t"
    "v_mfma_f32_32x32x16_bf16 v[192:207], v[140:143], a[160:163], v[192:207]\n\t"
    "ds_read_b64_tr_b16 v[136:137], v5 offset:60416\n\t"
    "buffer_load_dwordx4 v3, s[44:47], s68 offen offset:1024 lds\n\t"
    "ds_read_b64_tr_b16 v[138:139], v5 offset:60672\n\t"
    "v_fma_f32 v67, v67, v67, 0.5\n\t"
    "v_mfma_f32_32x32x16_bf16 v[208:223], v[140:143], a[164:167], v[208:223]\n\t"
    "v_rcp_f32 v66, v66\n\t"
    "v_fma_f32 v73, -v175, v73, v175\n\t"
    "v_cvt_pk_bf16_f32 v41, v72, v73\n\t"
    "v_exp_f32 v70, v180\n\t"
    "v_mov_b32 v16, 145672\n\t"
    "s_cmp_lg_u32 s64, 0\n\t"
    "s_cbranch_scc1 L_nopoll_4%=\n\t"
    "v_and_b32 v15, 31, v1\n\t"
    "v_lshlrev_b32 v15, 2, v15\n\t"
    "s_lshl_b32 s71, s66, 7\n\t"
    "s_add_u32 s71, s71, 256\n\t"
    "v_cmp_ne_u32 vcc, s67, v11\n\t"
    "s_mov_b32 s94, 0x40000\n\t"
    "s_cbranch_vccz L_ok_5%=\n\t"
    "L_spin_6%=:\n\t"
    "s_sleep 1\n\t"
    "buffer_load_dword v11, v15, s[40:43], s71 offen sc1\n\t"
    "s_waitcnt vmcnt(0)\n\t"
    "v_cmp_ne_u32 vcc, s67, v11\n\t"
    "s_sub_u32 s94, s94, 1\n\t"
    "s_nop 3\n\t"
    "s_cbranch_vccz L_ok_5%=\n\t"
    "s_cmp_lg_u32 s94, 0\n\t"
    "s_cbranch_scc1 L_spin_6%=\n\t"
    "L_ok_5%=:\n\t"
    "v_mov_b32 v15, 1\n\t"
    "ds_write_b32 v16, v15\n\t"
    "L_nopoll_4%=:\n\t"
    "s_mov_b32 s94, 0x400000\n\t"
    "L_ldsspin_7%=:\n\t"
    "ds_read_b32 v15, v16\n\t"
    "s_waitcnt lgkmcnt(0)\n\t"
    "v_cmp_ne_u32 vcc, 1, v15\n\t"
    "s_sub_u32 s94, s94, 1\n\t"
    "s_nop 3\n\t"
    "s_cbranch_vccz L_ldsok_8%=\n\t"
    "s_cmp_lg_u32 s94, 0\n\t"
    "s_cbranch_scc1 L_ldsspin_7%=\n\t"
    "L_ldsok_8%=:\n\t"
    "s_lshl_b32 s70, s66, 5\n\t"
    "s_add_u32 s70, s70, s65\n\t"
    "s_mul_i32 s70, s70, 12288\n\t"
    "s_add_u32 s70, s70, 129792\n\t"
    "buffer_load_dwordx4 v[18:21], v8, s[40:43], s70 offen sc1\n\t"
    "s_add_u32 s70, s70, 4096\n\t"
    "buffer_load_dwordx4 v[22:25], v8, s[40:43], s70 offen sc1\n\t"
    "s_add_u32 s70, s70, 4096\n\t"
    "buffer_load_dwordx4 v[26:29], v8, s[40:43], s70 offen sc1\n\t"
    "s_waitcnt vmcnt(11)\n\t"
    "v_mfma_f32_32x32x16_bf16 v[192:207], v[132:135], a[168:171], v[192:207]\n\t"
    "ds_read_b64_tr_b16 v[140:141], v5 offset:61440\n\t"
    "buffer_load_dwordx4 v4, s[44:47], s68 offen offset:2048 lds\n\t"
    "s_add_u32 s68, s68, 442368\n\t"
    "ds_read_b64_tr_b16 v[142:143], v5 offset:61696\n\t"
    "v_fma_f32 v69, v69, s78, s78\n\t"
    "v_fma_f32 v68, v68, v68, 0.5\n\t"
    "v_mfma_f32_32x32x16_bf16 v[208:223], v[132:135], a[172:175], v[208:223]\n\t"
    "v_rcp_f32 v67, v67\n\t"
    "v_fma_f32 v66, -v176, v66, v176\n\t"
    "v_exp_f32 v71, v181\n\t"
    "s_waitcnt vmcnt(11)\n\t"
    "v_mfma_f32_32x32x16_bf16 v[192:207], v[136:139], a[176:179], v[192:207]\n\t"
    "s_add_u32 m0, s69, 59392\n\t"
    "ds_read_b64_tr_b16 v[132:133], v5 offset:50176\n\t"
    "buffer_load_dwordx4 v2, s[44:47], s68 offen offset:0 lds\n\t"
    "ds_read_b64_tr_b16 v[134:135], v5 offset:50432\n\t"
    "v_fma_f32 v70, v70, s78, s78\n\t"
    "v_fma_f32 v69, v69, v69, 0.5\n\t"
    "v_mfma_f32_32x32x16_bf16 v[208:223], v[136:139], a[180:183], v[208:223]\n\t"
    "v_rcp_f32 v68, v68\n\t"
    "v_fma_f32 v67, -v177, v67, v177\n\t"
    "v_cvt_pk_bf16_f32 v42, v66, v67\n\t"
    "s_waitcnt vmcnt(11) lgkmcnt(2)\n\t"
    "v_mfma_f32_32x32x16_bf16 v[192:207], v[140:143], a[184:187], v[192:207]\n\t"
    "ds_read_b64_tr_b16 v[136:137], v5 offset:51200\n\t"
    "buffer_load_dwordx4 v3, s[44:47], s68 offen offset:1024 lds\n\t"
    "ds_read_b64_tr_b16 v[138:139], v5 offset:51456\n\t"
    "v_exp_f32 v72, v182\n\t"
    "v_fma_f32 v71, v71, s78, s78\n\t"
    "v_mfma_f32_32x32x16_bf16 v[208:223], v[140:143], a[188:191], v[208:223]\n\t"
    "v_fma_f32 v70, v70, v70, 0.5\n\t"
    "v_rcp_f32 v69, v69\n\t"
    "v_fma_f32 v68, -v178, v68, v178\n\t"
    "s_waitcnt vmcnt(11) lgkmcnt(2)\n\t"
    "v_mfma_f32_32x32x16_bf16 v[192:207], v[132:135], a[192:195], v[192:207]\n\t"
    "ds_read_b64_tr_b16 v[140:141], v5 offset:52224\n\t"
    "buffer_load_dwordx4 v4, s[44:47], s68 offen offset:2048 lds\n\t"
    "s_mov_b32 s68, s85\n\t"
    "ds_read_b64_tr_b16 v[142:143], v5 offset:52480\n\t"
    "v_exp_f32 v73, v183\n\t"
    "v_fma_f32 v72, v72, s78, s78\n\t"
    "v_mfma_f32_32x32x16_bf16 v[208:223], v[132:135], a[196:199], v[208:223]\n\t"
    "v_fma_f32 v71, v71, v71, 0.5\n\t"
    "v_rcp_f32 v70, v70\n\t"
    "v_fma_f32 v69, -v179, v69, v179\n\t"
    "v_cvt_pk_bf16_f32 v43, v68, v69\n\t"
    "s_waitcnt vmcnt(11) lgkmcnt(2)\n\t"
    "v_mfma_f32_32x32x16_bf16 v[192:207], v[136:139], a[200:203], v[192:207]\n\t"
    "s_add_u32 m0, s69, 50176\n\t"
    "ds_read_b64_tr_b16 v[132:133], v5 offset:53248\n\t"
    "buffer_load_dwordx4 v2, s[44:47], s68 offen offset:0 lds\n\t"
    "ds_read_b64_tr_b16 v[134:135], v5 offset:53504\n\t"
    "v_exp_f32 v66, v184\n\t"
    "v_mfma_f32_32x32x16_bf16 v[208:223], v[136:139], a[204:207], v[208:223]\n\t"
    "v_fma_f32 v73, v73, s78, s78\n\t"
    "v_fma_f32 v72, v72, v72, 0.5\n\t"
    "v_rcp_f32 v71, v71\n\t"
    "s_waitcnt vmcnt(11) lgkmcnt(2)\n\t"
    "v_mfma_f32_32x32x16_bf16 v[192:207], v[140:143], a[208:211], v[192:207]\n\t"
    "ds_read_b64_tr_b16 v[136:137], v5 offset:54272\n\t"
    "buffer_load_dwordx4 v3, s[44:47], s68 offen offset:1024 lds\n\t"
    "ds_read_b64_tr_b16 v[138:139], v5 offset:54528\n\t"
    "v_fma_f32 v70, -v180, v70, v180\n\t"
    "v_mfma_f32_32x32x16_bf16 v[208:223], v[140:143], a[212:215], v[208:223]\n\t"
    "v_exp_f32 v67, v185\n\t"
    "v_fma_f32 v66, v66, s78, s78\n\t"
    "v_fma_f32 v73, v73, v73, 0.5\n\t"
    "v_rcp_f32 v72, v72\n\t"
    "s_waitcnt vmcnt(11) lgkmcnt(2)\n\t"
    "v_mfma_f32_32x32x16_bf16 v[192:207], v[132:135], a[216:219], v[192:207]\n\t"
    "ds_read_b64_tr_b16 v[140:141], v5 offset:55296\n\t"
    "buffer_load_dwordx4 v4, s[44:47], s68 offen offset:2048 lds\n\t"
    "s_add_u32 s68, s68, 442368\n\t"
    "ds_read_b64_tr_b16 v[142:143], v5 offset:55552\n\t"
    "v_fma_f32 v71, -v181, v71, v181\n\t"
    "v_cvt_pk_bf16_f32 v44, v70, v71\n\t"
    "v_mfma_f32_32x32x16_bf16 v[208:223], v[132:135], a[220:223], v[208:223]\n\t"
    "v_exp_f32 v68, v186\n\t"
    "v_fma_f32 v67, v67, s78, s78\n\t"
    "v_fma_f32 v66, v66, v66, 0.5\n\t"
    "s_waitcnt vmcnt(11) lgkmcnt(2)\n\t"
    "v_mfma_f32_32x32x16_bf16 v[192:207], v[136:139], a[224:227], v[192:207]\n\t"
    "s_add_u32 m0, s69, 53248\n\t"
    "ds_read_b64_tr_b16 v[132:133], v5 offset:56320\n\t"
    "buffer_load_dwordx4 v2, s[44:47], s68 offen offset:0 lds\n\t"
    "ds_read_b64_tr_b16 v[134:135], v5 offset:56576\n\t"
    "v_rcp_f32 v73, v73\n\t"
    "v_fma_f32 v72, -v182, v72, v182\n\t"
    "v_mfma_f32_32x32x16_bf16 v[208:223], v[136:139], a[228:231], v[208:223]\n\t"
    "v_exp_f32 v69, v187\n\t"
    "v_fma_f32 v68, v68, s78, s78\n\t"
    "v_fma_f32 v67, v67, v67, 0.5\n\t"
    "s_waitcnt vmcnt(11) lgkmcnt(2)\n\t"
    "v_mfma_f32_32x32x16_bf16 v[192:207], v[140:143], a[232:235], v[192:207]\n\t"
    "ds_read_b64_tr_b16 v[136:137], v5 offset:57344\n\t"
    "buffer_load_dwordx4 v3, s[44:47], s68 offen offset:1024 lds\n\t"
    "ds_read_b64_tr_b16 v[138:139], v5 offset:57600\n\t"
    "v_rcp_f32 v66, v66\n\t"
    "v_fma_f32 v73, -v183, v73, v183\n\t"
    "v_mfma_f32_32x32x16_bf16 v[208:223], v[140:143], a[236:239], v[208:223]\n\t"
    "v_cvt_pk_bf16_f32 v45, v72, v73\n\t"
    "v_exp_f32 v70, v188\n\t"
    "v_fma_f32 v69, v69, s78, s78\n\t"
    "v_fma_f32 v68, v68, v68, 0.5\n\t"
    "s_waitcnt vmcnt(8) lgkmcnt(2)\n\t"
    "v_mfma_f32_32x32x16_bf16 v[192:207], v[132:135], a[240:243], v[192:207]\n\t"
    "ds_read_b64_tr_b16 v[140:141], v5 offset:58368\n\t"
    "buffer_load_dwordx4 v4, s[44:47], s68 offen offset:2048 lds\n\t"
    "s_add_u32 s68, s68, 442368\n\t"
    "ds_read_b64_tr_b16 v[142:143], v5 offset:58624\n\t"
    "v_rcp_f32 v67, v67\n\t"
    "v_mfma_f32_32x32x16_bf16 v[208:223], v[132:135], a[244:247], v[208:223]\n\t"
    "v_fma_f32 v66, -v184, v66, v184\n\t"
    "v_exp_f32 v71, v189\n\t"
    "v_fma_f32 v70, v70, s78, s78\n\t"
    "v_fma_f32 v69, v69, v69, 0.5\n\t"
    "s_waitcnt vmcnt(8) lgkmcnt(2)\n\t"
    "v_mfma_f32_32x32x16_bf16 v[192:207], v[136:139], a[248:251], v[192:207]\n\t"
    "s_add_u32 m0, s69, 56320\n\t"
    "ds_read_b64_tr_b16 v[132:133], v5 offset:59392\n\t"
    "buffer_load_dwordx4 v2, s[44:47], s68 offen offset:0 lds\n\t"
    "ds_read_b64_tr_b16 v[134:135], v5 offset:59648\n\t"
    "v_rcp_f32 v68, v68\n\t"
    "v_mfma_f32_32x32x16_bf16 v[208:223], v[136:139], a[252:255], v[208:223]\n\t"
    "v_fma_f32 v67, -v185, v67, v185\n\t"
    "v_cvt_pk_bf16_f32 v46, v66, v67\n\t"
    "v_exp_f32 v72, v190\n\t"
    "v_fma_f32 v71, v71, s78, s78\n\t"
    "s_waitcnt vmcnt(8) lgkmcnt(2)\n\t"
    "v_mfma_f32_32x32x16_bf16 v[192:207], v[140:143], v[224:227], v[192:207]\n\t"
    "ds_read_b64_tr_b16 v[136:137], v5 offset:60416\n\t"
    "buffer_load_dwordx4 v3, s[44:47], s68 offen offset:1024 lds\n\t"
    "ds_read_b64_tr_b16 v[138:139], v5 offset:60672\n\t"
    "v_fma_f32 v70, v70, v70, 0.5\n\t"
    "v_mfma_f32_32x32x16_bf16 v[208:223], v[140:143], v[228:231], v[208:223]\n\t"
    "v_rcp_f32 v69, v69\n\t"
    "v_fma_f32 v68, -v186, v68, v186\n\t"
    "v_exp_f32 v73, v191\n\t"
    "s_waitcnt vmcnt(8) lgkmcnt(2)\n\t"
    "v_mfma_f32_32x32x16_bf16 v[192:207], v[132:135], v[232:235], v[192:207]\n\t"
    "ds_read_b64_tr_b16 v[140:141], v5 offset:61440\n\t"
    "buffer_load_dwordx4 v4, s[44:47], s68 offen offset:2048 lds\n\t"
    "s_add_u32 s68, s68, 442368\n\t"
    "ds_read_b64_tr_b16 v[142:143], v5 offset:61696\n\t"
    "v_fma_f32 v72, v72, s78, s78\n\t"
    "v_fma_f32 v71, v71, v71, 0.5\n\t"
    "v_mfma_f32_32x32x16_bf16 v[208:223], v[132:135], v[236:239], v[208:223]\n\t"
    "v_rcp_f32 v70, v70\n\t"
    "v_fma_f32 v69, -v187, v69, v187\n\t"
    "v_cvt_pk_bf16_f32 v47, v68, v69\n\t"
    "v_fma_f32 v73, v73, s78, s78\n\t"
    "s_waitcnt lgkmcnt(2)\n\t"
    "v_mfma_f32_32x32x16_bf16 v[192:207], v[136:139], v[240:243], v[192:207]\n\t"
    "s_add_u32 m0, s69, 59392\n\t"
    "s_nop 0\n\t"
    "buffer_load_dwordx4 v2, s[44:47], s68 offen offset:0 lds\n\t"
    "v_fma_f32 v72, v72, v72, 0.5\n\t"
    "v_mfma_f32_32x32x16_bf16 v[208:223], v[136:139], v[244:247], v[208:223]\n\t"
    "v_rcp_f32 v71, v71\n\t"
    "v_fma_f32 v70, -v188, v70, v188\n\t"
    "v_fma_f32 v73, v73, v73, 0.5\n\t"
    "v_rcp_f32 v72, v72\n\t"
    "s_waitcnt lgkmcnt(0)\n\t"
    "v_mfma_f32_32x32x16_bf16 v[192:207], v[140:143], v[248:251], v[192:207]\n\t"
    "buffer_load_dwordx4 v3, s[44:47], s68 offen offset:1024 lds\n\t"
    "v_fma_f32 v71, -v189, v71, v189\n\t"
    "v_cvt_pk_bf16_f32 v48, v70, v71\n\t"
    "v_mfma_f32_32x32x16_bf16 v[208:223], v[140:143], v[252:255], v[208:223]\n\t"
    "v_rcp_f32 v73, v73\n\t"
    "v_fma_f32 v72, -v190, v72, v190\n\t"
    "v_fma_f32 v73, -v191, v73, v191\n\t"
    "v_cvt_pk_bf16_f32 v49, v72, v73\n\t"
    "ds_write_b128 v8, v[18:21] offset:0\n\t"
    "ds_write_b128 v8, v[22:25] offset:4096\n\t"
    "ds_write_b128 v8, v[26:29] offset:8192\n\t"
    "v_lshrrev_b32 v13, 5, v1\n\t"
    "v_and_b32 v15, 31, v1\n\t"
    "v_lshlrev_b32 v15, 6, v15\n\t"
    "v_add_u32 v15, 126208, v15\n\t"
    "v_lshl_add_u32 v16, v13, 4, v15\n\t"
    "v_lshl_add_u32 v15, v13, 3, v15\n\t"
    "ds_read_b128 v[92:95], v16 offset:0\n\t"
    "ds_read_b128 v[100:103], v16 offset:2048\n\t"
    "ds_read_b128 v[108:111], v16 offset:4096\n\t"
    "ds_read_b128 v[96:99], v16 offset:32\n\t"
    "ds_read_b128 v[104:107], v16 offset:2080\n\t"
    "ds_read_b128 v[112:115], v16 offset:4128\n\t"
    "ds_read_b64 v[144:145], v15 offset:0\n\t"
    "ds_read_b64 v[146:147], v15 offset:16\n\t"
    "ds_read_b64 v[152:153], v15 offset:2048\n\t"
    "ds_read_b64 v[154:155], v15 offset:2064\n\t"
    "ds_read_b64 v[132:133], v15 offset:4096\n\t"
    "ds_read_b64 v[134:135], v15 offset:4112\n\t"
    "ds_read_b64 v[148:149], v15 offset:32\n\t"
    "ds_read_b64 v[150:151], v15 offset:48\n\t"
    "ds_read_b64 v[156:157], v15 offset:2080\n\t"
    "ds_read_b64 v[158:159], v15 offset:2096\n\t"
    "ds_read_b64 v[136:137], v15 offset:4128\n\t"
    "ds_read_b64 v[138:139], v15 offset:4144\n\t"
    "s_lshl_b32 s70, s64, 5\n\t"
    "v_and_b32 v15, 31, v1\n\t"
    "v_add_u32 v15, s70, v15\n\t"
    "v_cmp_lt_u32 vcc, 47, v15\n\t"
    "v_subrev_u32 v16, 48, v15\n\t"
    "s_nop 0\n\t"
    "v_cndmask_b32 v16, v15, v16, vcc\n\t"
    "s_mul_i32 s71, s66, 36\n\t"
    "v_add_u32 v14, s71, v16\n\t"
    "v_mov_b32 v17, 0x3600\n\t"
    "v_mov_b32 v66, 0x3580\n\t"
    "v_cmp_eq_u32 vcc, 0, v14\n\t"
    "s_nop 1\n\t"
    "v_cndmask_b32 v17, v17, v66, vcc\n\t"
    "v_cmp_eq_u32 vcc, 288, v14\n\t"
    "s_nop 1\n\t"
    "v_cndmask_b32 v17, v17, v66, vcc\n\t"
    "s_cmp_eq_u32 s66, 7\n\t"
    "s_cselect_b32 s71, 37, 36\n\t"
    "v_cmp_gt_u32 vcc, s71, v16\n\t"
    "s_nop 1\n\t"
    "v_cndmask_b32 v17, 0, v17, vcc\n\t"
    "v_cmp_gt_u32 vcc, 96, v15\n\t"
    "s_nop 1\n\t"
    "v_cndmask_b32 v17, 0, v17, vcc\n\t"
    "v_lshlrev_b32 v16, 16, v17\n\t"
    "v_dot2_f32_bf16 v116, v50, v17, 0\n\t"
    "v_dot2_f32_bf16 v76, v50, v16, 0\n\t"
    "v_dot2_f32_bf16 v117, v51, v17, 0\n\t"
    "v_dot2_f32_bf16 v77, v51, v16, 0\n\t"
    "v_dot2_f32_bf16 v118, v52, v17, 0\n\t"
    "v_dot2_f32_bf16 v78, v52, v16, 0\n\t"
    "v_dot2_f32_bf16 v119, v53, v17, 0\n\t"
    "v_dot2_f32_bf16 v79, v53, v16, 0\n\t"
    "v_dot2_f32_bf16 v120, v54, v17, 0\n\t"
    "v_dot2_f32_bf16 v80, v54, v16, 0\n\t"
    "v_dot2_f32_bf16 v121, v55, v17, 0\n\t"
    "v_dot2_f32_bf16 v81, v55, v16, 0\n\t"
    "v_dot2_f32_bf16 v122, v56, v17, 0\n\t"
    "v_dot2_f32_bf16 v82, v56, v16, 0\n\t"
    "v_dot2_f32_bf16 v123, v57, v17, 0\n\t"
    "v_dot2_f32_bf16 v83, v57, v16, 0\n\t"
    "v_dot2_f32_bf16 v124, v58, v17, 0\n\t"
    "v_dot2_f32_bf16 v84, v58, v16, 0\n\t"
    "v_dot2_f32_bf16 v125, v59, v17, 0\n\t"
    "v_dot2_f32_bf16 v85, v59, v16, 0\n\t"
    "v_dot2_f32_bf16 v126, v60, v17, 0\n\t"
    "v_dot2_f32_bf16 v86, v60, v16, 0\n\t"
    "v_dot2_f32_bf16 v127, v61, v17, 0\n\t"
    "v_dot2_f32_bf16 v87, v61, v16, 0\n\t"
    "v_dot2_f32_bf16 v128, v62, v17, 0\n\t"
    "v_dot2_f32_bf16 v88, v62, v16, 0\n\t"
    "v_dot2_f32_bf16 v129, v63, v17, 0\n\t"
    "v_dot2_f32_bf16 v89, v63, v16, 0\n\t"
    "v_dot2_f32_bf16 v130, v64, v17, 0\n\t"
    "v_dot2_f32_bf16 v90, v64, v16, 0\n\t"
    "v_dot2_f32_bf16 v131, v65, v17, 0\n\t"
    "v_dot2_f32_bf16 v91, v65, v16, 0\n\t"
    "v_bfe_u32 v14, v1, 2, 2\n\t"
    "v_lshl_add_u32 v14, v13, 3, v14\n\t"
    "v_mul_u32_u24 v14, 192, v14\n\t"
    "v_bfe_u32 v15, v1, 4, 1\n\t"
    "v_lshl_add_u32 v14, v15, 5, v14\n\t"
    "v_and_b32 v15, 3, v1\n\t"
    "v_lshl_add_u32 v14, v15, 3, v14\n\t"
    "s_lshl_b32 s70, s64, 6\n\t"
    "s_add_u32 s70, s70, 132352\n\t"
    "v_add_u32 v14, s70, v14\n\t"
    "s_waitcnt lgkmcnt(0)\n\t"
    "s_barrier\n\t"
    "s_cmp_lg_u32 s64, 3\n\t"
    "s_cbranch_scc1 L_nowarm_9%=\n\t"
    "v_mov_b32 v73, v14\n\t"
    "s_mov_b32 m0, 145728\n\t"
    "s_and_b32 s72, s65, 15\n\t"
    "v_mov_b32 v16, v1\n\t"
    "s_mul_i32 s70, s72, 36\n\t"
    "s_mul_i32 s71, s66, 1152\n\t"
    "s_mul_i32 s70, s66, 576\n\t"
    "s_mul_i32 s71, s72, 36\n\t"
    "s_add_u32 s70, s70, s71\n\t"
    "s_mul_i32 s70, s70, 1152\n\t"
    "v_add_u32 v13, 0, v16\n\t"
    "v_lshlrev_b32 v14, 6, v13\n\t"
    "v_add_u32 v14, s70, v14\n\t"
    "buffer_load_dword v14, s[48:51], 0 offen lds\n\t"
    "v_add_u32 v13, 64, v16\n\t"
    "v_lshlrev_b32 v14, 6, v13\n\t"
    "v_add_u32 v14, s70, v14\n\t"
    "buffer_load_dword v14, s[48:51], 0 offen lds\n\t"
    "v_add_u32 v13, 128, v16\n\t"
    "v_lshlrev_b32 v14, 6, v13\n\t"
    "v_add_u32 v14, s70, v14\n\t"
    "buffer_load_dword v14, s[48:51], 0 offen lds\n\t"
    "v_add_u32 v13, 192, v16\n\t"
    "v_lshlrev_b32 v14, 6, v13\n\t"
    "v_add_u32 v14, s70, v14\n\t"
    "buffer_load_dword v14, s[48:51], 0 offen lds\n\t"
    "v_add_u32 v13, 256, v16\n\t"
    "v_lshlrev_b32 v14, 6, v13\n\t"
    "v_add_u32 v14, s70, v14\n\t"
    "buffer_load_dword v14, s[48:51], 0 offen lds\n\t"
    "v_add_u32 v13, 320, v16\n\t"
    "v_lshlrev_b32 v14, 6, v13\n\t"
    "v_add_u32 v14, s70, v14\n\t"
    "buffer_load_dword v14, s[48:51], 0 offen lds\n\t"
    "v_add_u32 v13, 384, v16\n\t"
    "v_lshlrev_b32 v14, 6, v13\n\t"
    "v_add_u32 v14, s70, v14\n\t"
    "buffer_load_dword v14, s[48:51], 0 offen lds\n\t"
    "v_add_u32 v13, 448, v16\n\t"
    "v_lshlrev_b32 v14, 6, v13\n\t"
    "v_add_u32 v14, s70, v14\n\t"
    "buffer_load_dword v14, s[48:51], 0 offen lds\n\t"
    "v_add_u32 v13, 512, v16\n\t"
    "v_lshlrev_b32 v14, 6, v13\n\t"
    "v_add_u32 v14, s70, v14\n\t"
    "buffer_load_dword v14, s[48:51], 0 offen lds\n\t"
    "v_add_u32 v13, 576, v16\n\t"
    "v_lshlrev_b32 v14, 6, v13\n\t"
    "v_add_u32 v14, s70, v14\n\t"
    "buffer_load_dword v14, s[48:51], 0 offen lds\n\t"
    "v_add_u32 v13, 640, v16\n\t"
    "v_lshlrev_b32 v14, 6, v13\n\t"
    "v_add_u32 v14, s70, v14\n\t"
    "v_cmp_gt_u32 vcc, 648, v13\n\t"
    "v_mov_b32 v15, 2146435072\n\t"
    "s_nop 0\n\t"
    "v_cndmask_b32 v14, v15, v14, vcc\n\t"
    "buffer_load_dword v14, s[48:51], 0 offen lds\n\t"
    "v_mov_b32 v14, v73\n\t"
    "v_lshrrev_b32 v13, 5, v1\n\t"
    "L_nowarm_9%=:\n\t"
    "ds_read_b64_tr_b16 v[18:19], v14 offset:0\n\t"
    "ds_read_b64_tr_b16 v[20:21], v14 offset:768\n\t"
    "ds_read_b64_tr_b16 v[26:27], v14 offset:6144\n\t"
    "ds_read_b64_tr_b16 v[28:29], v14 offset:6912\n\t"
    "ds_read_b64_tr_b16 v[22:23], v14 offset:3072\n\t"
    "ds_read_b64_tr_b16 v[24:25], v14 offset:3840\n\t"
    "ds_read_b64_tr_b16 v[30:31], v14 offset:9216\n\t"
    "ds_read_b64_tr_b16 v[32:33], v14 offset:9984\n\t"
    "v_and_b32 v15, 31, v1\n\t"
    "v_mul_u32_u24 v15, 384, v15\n\t"
    "v_lshl_add_u32 v15, v13, 3, v15\n\t"
    "s_lshl_b32 s71, s64, 6\n\t"
    "s_add_u32 s71, s71, 151552\n\t"
    "v_add_u32 v70, s71, v15\n\t"
    "s_cmp_eq_u32 s64, 3\n\t"
    "s_cselect_b64 vcc, -1, 0\n\t"
    "v_mov_b32 v17, 145728\n\t"
    "v_cndmask_b32 v70, v70, v17, vcc\n\t"
    "s_waitcnt lgkmcnt(6)\n\t"
    "v_mfma_f32_32x32x16_bf16 v[160:175], v[92:95], v[18:21], 0\n\t"
    "v_mfma_f32_32x32x16_bf16 v[176:191], v[108:111], v[18:21], 0\n\t"
    "s_waitcnt lgkmcnt(4)\n\t"
    "v_mfma_f32_32x32x16_bf16 v[160:175], v[100:103], v[26:29], v[160:175]\n\t"
    "v_mfma_f32_32x32x16_bf16 v[176:191], v[92:95], v[26:29], v[176:191]\n\t"
    "s_waitcnt lgkmcnt(2)\n\t"
    "v_mfma_f32_32x32x16_bf16 v[160:175], v[96:99], v[22:25], v[160:175]\n\t"
    "v_mfma_f32_32x32x16_bf16 v[176:191], v[112:115], v[22:25], v[176:191]\n\t"
    "s_waitcnt lgkmcnt(0)\n\t"
    "v_mfma_f32_32x32x16_bf16 v[160:175], v[104:107], v[30:33], v[160:175]\n\t"
    "v_mfma_f32_32x32x16_bf16 v[176:191], v[96:99], v[30:33], v[176:191]\n\t"
    "s_lshl_b32 s70, s64, 4\n\t"
    "v_and_b32 v114, 15, v1\n\t"
    "v_add_u32 v114, s70, v114\n\t"
    "v_lshlrev_b32 v114, 5, v114\n\t"
    "v_add_u32 v114, s65, v114\n\t"
    "v_cvt_f32_u32 v109, v114\n\t"
    "v_mul_f32 v109, 0x3a000000, v109\n\t"
    "v_lshrrev_b32 v114, 4, v1\n\t"
    "v_lshlrev_b32 v114, 3, v114\n\t"
    "v_add_u32 v114, 0, v114\n\t"
    "v_cvt_f32_u32 v108, v114\n\t"
    "v_mul_f32 v108, v108, v109\n\t"
    "v_cos_f32 v110, v108\n\t"
    "v_sin_f32 v112, v108\n\t"
    "v_add_f32 v108, v108, v109\n\t"
    "v_cos_f32 v111, v108\n\t"
    "v_sin_f32 v113, v108\n\t"
    "v_add_f32 v108, v108, v109\n\t"
    "v_cvt_pk_bf16_f32 v50, v110, v111\n\t"
    "v_cvt_pk_bf16_f32 v58, v112, v113\n\t"
    "v_cos_f32 v110, v108\n\t"
    "v_sin_f32 v112, v108\n\t"
    "v_add_f32 v108, v108, v109\n\t"
    "v_cos_f32 v111, v108\n\t"
    "v_sin_f32 v113, v108\n\t"
    "v_add_f32 v108, v108, v109\n\t"
    "v_cvt_pk_bf16_f32 v51, v110, v111\n\t"
    "v_cvt_pk_bf16_f32 v59, v112, v113\n\t"
    "v_cos_f32 v110, v108\n\t"
    "v_sin_f32 v112, v108\n\t"
    "v_add_f32 v108, v108, v109\n\t"
    "v_cos_f32 v111, v108\n\t"
    "v_sin_f32 v113, v108\n\t"
    "v_add_f32 v108, v108, v109\n\t"
    "v_cvt_pk_bf16_f32 v52, v110, v111\n\t"
    "v_cvt_pk_bf16_f32 v60, v112, v113\n\t"
    "v_cos_f32 v110, v108\n\t"
    "v_sin_f32 v112, v108\n\t"
    "v_add_f32 v108, v108, v109\n\t"
    "v_cos_f32 v111, v108\n\t"
    "v_sin_f32 v113, v108\n\t"
    "v_cvt_pk_bf16_f32 v53, v110, v111\n\t"
    "v_cvt_pk_bf16_f32 v61, v112, v113\n\t"
    "v_mul_f32 v92, v160, v116\n\t"
    "v_mul_f32 v94, v160, v76\n\t"
    "v_mul_f32 v93, v161, v117\n\t"
    "v_mul_f32 v95, v161, v77\n\t"
    "v_fma_f32 v92, -v176, v76, v92\n\t"
    "v_fma_f32 v94, v176, v116, v94\n\t"
    "v_fma_f32 v93, -v177, v77, v93\n\t"
    "v_fma_f32 v95, v177, v117, v95\n\t"
    "v_cvt_pk_bf16_f32 v18, v92, v93\n\t"
    "v_cvt_pk_bf16_f32 v26, v94, v95\n\t"
    "v_mul_f32 v96, v162, v118\n\t"
    "v_mul_f32 v98, v162, v78\n\t"
    "v_mul_f32 v97, v163, v119\n\t"
    "v_mul_f32 v99, v163, v79\n\t"
    "v_fma_f32 v96, -v178, v78, v96\n\t"
    "v_fma_f32 v98, v178, v118, v98\n\t"
    "v_fma_f32 v97, -v179, v79, v97\n\t"
    "v_fma_f32 v99, v179, v119, v99\n\t"
    "v_cvt_pk_bf16_f32 v19, v96, v97\n\t"
    "v_cvt_pk_bf16_f32 v27, v98, v99\n\t"
    "v_mul_f32 v92, v164, v120\n\t"
    "v_mul_f32 v94, v164, v80\n\t"
    "v_mul_f32 v93, v165, v121\n\t"
    "v_mul_f32 v95, v165, v81\n\t"
    "v_fma_f32 v92, -v180, v80, v92\n\t"
    "v_fma_f32 v94, v180, v120, v94\n\t"
    "v_fma_f32 v93, -v181, v81, v93\n\t"
    "v_fma_f32 v95, v181, v121, v95\n\t"
    "v_cvt_pk_bf16_f32 v20, v92, v93\n\t"
    "v_cvt_pk_bf16_f32 v28, v94, v95\n\t"
    "v_mul_f32 v96, v166, v122\n\t"
    "v_mul_f32 v98, v166, v82\n\t"
    "v_mul_f32 v97, v167, v123\n\t"
    "v_mul_f32 v99, v167, v83\n\t"
    "v_fma_f32 v96, -v182, v82, v96\n\t"
    "v_fma_f32 v98, v182, v122, v98\n\t"
    "v_fma_f32 v97, -v183, v83, v97\n\t"
    "v_fma_f32 v99, v183, v123, v99\n\t"
    "v_cvt_pk_bf16_f32 v21, v96, v97\n\t"
    "v_cvt_pk_bf16_f32 v29, v98, v99\n\t"
    "v_mul_f32 v92, v168, v124\n\t"
    "v_mul_f32 v94, v168, v84\n\t"
    "v_mul_f32 v93, v169, v125\n\t"
    "v_mul_f32 v95, v169, v85\n\t"
    "v_fma_f32 v92, -v184, v84, v92\n\t"
    "v_fma_f32 v94, v184, v124, v94\n\t"
    "v_fma_f32 v93, -v185, v85, v93\n\t"
    "v_fma_f32 v95, v185, v125, v95\n\t"
    "v_cvt_pk_bf16_f32 v22, v92, v93\n\t"
    "v_cvt_pk_bf16_f32 v30, v94, v95\n\t"
    "v_mul_f32 v96, v170, v126\n\t"
    "v_mul_f32 v98, v170, v86\n\t"
    "v_mul_f32 v97, v171, v127\n\t"
    "v_mul_f32 v99, v171, v87\n\t"
    "v_fma_f32 v96, -v186, v86, v96\n\t"
    "v_fma_f32 v98, v186, v126, v98\n\t"
    "v_fma_f32 v97, -v187, v87, v97\n\t"
    "v_fma_f32 v99, v187, v127, v99\n\t"
    "v_cvt_pk_bf16_f32 v23, v96, v97\n\t"
    "v_cvt_pk_bf16_f32 v31, v98, v99\n\t"
    "v_mul_f32 v92, v172, v128\n\t"
    "v_mul_f32 v94, v172, v88\n\t"
    "v_mul_f32 v93, v173, v129\n\t"
    "v_mul_f32 v95, v173, v89\n\t"
    "v_fma_f32 v92, -v188, v88, v92\n\t"
    "v_fma_f32 v94, v188, v128, v94\n\t"
    "v_fma_f32 v93, -v189, v89, v93\n\t"
    "v_fma_f32 v95, v189, v129, v95\n\t"
    "v_cvt_pk_bf16_f32 v24, v92, v93\n\t"
    "v_cvt_pk_bf16_f32 v32, v94, v95\n\t"
    "v_mul_f32 v96, v174, v130\n\t"
    "v_mul_f32 v98, v174, v90\n\t"
    "v_mul_f32 v97, v175, v131\n\t"
    "v_mul_f32 v99, v175, v91\n\t"
    "v_fma_f32 v96, -v190, v90, v96\n\t"
    "v_fma_f32 v98, v190, v130, v98\n\t"
    "v_fma_f32 v97, -v191, v91, v97\n\t"
    "v_fma_f32 v99, v191, v131, v99\n\t"
    "v_cvt_pk_bf16_f32 v25, v96, v97\n\t"
    "v_cvt_pk_bf16_f32 v33, v98, v99\n\t"
    "v_mfma_f32_32x32x16_bf16 v[160:175], v[18:21], v[144:147], 0\n\t"
    "v_mfma_f32_32x32x16_bf16 v[176:191], v[18:21], v[152:155], 0\n\t"
    "v_mfma_f32_32x32x16_bf16 v[160:175], v[26:29], v[132:135], v[160:175]\n\t"
    "v_mfma_f32_32x32x16_bf16 v[176:191], v[26:29], v[144:147], v[176:191]\n\t"
    "v_mfma_f32_32x32x16_bf16 v[160:175], v[22:25], v[148:151], v[160:175]\n\t"
    "v_mfma_f32_32x32x16_bf16 v[176:191], v[22:25], v[156:159], v[176:191]\n\t"
    "v_mfma_f32_32x32x16_bf16 v[160:175], v[30:33], v[136:139], v[160:175]\n\t"
    "v_mfma_f32_32x32x16_bf16 v[176:191], v[30:33], v[148:151], v[176:191]\n\t"
    "s_lshl_b32 s70, s64, 4\n\t"
    "v_and_b32 v114, 15, v1\n\t"
    "v_add_u32 v114, s70, v114\n\t"
    "v_lshlrev_b32 v114, 5, v114\n\t"
    "v_add_u32 v114, s65, v114\n\t"
    "v_cvt_f32_u32 v109, v114\n\t"
    "v_mul_f32 v109, 0x3a000000, v109\n\t"
    "v_lshrrev_b32 v114, 4, v1\n\t"
    "v_lshlrev_b32 v114, 3, v114\n\t"
    "v_add_u32 v114, 32, v114\n\t"
    "v_cvt_f32_u32 v108, v114\n\t"
    "v_mul_f32 v108, v108, v109\n\t"
    "v_cos_f32 v110, v108\n\t"
    "v_sin_f32 v112, v108\n\t"
    "v_add_f32 v108, v108, v109\n\t"
    "v_cos_f32 v111, v108\n\t"
    "v_sin_f32 v113, v108\n\t"
    "v_add_f32 v108, v108, v109\n\t"
    "v_cvt_pk_bf16_f32 v54, v110, v111\n\t"
    "v_cvt_pk_bf16_f32 v62, v112, v113\n\t"
    "v_cos_f32 v110, v108\n\t"
    "v_sin_f32 v112, v108\n\t"
    "v_add_f32 v108, v108, v109\n\t"
    "v_cos_f32 v111, v108\n\t"
    "v_sin_f32 v113, v108\n\t"
    "v_add_f32 v108, v108, v109\n\t"
    "v_cvt_pk_bf16_f32 v55, v110, v111\n\t"
    "v_cvt_pk_bf16_f32 v63, v112, v113\n\t"
    "v_cos_f32 v110, v108\n\t"
    "v_sin_f32 v112, v108\n\t"
    "v_add_f32 v108, v108, v109\n\t"
    "v_cos_f32 v111, v108\n\t"
    "v_sin_f32 v113, v108\n\t"
    "v_add_f32 v108, v108, v109\n\t"
    "v_cvt_pk_bf16_f32 v56, v110, v111\n\t"
    "v_cvt_pk_bf16_f32 v64, v112, v113\n\t"
    "v_cos_f32 v110, v108\n\t"
    "v_sin_f32 v112, v108\n\t"
    "v_add_f32 v108, v108, v109\n\t"
    "v_cos_f32 v111, v108\n\t"
    "v_sin_f32 v113, v108\n\t"
    "v_cvt_pk_bf16_f32 v57, v110, v111\n\t"
    "v_cvt_pk_bf16_f32 v65, v112, v113\n\t"
    "v_cvt_pk_bf16_f32 v92, v160, v161\n\t"
    "v_cvt_pk_bf16_f32 v93, v162, v163\n\t"
    "ds_write_b64 v70, v[92:93] offset:0\n\t"
    "v_cvt_pk_bf16_f32 v94, v176, v177\n\t"
    "v_cvt_pk_bf16_f32 v95, v178, v179\n\t"
    "ds_write_b64 v70, v[94:95] offset:192\n\t"
    "v_cvt_pk_bf16_f32 v96, v164, v165\n\t"
    "v_cvt_pk_bf16_f32 v97, v166, v167\n\t"
    "ds_write_b64 v70, v[96:97] offset:16\n\t"
    "v_cvt_pk_bf16_f32 v98, v180, v181\n\t"
    "v_cvt_pk_bf16_f32 v99, v182, v183\n\t"
    "ds_write_b64 v70, v[98:99] offset:208\n\t"
    "v_cvt_pk_bf16_f32 v92, v168, v169\n\t"
    "v_cvt_pk_bf16_f32 v93, v170, v171\n\t"
    "ds_write_b64 v70, v[92:93] offset:32\n\t"
    "v_cvt_pk_bf16_f32 v94, v184, v185\n\t"
    "v_cvt_pk_bf16_f32 v95, v186, v187\n\t"
    "ds_write_b64 v70, v[94:95] offset:224\n\t"
    "v_cvt_pk_bf16_f32 v96, v172, v173\n\t"
    "v_cvt_pk_bf16_f32 v97, v174, v175\n\t"
    "ds_write_b64 v70, v[96:97] offset:48\n\t"
    "v_cvt_pk_bf16_f32 v98, v188, v189\n\t"
    "v_cvt_pk_bf16_f32 v99, v190, v191\n\t"
    "ds_write_b64 v70, v[98:99] offset:240\n\t"
    "s_mul_i32 s70, s66, 393216\n\t"
    "s_mul_i32 s71, s65, 192\n\t"
    "s_add_u32 s70, s70, s71\n\t"
    "s_add_u32 s70, s70, 3407872\n\t"
    "s_waitcnt lgkmcnt(0)\n\t"
    "s_barrier\n\t"
    "ds_read_b128 v[18:21], v75 offset:0\n\t"
    "ds_read_b128 v[22:25], v75 offset:64\n\t"
    "ds_read_b128 v[26:29], v75 offset:128\n\t"
    "s_waitcnt lgkmcnt(2)\n\t"
    "buffer_store_dwordx4 v[18:21], v9, s[40:43], s70 offen offset:0 sc1\n\t"
    "s_waitcnt lgkmcnt(1)\n\t"
    "buffer_store_dwordx4 v[22:25], v9, s[40:43], s70 offen offset:64 sc1\n\t"
    "s_waitcnt lgkmcnt(0)\n\t"
    "buffer_store_dwordx4 v[26:29], v9, s[40:43], s70 offen offset:128 sc1\n\t"
    "s_lshl_b32 s70, s81, 1\n\t"
    "v_add_u32 v74, s70, v6\n\t"
    "ds_read_b128 v[144:147], v74 offset:0\n\t"
    "ds_read_b128 v[148:151], v74 offset:32\n\t"
    "ds_read_b128 v[152:155], v74 offset:64\n\t"
    "ds_read_b128 v[156:159], v74 offset:96\n\t"
    "s_waitcnt vmcnt(12)\n\t"
    "ds_read_b64_tr_b16 v[132:133], v5 offset:50176\n\t"
    "ds_read_b64_tr_b16 v[134:135], v5 offset:50432\n\t"
    "ds_read_b64_tr_b16 v[136:137], v5 offset:51200\n\t"
    "ds_read_b64_tr_b16 v[138:139], v5 offset:51456\n\t"
    "s_waitcnt vmcnt(11) lgkmcnt(2)\n\t"
    "v_mfma_f32_32x32x16_bf16 v[160:175], v[132:135], a[0:3], v[144:159]\n\t"
    "s_add_u32 m0, s69, 59392\n\t"
    "ds_read_b64_tr_b16 v[140:141], v5 offset:52224\n\t"
    "buffer_load_dwordx4 v4, s[44:47], s68 offen offset:2048 lds\n\t"
    "s_add_u32 s68, s68, 442368\n\t"
    "ds_read_b64_tr_b16 v[142:143], v5 offset:52480\n\t"
    "v_mfma_f32_32x32x16_bf16 v[176:191], v[132:135], a[4:7], v[144:159]\n\t"
    "s_waitcnt vmcnt(11) lgkmcnt(2)\n\t"
    "v_mfma_f32_32x32x16_bf16 v[160:175], v[136:139], a[8:11], v[160:175]\n\t"
    "s_add_u32 m0, s69, 50176\n\t"
    "ds_read_b64_tr_b16 v[132:133], v5 offset:53248\n\t"
    "buffer_load_dwordx4 v2, s[44:47], s68 offen offset:0 lds\n\t"
    "ds_read_b64_tr_b16 v[134:135], v5 offset:53504\n\t"
    "v_exp_f32 v66, v192\n\t"
    "v_mfma_f32_32x32x16_bf16 v[176:191], v[136:139], a[12:15], v[176:191]\n\t"
    "v_exp_f32 v67, v193\n\t"
    "v_fma_f32 v66, v66, s78, s78\n\t"
    "v_exp_f32 v68, v194\n\t"
    "s_waitcnt vmcnt(11) lgkmcnt(2)\n\t"
    "v_mfma_f32_32x32x16_bf16 v[160:175], v[140:143], a[16:19], v[160:175]\n\t"
    "ds_read_b64_tr_b16 v[136:137], v5 offset:54272\n\t"
    "buffer_load_dwordx4 v3, s[44:47], s68 offen offset:1024 lds\n\t"
    "ds_read_b64_tr_b16 v[138:139], v5 offset:54528\n\t"
    "v_fma_f32 v67, v67, s78, s78\n\t"
    "v_fma_f32 v66, v66, v66, 0.5\n\t"
    "v_mfma_f32_32x32x16_bf16 v[176:191], v[140:143], a[20:23], v[176:191]\n\t"
    "v_exp_f32 v69, v195\n\t"
    "v_fma_f32 v68, v68, s78, s78\n\t"
    "v_fma_f32 v67, v67, v67, 0.5\n\t"
    "s_waitcnt vmcnt(11) lgkmcnt(2)\n\t"
    "v_mfma_f32_32x32x16_bf16 v[160:175], v[132:135], a[24:27], v[160:175]\n\t"
    "ds_read_b64_tr_b16 v[140:141], v5 offset:55296\n\t"
    "buffer_load_dwordx4 v4, s[44:47], s68 offen offset:2048 lds\n\t"
    "s_add_u32 s68, s68, 442368\n\t"
    "ds_read_b64_tr_b16 v[142:143], v5 offset:55552\n\t"
    "v_rcp_f32 v66, v66\n\t"
    "v_mfma_f32_32x32x16_bf16 v[176:191], v[132:135], a[28:31], v[176:191]\n\t"
    "v_exp_f32 v70, v196\n\t"
    "v_fma_f32 v69, v69, s78, s78\n\t"
    "v_fma_f32 v68, v68, v68, 0.5\n\t"
    "v_rcp_f32 v67, v67\n\t"
    "s_waitcnt vmcnt(11) lgkmcnt(2)\n\t"
    "v_mfma_f32_32x32x16_bf16 v[160:175], v[136:139], a[32:35], v[160:175]\n\t"
    "s_add_u32 m0, s69, 53248\n\t"
    "ds_read_b64_tr_b16 v[132:133], v5 offset:56320\n\t"
    "buffer_load_dwordx4 v2, s[44:47], s68 offen offset:0 lds\n\t"
    "ds_read_b64_tr_b16 v[134:135], v5 offset:56576\n\t"
    "v_fma_f32 v66, -v192, v66, v192\n\t"
    "v_mfma_f32_32x32x16_bf16 v[176:191], v[136:139], a[36:39], v[176:191]\n\t"
    "v_exp_f32 v71, v197\n\t"
    "v_fma_f32 v70, v70, s78, s78\n\t"
    "v_fma_f32 v69, v69, v69, 0.5\n\t"
    "s_waitcnt vmcnt(11) lgkmcnt(2)\n\t"
    "v_mfma_f32_32x32x16_bf16 v[160:175], v[140:143], a[40:43], v[160:175]\n\t"
    "ds_read_b64_tr_b16 v[136:137], v5 offset:57344\n\t"
    "buffer_load_dwordx4 v3, s[44:47], s68 offen offset:1024 lds\n\t"
    "ds_read_b64_tr_b16 v[138:139], v5 offset:57600\n\t"
    "v_rcp_f32 v68, v68\n\t"
    "v_fma_f32 v67, -v193, v67, v193\n\t"
    "v_mfma_f32_32x32x16_bf16 v[176:191], v[140:143], a[44:47], v[176:191]\n\t"
    "v_cvt_pk_bf16_f32 v76, v66, v67\n\t"
    "v_exp_f32 v72, v198\n\t"
    "v_fma_f32 v71, v71, s78, s78\n\t"
    "v_fma_f32 v70, v70, v70, 0.5\n\t"
    "s_waitcnt vmcnt(11) lgkmcnt(2)\n\t"
    "v_mfma_f32_32x32x16_bf16 v[160:175], v[132:135], a[48:51], v[160:175]\n\t"
    "ds_read_b64_tr_b16 v[140:141], v5 offset:58368\n\t"
    "buffer_load_dwordx4 v4, s[44:47], s68 offen offset:2048 lds\n\t"
    "s_add_u32 s68, s68, 442368\n\t"
    "ds_read_b64_tr_b16 v[142:143], v5 offset:58624\n\t"
    "v_rcp_f32 v69, v69\n\t"
    "v_mfma_f32_32x32x16_bf16 v[176:191], v[132:135], a[52:55], v[176:191]\n\t"
    "v_fma_f32 v68, -v194, v68, v194\n\t"
    "v_exp_f32 v73, v199\n\t"
    "v_fma_f32 v72, v72, s78, s78\n\t"
    "v_fma_f32 v71, v71, v71, 0.5\n\t"
    "s_waitcnt vmcnt(11) lgkmcnt(2)\n\t"
    "v_mfma_f32_32x32x16_bf16 v[160:175], v[136:139], a[56:59], v[160:175]\n\t"
    "s_add_u32 m0, s69, 56320\n\t"
    "ds_read_b64_tr_b16 v[132:133], v5 offset:59392\n\t"
    "buffer_load_dwordx4 v2, s[44:47], s68 offen offset:0 lds\n\t"
    "ds_read_b64_tr_b16 v[134:135], v5 offset:59648\n\t"
    "v_rcp_f32 v70, v70\n\t"
    "v_mfma_f32_32x32x16_bf16 v[176:191], v[136:139], a[60:63], v[176:191]\n\t"
    "v_fma_f32 v69, -v195, v69, v195\n\t"
    "v_cvt_pk_bf16_f32 v77, v68, v69\n\t"
    "v_exp_f32 v66, v200\n\t"
    "v_fma_f32 v73, v73, s78, s78\n\t"
    "s_waitcnt vmcnt(11) lgkmcnt(2)\n\t"
    "v_mfma_f32_32x32x16_bf16 v[160:175], v[140:143], a[64:67], v[160:175]\n\t"
    "ds_read_b64_tr_b16 v[136:137], v5 offset:60416\n\t"
    "buffer_load_dwordx4 v3, s[44:47], s68 offen offset:1024 lds\n\t"
    "ds_read_b64_tr_b16 v[138:139], v5 offset:60672\n\t"
    "v_fma_f32 v72, v72, v72, 0.5\n\t"
    "v_mfma_f32_32x32x16_bf16 v[176:191], v[140:143], a[68:71], v[176:191]\n\t"
    "v_rcp_f32 v71, v71\n\t"
    "v_fma_f32 v70, -v196, v70, v196\n\t"
    "v_exp_f32 v67, v201\n\t"
    "v_fma_f32 v66, v66, s78, s78\n\t"
    "s_waitcnt vmcnt(8) lgkmcnt(2)\n\t"
    "v_mfma_f32_32x32x16_bf16 v[160:175], v[132:135], a[72:75], v[160:175]\n\t"
    "ds_read_b64_tr_b16 v[140:141], v5 offset:61440\n\t"
    "buffer_load_dwordx4 v4, s[44:47], s68 offen offset:2048 lds\n\t"
    "s_add_u32 s68, s68, 442368\n\t"
    "ds_read_b64_tr_b16 v[142:143], v5 offset:61696\n\t"
    "v_fma_f32 v73, v73, v73, 0.5\n\t"
    "v_mfma_f32_32x32x16_bf16 v[176:191], v[132:135], a[76:79], v[176:191]\n\t"
    "v_rcp_f32 v72, v72\n\t"
    "v_fma_f32 v71, -v197, v71, v197\n\t"
    "v_cvt_pk_bf16_f32 v78, v70, v71\n\t"
    "v_exp_f32 v68, v202\n\t"
    "s_waitcnt vmcnt(8) lgkmcnt(2)\n\t"
    "v_mfma_f32_32x32x16_bf16 v[160:175], v[136:139], a[80:83], v[160:175]\n\t"
    "s_add_u32 m0, s69, 59392\n\t"
    "ds_read_b64_tr_b16 v[132:133], v5 offset:50176\n\t"
    "buffer_load_dwordx4 v2, s[44:47], s68 offen offset:0 lds\n\t"
    "ds_read_b64_tr_b16 v[134:135], v5 offset:50432\n\t"
    "v_fma_f32 v67, v67, s78, s78\n\t"
    "v_fma_f32 v66, v66, v66, 0.5\n\t"
    "v_mfma_f32_32x32x16_bf16 v[176:191], v[136:139], a[84:87], v[176:191]\n\t"
    "v_rcp_f32 v73, v73\n\t"
    "v_fma_f32 v72, -v198, v72, v198\n\t"
    "v_exp_f32 v69, v203\n\t"
    "s_lshl_b32 s70, s82, 1\n\t"
    "v_add_u32 v74, s70, v6\n\t"
    "ds_read_b128 v[144:147], v74 offset:0\n\t"
    "ds_read_b128 v[148:151], v74 offset:32\n\t"
    "ds_read_b128 v[152:155], v74 offset:64\n\t"
    "ds_read_b128 v[156:159], v74 offset:96\n\t"
    "s_waitcnt vmcnt(8) lgkmcnt(6)\n\t"
    "v_mfma_f32_32x32x16_bf16 v[160:175], v[140:143], a[88:91], v[160:175]\n\t"
    "ds_read_b64_tr_b16 v[136:137], v5 offset:51200\n\t"
    "buffer_load_dwordx4 v3, s[44:47], s68 offen offset:1024 lds\n\t"
    "ds_read_b64_tr_b16 v[138:139], v5 offset:51456\n\t"
    "v_fma_f32 v68, v68, s78, s78\n\t"
    "v_fma_f32 v67, v67, v67, 0.5\n\t"
    "v_mfma_f32_32x32x16_bf16 v[176:191], v[140:143], a[92:95], v[176:191]\n\t"
    "v_rcp_f32 v66, v66\n\t"
    "v_fma_f32 v73, -v199, v73, v199\n\t"
    "v_cvt_pk_bf16_f32 v79, v72, v73\n\t"
    "s_waitcnt vmcnt(8) lgkmcnt(6)\n\t"
    "v_mfma_f32_32x32x16_bf16 v[160:175], v[132:135], a[96:99], v[160:175]\n\t"
    "ds_read_b64_tr_b16 v[140:141], v5 offset:52224\n\t"
    "buffer_load_dwordx4 v4, s[44:47], s68 offen offset:2048 lds\n\t"
    "s_add_u32 s68, s68, 442368\n\t"
    "ds_read_b64_tr_b16 v[142:143], v5 offset:52480\n\t"
    "v_exp_f32 v70, v204\n\t"
    "v_mfma_f32_32x32x16_bf16 v[176:191], v[132:135], a[100:103], v[176:191]\n\t"
    "v_fma_f32 v69, v69, s78, s78\n\t"
    "v_fma_f32 v68, v68, v68, 0.5\n\t"
    "v_rcp_f32 v67, v67\n\t"
    "v_fma_f32 v66, -v200, v66, v200\n\t"
    "s_waitcnt vmcnt(8) lgkmcnt(2)\n\t"
    "v_mfma_f32_32x32x16_bf16 v[160:175], v[136:139], a[104:107], v[160:175]\n\t"
    "s_add_u32 m0, s69, 50176\n\t"
    "ds_read_b64_tr_b16 v[132:133], v5 offset:53248\n\t"
    "buffer_load_dwordx4 v2, s[44:47], s68 offen offset:0 lds\n\t"
    "ds_read_b64_tr_b16 v[134:135], v5 offset:53504\n\t"
    "v_exp_f32 v71, v205\n\t"
    "v_mfma_f32_32x32x16_bf16 v[176:191], v[136:139], a[108:111], v[176:191]\n\t"
    "v_fma_f32 v70, v70, s78, s78\n\t"
    "v_fma_f32 v69, v69, v69, 0.5\n\t"
    "v_rcp_f32 v68, v68\n\t"
    "v_fma_f32 v67, -v201, v67, v201\n\t"
    "s_waitcnt vmcnt(8) lgkmcnt(2)\n\t"
    "v_mfma_f32_32x32x16_bf16 v[160:175], v[140:143], a[112:115], v[160:175]\n\t"
    "ds_read_b64_tr_b16 v[136:137], v5 offset:54272\n\t"
    "buffer_load_dwordx4 v3, s[44:47], s68 offen offset:1024 lds\n\t"
    "ds_read_b64_tr_b16 v[138:139], v5 offset:54528\n\t"
    "v_cvt_pk_bf16_f32 v80, v66, v67\n\t"
    "v_mfma_f32_32x32x16_bf16 v[176:191], v[140:143], a[116:119], v[176:191]\n\t"
    "v_exp_f32 v72, v206\n\t"
    "v_fma_f32 v71, v71, s78, s78\n\t"
    "v_fma_f32 v70, v70, v70, 0.5\n\t"
    "v_rcp_f32 v69, v69\n\t"
    "v_mov_b32 v13, 145668\n\t"
    "v_cmp_eq_u32 vcc, 0, v1\n\t"
    "s_nop 1\n\t"
    "v_cndmask_b32 v14, 0, 1, vcc\n\t"
    "ds_add_rtn_u32 v12, v13, v14\n\t"
    "s_waitcnt vmcnt(8) lgkmcnt(3)\n\t"
    "v_mfma_f32_32x32x16_bf16 v[160:175], v[132:135], a[120:123], v[160:175]\n\t"
    "ds_read_b64_tr_b16 v[140:141], v5 offset:55296\n\t"
    "buffer_load_dwordx4 v4, s[44:47], s68 offen offset:2048 lds\n\t"
    "s_add_u32 s68, s68, 442368\n\t"
    "ds_read_b64_tr_b16 v[142:143], v5 offset:55552\n\t"
    "v_fma_f32 v68, -v202, v68, v202\n\t"
    "v_mfma_f32_32x32x16_bf16 v[176:191], v[132:135], a[124:127], v[176:191]\n\t"
    "v_exp_f32 v73, v207\n\t"
    "v_fma_f32 v72, v72, s78, s78\n\t"
    "v_fma_f32 v71, v71, v71, 0.5\n\t"
    "v_rcp_f32 v70, v70\n\t"
    "s_waitcnt vmcnt(8) lgkmcnt(3)\n\t"
    "v_mfma_f32_32x32x16_bf16 v[160:175], v[136:139], a[128:131], v[160:175]\n\t"
    "s_add_u32 m0, s69, 53248\n\t"
    "ds_read_b64_tr_b16 v[132:133], v5 offset:56320\n\t"
    "buffer_load_dwordx4 v2, s[44:47], s68 offen offset:0 lds\n\t"
    "ds_read_b64_tr_b16 v[134:135], v5 offset:56576\n\t"
    "v_fma_f32 v69, -v203, v69, v203\n\t"
    "v_cvt_pk_bf16_f32 v81, v68, v69\n\t"
    "v_mfma_f32_32x32x16_bf16 v[176:191], v[136:139], a[132:135], v[176:191]\n\t"
    "v_exp_f32 v66, v208\n\t"
    "v_fma_f32 v73, v73, s78, s78\n\t"
    "v_fma_f32 v72, v72, v72, 0.5\n\t"
    "s_waitcnt vmcnt(8) lgkmcnt(2)\n\t"
    "v_mfma_f32_32x32x16_bf16 v[160:175], v[140:143], a[136:139], v[160:175]\n\t"
    "ds_read_b64_tr_b16 v[136:137], v5 offset:57344\n\t"
    "buffer_load_dwordx4 v3, s[44:47], s68 offen offset:1024 lds\n\t"
    "ds_read_b64_tr_b16 v[138:139], v5 offset:57600\n\t"
    "v_rcp_f32 v71, v71\n\t"
    "v_fma_f32 v70, -v204, v70, v204\n\t"
    "v_mfma_f32_32x32x16_bf16 v[176:191], v[140:143], a[140:143], v[176:191]\n\t"
    "v_exp_f32 v67, v209\n\t"
    "v_fma_f32 v66, v66, s78, s78\n\t"
    "v_fma_f32 v73, v73, v73, 0.5\n\t"
    "s_waitcnt vmcnt(8) lgkmcnt(2)\n\t"
    "v_mfma_f32_32x32x16_bf16 v[160:175], v[132:135], a[144:147], v[160:175]\n\t"
    "ds_read_b64_tr_b16 v[140:141], v5 offset:58368\n\t"
    "buffer_load_dwordx4 v4, s[44:47], s68 offen offset:2048 lds\n\t"
    "s_add_u32 s68, s68, 442368\n\t"
    "ds_read_b64_tr_b16 v[142:143], v5 offset:58624\n\t"
    "v_rcp_f32 v72, v72\n\t"
    "v_fma_f32 v71, -v205, v71, v205\n\t"
    "v_mfma_f32_32x32x16_bf16 v[176:191], v[132:135], a[148:151], v[176:191]\n\t"
    "v_cvt_pk_bf16_f32 v82, v70, v71\n\t"
    "v_exp_f32 v68, v210\n\t"
    "v_fma_f32 v67, v67, s78, s78\n\t"
    "s_lshl_b32 s71, s66, 7\n\t"
    "s_add_u32 s71, s71, 1280\n\t"
    "s_lshl_b32 s76, s65, 2\n\t"
    "s_add_u32 s71, s71, s76\n\t"
    "v_cmp_eq_u32 vcc, 3, v12\n\t"
    "v_mov_b32 v13, 2146435072\n\t"
    "s_nop 0\n\t"
    "v_cndmask_b32 v13, v13, v10, vcc\n\t"
    "v_mov_b32 v14, s67\n\t"
    "buffer_store_dword v14, v13, s[40:43], s71 offen sc1\n\t"
    "s_waitcnt vmcnt(9) lgkmcnt(2)\n\t"
    "v_mfma_f32_32x32x16_bf16 v[160:175], v[136:139], a[152:155], v[160:175]\n\t"
    "s_add_u32 m0, s69, 56320\n\t"
    "ds_read_b64_tr_b16 v[132:133], v5 offset:59392\n\t"
    "buffer_load_dwordx4 v2, s[44:47], s68 offen offset:0 lds\n\t"
    "ds_read_b64_tr_b16 v[134:135], v5 offset:59648\n\t"
    "v_fma_f32 v66, v66, v66, 0.5\n\t"
    "v_mfma_f32_32x32x16_bf16 v[176:191], v[136:139], a[156:159], v[176:191]\n\t"
    "v_rcp_f32 v73, v73\n\t"
    "v_fma_f32 v72, -v206, v72, v206\n\t"
    "v_exp_f32 v69, v211\n\t"
    "v_fma_f32 v68, v68, s78, s78\n\t"
    "s_waitcnt vmcnt(9) lgkmcnt(2)\n\t"
    "v_mfma_f32_32x32x16_bf16 v[160:175], v[140:143], a[160:163], v[160:175]\n\t"
    "ds_read_b64_tr_b16 v[136:137], v5 offset:60416\n\t"
    "buffer_load_dwordx4 v3, s[44:47], s68 offen offset:1024 lds\n\t"
    "ds_read_b64_tr_b16 v[138:139], v5 offset:60672\n\t"
    "v_fma_f32 v67, v67, v67, 0.5\n\t"
    "v_mfma_f32_32x32x16_bf16 v[176:191], v[140:143], a[164:167], v[176:191]\n\t"
    "v_rcp_f32 v66, v66\n\t"
    "v_fma_f32 v73, -v207, v73, v207\n\t"
    "v_cvt_pk_bf16_f32 v83, v72, v73\n\t"
    "v_exp_f32 v70, v212\n\t"
    "s_waitcnt vmcnt(9) lgkmcnt(2)\n\t"
    "v_mfma_f32_32x32x16_bf16 v[160:175], v[132:135], a[168:171], v[160:175]\n\t"
    "ds_read_b64_tr_b16 v[140:141], v5 offset:61440\n\t"
    "buffer_load_dwordx4 v4, s[44:47], s68 offen offset:2048 lds\n\t"
    "s_add_u32 s68, s68, 442368\n\t"
    "ds_read_b64_tr_b16 v[142:143], v5 offset:61696\n\t"
    "v_fma_f32 v69, v69, s78, s78\n\t"
    "v_fma_f32 v68, v68, v68, 0.5\n\t"
    "v_mfma_f32_32x32x16_bf16 v[176:191], v[132:135], a[172:175], v[176:191]\n\t"
    "v_rcp_f32 v67, v67\n\t"
    "v_fma_f32 v66, -v208, v66, v208\n\t"
    "v_exp_f32 v71, v213\n\t"
    "s_waitcnt vmcnt(9) lgkmcnt(2)\n\t"
    "v_mfma_f32_32x32x16_bf16 v[160:175], v[136:139], a[176:179], v[160:175]\n\t"
    "s_add_u32 m0, s69, 59392\n\t"
    "ds_read_b64_tr_b16 v[132:133], v5 offset:50176\n\t"
    "buffer_load_dwordx4 v2, s[44:47], s68 offen offset:0 lds\n\t"
    "ds_read_b64_tr_b16 v[134:135], v5 offset:50432\n\t"
    "v_fma_f32 v70, v70, s78, s78\n\t"
    "v_fma_f32 v69, v69, v69, 0.5\n\t"
    "v_mfma_f32_32x32x16_bf16 v[176:191], v[136:139], a[180:183], v[176:191]\n\t"
    "v_rcp_f32 v68, v68\n\t"
    "v_fma_f32 v67, -v209, v67, v209\n\t"
    "v_cvt_pk_bf16_f32 v84, v66, v67\n\t"
    "s_waitcnt vmcnt(9) lgkmcnt(2)\n\t"
    "v_mfma_f32_32x32x16_bf16 v[160:175], v[140:143], a[184:187], v[160:175]\n\t"
    "ds_read_b64_tr_b16 v[136:137], v5 offset:51200\n\t"
    "buffer_load_dwordx4 v3, s[44:47], s68 offen offset:1024 lds\n\t"
    "ds_read_b64_tr_b16 v[138:139], v5 offset:51456\n\t"
    "v_exp_f32 v72, v214\n\t"
    "v_fma_f32 v71, v71, s78, s78\n\t"
    "v_mfma_f32_32x32x16_bf16 v[176:191], v[140:143], a[188:191], v[176:191]\n\t"
    "v_fma_f32 v70, v70, v70, 0.5\n\t"
    "v_rcp_f32 v69, v69\n\t"
    "v_fma_f32 v68, -v210, v68, v210\n\t"
    "s_waitcnt vmcnt(9) lgkmcnt(2)\n\t"
    "v_mfma_f32_32x32x16_bf16 v[160:175], v[132:135], a[192:195], v[160:175]\n\t"
    "ds_read_b64_tr_b16 v[140:141], v5 offset:52224\n\t"
    "buffer_load_dwordx4 v4, s[44:47], s68 offen offset:2048 lds\n\t"
    "s_mov_b32 s68, s86\n\t"
    "ds_read_b64_tr_b16 v[142:143], v5 offset:52480\n\t"
    "v_exp_f32 v73, v215\n\t"
    "v_fma_f32 v72, v72, s78, s78\n\t"
    "v_mfma_f32_32x32x16_bf16 v[176:191], v[132:135], a[196:199], v[176:191]\n\t"
    "v_fma_f32 v71, v71, v71, 0.5\n\t"
    "v_rcp_f32 v70, v70\n\t"
    "v_fma_f32 v69, -v211, v69, v211\n\t"
    "v_cvt_pk_bf16_f32 v85, v68, v69\n\t"
    "s_waitcnt vmcnt(9) lgkmcnt(2)\n\t"
    "v_mfma_f32_32x32x16_bf16 v[160:175], v[136:139], a[200:203], v[160:175]\n\t"
    "s_add_u32 m0, s69, 50176\n\t"
    "ds_read_b64_tr_b16 v[132:133], v5 offset:53248\n\t"
    "buffer_load_dwordx4 v2, s[44:47], s68 offen offset:0 lds\n\t"
    "ds_read_b64_tr_b16 v[134:135], v5 offset:53504\n\t"
    "v_exp_f32 v66, v216\n\t"
    "v_mfma_f32_32x32x16_bf16 v[176:191], v[136:139], a[204:207], v[176:191]\n\t"
    "v_fma_f32 v73, v73, s78, s78\n\t"
    "v_fma_f32 v72, v72, v72, 0.5\n\t"
    "v_rcp_f32 v71, v71\n\t"
    "s_waitcnt vmcnt(9) lgkmcnt(2)\n\t"
    "v_mfma_f32_32x32x16_bf16 v[160:175], v[140:143], a[208:211], v[160:175]\n\t"
    "ds_read_b64_tr_b16 v[136:137], v5 offset:54272\n\t"
    "buffer_load_dwordx4 v3, s[44:47], s68 offen offset:1024 lds\n\t"
    "ds_read_b64_tr_b16 v[138:139], v5 offset:54528\n\t"
    "v_fma_f32 v70, -v212, v70, v212\n\t"
    "v_mfma_f32_32x32x16_bf16 v[176:191], v[140:143], a[212:215], v[176:191]\n\t"
    "v_exp_f32 v67, v217\n\t"
    "v_fma_f32 v66, v66, s78, s78\n\t"
    "v_fma_f32 v73, v73, v73, 0.5\n\t"
    "v_rcp_f32 v72, v72\n\t"
    "s_waitcnt vmcnt(9) lgkmcnt(2)\n\t"
    "v_mfma_f32_32x32x16_bf16 v[160:175], v[132:135], a[216:219], v[160:175]\n\t"
    "ds_read_b64_tr_b16 v[140:141], v5 offset:55296\n\t"
    "buffer_load_dwordx4 v4, s[44:47], s68 offen offset:2048 lds\n\t"
    "s_add_u32 s68, s68, 442368\n\t"
    "ds_read_b64_tr_b16 v[142:143], v5 offset:55552\n\t"
    "v_fma_f32 v71, -v213, v71, v213\n\t"
    "v_cvt_pk_bf16_f32 v86, v70, v71\n\t"
    "v_mfma_f32_32x32x16_bf16 v[176:191], v[132:135], a[220:223], v[176:191]\n\t"
    "v_exp_f32 v68, v218\n\t"
    "v_fma_f32 v67, v67, s78, s78\n\t"
    "v_fma_f32 v66, v66, v66, 0.5\n\t"
    "s_waitcnt vmcnt(8) lgkmcnt(2)\n\t"
    "v_mfma_f32_32x32x16_bf16 v[160:175], v[136:139], a[224:227], v[160:175]\n\t"
    "s_add_u32 m0, s69, 53248\n\t"
    "ds_read_b64_tr_b16 v[132:133], v5 offset:56320\n\t"
    "buffer_load_dwordx4 v2, s[44:47], s68 offen offset:0 lds\n\t"
    "ds_read_b64_tr_b16 v[134:135], v5 offset:56576\n\t"
    "v_rcp_f32 v73, v73\n\t"
    "v_fma_f32 v72, -v214, v72, v214\n\t"
    "v_mfma_f32_32x32x16_bf16 v[176:191], v[136:139], a[228:231], v[176:191]\n\t"
    "v_exp_f32 v69, v219\n\t"
    "v_fma_f32 v68, v68, s78, s78\n\t"
    "v_fma_f32 v67, v67, v67, 0.5\n\t"
    "s_waitcnt vmcnt(8) lgkmcnt(2)\n\t"
    "v_mfma_f32_32x32x16_bf16 v[160:175], v[140:143], a[232:235], v[160:175]\n\t"
    "ds_read_b64_tr_b16 v[136:137], v5 offset:57344\n\t"
    "buffer_load_dwordx4 v3, s[44:47], s68 offen offset:1024 lds\n\t"
    "ds_read_b64_tr_b16 v[138:139], v5 offset:57600\n\t"
    "v_rcp_f32 v66, v66\n\t"
    "v_fma_f32 v73, -v215, v73, v215\n\t"
    "v_mfma_f32_32x32x16_bf16 v[176:191], v[140:143], a[236:239], v[176:191]\n\t"
    "v_cvt_pk_bf16_f32 v87, v72, v73\n\t"
    "v_exp_f32 v70, v220\n\t"
    "v_fma_f32 v69, v69, s78, s78\n\t"
    "v_fma_f32 v68, v68, v68, 0.5\n\t"
    "s_waitcnt vmcnt(8) lgkmcnt(2)\n\t"
    "v_mfma_f32_32x32x16_bf16 v[160:175], v[132:135], a[240:243], v[160:175]\n\t"
    "ds_read_b64_tr_b16 v[140:141], v5 offset:58368\n\t"
    "buffer_load_dwordx4 v4, s[44:47], s68 offen offset:2048 lds\n\t"
    "s_add_u32 s68, s68, 442368\n\t"
    "ds_read_b64_tr_b16 v[142:143], v5 offset:58624\n\t"
    "v_rcp_f32 v67, v67\n\t"
    "v_mfma_f32_32x32x16_bf16 v[176:191], v[132:135], a[244:247], v[176:191]\n\t"
    "v_fma_f32 v66, -v216, v66, v216\n\t"
    "v_exp_f32 v71, v221\n\t"
    "v_fma_f32 v70, v70, s78, s78\n\t"
    "v_fma_f32 v69, v69, v69, 0.5\n\t"
    "s_waitcnt vmcnt(8) lgkmcnt(2)\n\t"
    "v_mfma_f32_32x32x16_bf16 v[160:175], v[136:139], a[248:251], v[160:175]\n\t"
    "s_add_u32 m0, s69, 56320\n\t"
    "ds_read_b64_tr_b16 v[132:133], v5 offset:59392\n\t"
    "buffer_load_dwordx4 v2, s[44:47], s68 offen offset:0 lds\n\t"
    "ds_read_b64_tr_b16 v[134:135], v5 offset:59648\n\t"
    "v_rcp_f32 v68, v68\n\t"
    "v_mfma_f32_32x32x16_bf16 v[176:191], v[136:139], a[252:255], v[176:191]\n\t"
    "v_fma_f32 v67, -v217, v67, v217\n\t"
    "v_cvt_pk_bf16_f32 v88, v66, v67\n\t"
    "v_exp_f32 v72, v222\n\t"
    "v_fma_f32 v71, v71, s78, s78\n\t"
    "s_waitcnt vmcnt(8) lgkmcnt(2)\n\t"
    "v_mfma_f32_32x32x16_bf16 v[160:175], v[140:143], v[224:227], v[160:175]\n\t"
    "ds_read_b64_tr_b16 v[136:137], v5 offset:60416\n\t"
    "buffer_load_dwordx4 v3, s[44:47], s68 offen offset:1024 lds\n\t"
    "ds_read_b64_tr_b16 v[138:139], v5 offset:60672\n\t"
    "v_fma_f32 v70, v70, v70, 0.5\n\t"
    "v_mfma_f32_32x32x16_bf16 v[176:191], v[140:143], v[228:231], v[176:191]\n\t"
    "v_rcp_f32 v69, v69\n\t"
    "v_fma_f32 v68, -v218, v68, v218\n\t"
    "v_exp_f32 v73, v223\n\t"
    "s_waitcnt vmcnt(8) lgkmcnt(2)\n\t"
    "v_mfma_f32_32x32x16_bf16 v[160:175], v[132:135], v[232:235], v[160:175]\n\t"
    "ds_read_b64_tr_b16 v[140:141], v5 offset:61440\n\t"
    "buffer_load_dwordx4 v4, s[44:47], s68 offen offset:2048 lds\n\t"
    "s_add_u32 s68, s68, 442368\n\t"
    "ds_read_b64_tr_b16 v[142:143], v5 offset:61696\n\t"
    "v_fma_f32 v72, v72, s78, s78\n\t"
    "v_fma_f32 v71, v71, v71, 0.5\n\t"
    "v_mfma_f32_32x32x16_bf16 v[176:191], v[132:135], v[236:239], v[176:191]\n\t"
    "v_rcp_f32 v70, v70\n\t"
    "v_fma_f32 v69, -v219, v69, v219\n\t"
    "v_cvt_pk_bf16_f32 v89, v68, v69\n\t"
    "v_fma_f32 v73, v73, s78, s78\n\t"
    "s_waitcnt vmcnt(8) lgkmcnt(2)\n\t"
    "v_mfma_f32_32x32x16_bf16 v[160:175], v[136:139], v[240:243], v[160:175]\n\t"
    "s_add_u32 m0, s69, 59392\n\t"
    "ds_read_b64_tr_b16 v[132:133], v5 offset:50176\n\t"
    "buffer_load_dwordx4 v2, s[44:47], s68 offen offset:0 lds\n\t"
    "ds_read_b64_tr_b16 v[134:135], v5 offset:50432\n\t"
    "v_fma_f32 v72, v72, v72, 0.5\n\t"
    "v_mfma_f32_32x32x16_bf16 v[176:191], v[136:139], v[244:247], v[176:191]\n\t"
    "v_rcp_f32 v71, v71\n\t"
    "v_fma_f32 v70, -v220, v70, v220\n\t"
    "v_fma_f32 v73, v73, v73, 0.5\n\t"
    "v_rcp_f32 v72, v72\n\t"
    "s_waitcnt vmcnt(8) lgkmcnt(2)\n\t"
    "v_mfma_f32_32x32x16_bf16 v[160:175], v[140:143], v[248:251], v[160:175]\n\t"
    "ds_read_b64_tr_b16 v[136:137], v5 offset:51200\n\t"
    "buffer_load_dwordx4 v3, s[44:47], s68 offen offset:1024 lds\n\t"
    "ds_read_b64_tr_b16 v[138:139], v5 offset:51456\n\t"
    "v_fma_f32 v71, -v221, v71, v221\n\t"
    "v_cvt_pk_bf16_f32 v90, v70, v71\n\t"
    "v_mfma_f32_32x32x16_bf16 v[176:191], v[140:143], v[252:255], v[176:191]\n\t"
    "v_rcp_f32 v73, v73\n\t"
    "v_fma_f32 v72, -v222, v72, v222\n\t"
    "v_fma_f32 v73, -v223, v73, v223\n\t"
    "v_cvt_pk_bf16_f32 v91, v72, v73\n\t"
    "s_waitcnt vmcnt(8) lgkmcnt(2)\n\t"
    "v_mfma_f32_32x32x16_bf16 v[192:207], v[132:135], a[0:3], v[144:159]\n\t"
    "ds_read_b64_tr_b16 v[140:141], v5 offset:52224\n\t"
    "buffer_load_dwordx4 v4, s[44:47], s68 offen offset:2048 lds\n\t"
    "s_add_u32 s68, s68, 442368\n\t"
    "ds_read_b64_tr_b16 v[142:143], v5 offset:52480\n\t"
    "v_mfma_f32_32x32x16_bf16 v[208:223], v[132:135], a[4:7], v[144:159]\n\t"
    "s_waitcnt vmcnt(8) lgkmcnt(2)\n\t"
    "v_mfma_f32_32x32x16_bf16 v[192:207], v[136:139], a[8:11], v[192:207]\n\t"
    "s_add_u32 m0, s69, 50176\n\t"
    "ds_read_b64_tr_b16 v[132:133], v5 offset:53248\n\t"
    "buffer_load_dwordx4 v2, s[44:47], s68 offen offset:0 lds\n\t"
    "ds_read_b64_tr_b16 v[134:135], v5 offset:53504\n\t"
    "v_exp_f32 v66, v160\n\t"
    "v_mfma_f32_32x32x16_bf16 v[208:223], v[136:139], a[12:15], v[208:223]\n\t"
    "v_exp_f32 v67, v161\n\t"
    "v_fma_f32 v66, v66, s78, s78\n\t"
    "v_exp_f32 v68, v162\n\t"
    "s_waitcnt vmcnt(8) lgkmcnt(2)\n\t"
    "v_mfma_f32_32x32x16_bf16 v[192:207], v[140:143], a[16:19], v[192:207]\n\t"
    "ds_read_b64_tr_b16 v[136:137], v5 offset:54272\n\t"
    "buffer_load_dwordx4 v3, s[44:47], s68 offen offset:1024 lds\n\t"
    "ds_read_b64_tr_b16 v[138:139], v5 offset:54528\n\t"
    "v_fma_f32 v67, v67, s78, s78\n\t"
    "v_fma_f32 v66, v66, v66, 0.5\n\t"
    "v_mfma_f32_32x32x16_bf16 v[208:223], v[140:143], a[20:23], v[208:223]\n\t"
    "v_exp_f32 v69, v163\n\t"
    "v_fma_f32 v68, v68, s78, s78\n\t"
    "v_fma_f32 v67, v67, v67, 0.5\n\t"
    "v_and_b32 v15, 31, v1\n\t"
    "v_lshlrev_b32 v15, 2, v15\n\t"
    "s_lshl_b32 s71, s66, 7\n\t"
    "s_add_u32 s71, s71, 1280\n\t"
    "s_cmp_eq_u32 s64, 0\n\t"
    "s_cselect_b32 s71, s71, 2146435072\n\t"
    "buffer_load_dword v11, v15, s[40:43], s71 offen sc1\n\t"
    "s_waitcnt vmcnt(9) lgkmcnt(2)\n\t"
    "v_mfma_f32_32x32x16_bf16 v[192:207], v[132:135], a[24:27], v[192:207]\n\t"
    "ds_read_b64_tr_b16 v[140:141], v5 offset:55296\n\t"
    "buffer_load_dwordx4 v4, s[44:47], s68 offen offset:2048 lds\n\t"
    "s_add_u32 s68, s68, 442368\n\t"
    "ds_read_b64_tr_b16 v[142:143], v5 offset:55552\n\t"
    "v_rcp_f32 v66, v66\n\t"
    "v_mfma_f32_32x32x16_bf16 v[208:223], v[132:135], a[28:31], v[208:223]\n\t"
    "v_exp_f32 v70, v164\n\t"
    "v_fma_f32 v69, v69, s78, s78\n\t"
    "v_fma_f32 v68, v68, v68, 0.5\n\t"
    "v_rcp_f32 v67, v67\n\t"
    "s_waitcnt vmcnt(9) lgkmcnt(2)\n\t"
    "v_mfma_f32_32x32x16_bf16 v[192:207], v[136:139], a[32:35], v[192:207]\n\t"
    "s_add_u32 m0, s69, 53248\n\t"
    "ds_read_b64_tr_b16 v[132:133], v5 offset:56320\n\t"
    "buffer_load_dwordx4 v2, s[44:47], s68 offen offset:0 lds\n\t"
    "ds_read_b64_tr_b16 v[134:135], v5 offset:56576\n\t"
    "v_fma_f32 v66, -v160, v66, v160\n\t"
    "v_mfma_f32_32x32x16_bf16 v[208:223], v[136:139], a[36:39], v[208:223]\n\t"
    "v_exp_f32 v71, v165\n\t"
    "v_fma_f32 v70, v70, s78, s78\n\t"
    "v_fma_f32 v69, v69, v69, 0.5\n\t"
    "s_waitcnt vmcnt(9) lgkmcnt(2)\n\t"
    "v_mfma_f32_32x32x16_bf16 v[192:207], v[140:143], a[40:43], v[192:207]\n\t"
    "ds_read_b64_tr_b16 v[136:137], v5 offset:57344\n\t"
    "buffer_load_dwordx4 v3, s[44:47], s68 offen offset:1024 lds\n\t"
    "ds_read_b64_tr_b16 v[138:139], v5 offset:57600\n\t"
    "v_rcp_f32 v68, v68\n\t"
    "v_fma_f32 v67, -v161, v67, v161\n\t"
    "v_mfma_f32_32x32x16_bf16 v[208:223], v[140:143], a[44:47], v[208:223]\n\t"
    "v_cvt_pk_bf16_f32 v92, v66, v67\n\t"
    "v_exp_f32 v72, v166\n\t"
    "v_fma_f32 v71, v71, s78, s78\n\t"
    "v_fma_f32 v70, v70, v70, 0.5\n\t"
    "s_waitcnt vmcnt(9) lgkmcnt(2)\n\t"
    "v_mfma_f32_32x32x16_bf16 v[192:207], v[132:135], a[48:51], v[192:207]\n\t"
    "ds_read_b64_tr_b16 v[140:141], v5 offset:58368\n\t"
    "buffer_load_dwordx4 v4, s[44:47], s68 offen offset:2048 lds\n\t"
    "s_add_u32 s68, s68, 442368\n\t"
    "ds_read_b64_tr_b16 v[142:143], v5 offset:58624\n\t"
    "v_rcp_f32 v69, v69\n\t"
    "v_mfma_f32_32x32x16_bf16 v[208:223], v[132:135], a[52:55], v[208:223]\n\t"
    "v_fma_f32 v68, -v162, v68, v162\n\t"
    "v_exp_f32 v73, v167\n\t"
    "v_fma_f32 v72, v72, s78, s78\n\t"
    "v_fma_f32 v71, v71, v71, 0.5\n\t"
    "s_waitcnt vmcnt(9) lgkmcnt(2)\n\t"
    "v_mfma_f32_32x32x16_bf16 v[192:207], v[136:139], a[56:59], v[192:207]\n\t"
    "s_add_u32 m0, s69, 56320\n\t"
    "ds_read_b64_tr_b16 v[132:133], v5 offset:59392\n\t"
    "buffer_load_dwordx4 v2, s[44:47], s68 offen offset:0 lds\n\t"
    "ds_read_b64_tr_b16 v[134:135], v5 offset:59648\n\t"
    "v_rcp_f32 v70, v70\n\t"
    "v_mfma_f32_32x32x16_bf16 v[208:223], v[136:139], a[60:63], v[208:223]\n\t"
    "v_fma_f32 v69, -v163, v69, v163\n\t"
    "v_cvt_pk_bf16_f32 v93, v68, v69\n\t"
    "v_exp_f32 v66, v168\n\t"
    "v_fma_f32 v73, v73, s78, s78\n\t"
    "s_waitcnt vmcnt(9) lgkmcnt(2)\n\t"
    "v_mfma_f32_32x32x16_bf16 v[192:207], v[140:143], a[64:67], v[192:207]\n\t"
    "ds_read_b64_tr_b16 v[136:137], v5 offset:60416\n\t"
    "buffer_load_dwordx4 v3, s[44:47], s68 offen offset:1024 lds\n\t"
    "ds_read_b64_tr_b16 v[138:139], v5 offset:60672\n\t"
    "v_fma_f32 v72, v72, v72, 0.5\n\t"
    "v_mfma_f32_32x32x16_bf16 v[208:223], v[140:143], a[68:71], v[208:223]\n\t"
    "v_rcp_f32 v71, v71\n\t"
    "v_fma_f32 v70, -v164, v70, v164\n\t"
    "v_exp_f32 v67, v169\n\t"
    "v_fma_f32 v66, v66, s78, s78\n\t"
    "s_waitcnt vmcnt(9) lgkmcnt(2)\n\t"
    "v_mfma_f32_32x32x16_bf16 v[192:207], v[132:135], a[72:75], v[192:207]\n\t"
    "ds_read_b64_tr_b16 v[140:141], v5 offset:61440\n\t"
    "buffer_load_dwordx4 v4, s[44:47], s68 offen offset:2048 lds\n\t"
    "s_add_u32 s68, s68, 442368\n\t"
    "ds_read_b64_tr_b16 v[142:143], v5 offset:61696\n\t"
    "v_fma_f32 v73, v73, v73, 0.5\n\t"
    "v_mfma_f32_32x32x16_bf16 v[208:223], v[132:135], a[76:79], v[208:223]\n\t"
    "v_rcp_f32 v72, v72\n\t"
    "v_fma_f32 v71, -v165, v71, v165\n\t"
    "v_cvt_pk_bf16_f32 v94, v70, v71\n\t"
    "v_exp_f32 v68, v170\n\t"
    "s_waitcnt vmcnt(9) lgkmcnt(2)\n\t"
    "v_mfma_f32_32x32x16_bf16 v[192:207], v[136:139], a[80:83], v[192:207]\n\t"
    "s_add_u32 m0, s69, 59392\n\t"
    "ds_read_b64_tr_b16 v[132:133], v5 offset:50176\n\t"
    "buffer_load_dwordx4 v2, s[44:47], s68 offen offset:0 lds\n\t"
    "ds_read_b64_tr_b16 v[134:135], v5 offset:50432\n\t"
    "v_fma_f32 v67, v67, s78, s78\n\t"
    "v_fma_f32 v66, v66, v66, 0.5\n\t"
    "v_mfma_f32_32x32x16_bf16 v[208:223], v[136:139], a[84:87], v[208:223]\n\t"
    "v_rcp_f32 v73, v73\n\t"
    "v_fma_f32 v72, -v166, v72, v166\n\t"
    "v_exp_f32 v69, v171\n\t"
    "s_lshl_b32 s70, s83, 1\n\t"
    "v_add_u32 v74, s70, v6\n\t"
    "ds_read_b128 v[144:147], v74 offset:0\n\t"
    "ds_read_b128 v[148:151], v74 offset:32\n\t"
    "ds_read_b128 v[152:155], v74 offset:64\n\t"
    "ds_read_b128 v[156:159], v74 offset:96\n\t"
    "s_waitcnt vmcnt(9) lgkmcnt(6)\n\t"
    "v_mfma_f32_32x32x16_bf16 v[192:207], v[140:143], a[88:91], v[192:207]\n\t"
    "ds_read_b64_tr_b16 v[136:137], v5 offset:51200\n\t"
    "buffer_load_dwordx4 v3, s[44:47], s68 offen offset:1024 lds\n\t"
    "ds_read_b64_tr_b16 v[138:139], v5 offset:51456\n\t"
    "v_fma_f32 v68, v68, s78, s78\n\t"
    "v_fma_f32 v67, v67, v67, 0.5\n\t"
    "v_mfma_f32_32x32x16_bf16 v[208:223], v[140:143], a[92:95], v[208:223]\n\t"
    "v_rcp_f32 v66, v66\n\t"
    "v_fma_f32 v73, -v167, v73, v167\n\t"
    "v_cvt_pk_bf16_f32 v95, v72, v73\n\t"
    "s_waitcnt vmcnt(8) lgkmcnt(6)\n\t"
    "v_mfma_f32_32x32x16_bf16 v[192:207], v[132:135], a[96:99], v[192:207]\n\t"
    "ds_read_b64_tr_b16 v[140:141], v5 offset:52224\n\t"
    "buffer_load_dwordx4 v4, s[44:47], s68 offen offset:2048 lds\n\t"
    "s_add_u32 s68, s68, 442368\n\t"
    "ds_read_b64_tr_b16 v[142:143], v5 offset:52480\n\t"
    "v_exp_f32 v70, v172\n\t"
    "v_mfma_f32_32x32x16_bf16 v[208:223], v[132:135], a[100:103], v[208:223]\n\t"
    "v_fma_f32 v69, v69, s78, s78\n\t"
    "v_fma_f32 v68, v68, v68, 0.5\n\t"
    "v_rcp_f32 v67, v67\n\t"
    "v_fma_f32 v66, -v168, v66, v168\n\t"
    "s_waitcnt vmcnt(8) lgkmcnt(2)\n\t"
    "v_mfma_f32_32x32x16_bf16 v[192:207], v[136:139], a[104:107], v[192:207]\n\t"
    "s_add_u32 m0, s69, 50176\n\t"
    "ds_read_b64_tr_b16 v[132:133], v5 offset:53248\n\t"
    "buffer_load_dwordx4 v2, s[44:47], s68 offen offset:0 lds\n\t"
    "ds_read_b64_tr_b16 v[134:135], v5 offset:53504\n\t"
    "v_exp_f32 v71, v173\n\t"
    "v_mfma_f32_32x32x16_bf16 v[208:223], v[136:139], a[108:111], v[208:223]\n\t"
    "v_fma_f32 v70, v70, s78, s78\n\t"
    "v_fma_f32 v69, v69, v69, 0.5\n\t"
    "v_rcp_f32 v68, v68\n\t"
    "v_fma_f32 v67, -v169, v67, v169\n\t"
    "s_waitcnt vmcnt(8) lgkmcnt(2)\n\t"
    "v_mfma_f32_32x32x16_bf16 v[192:207], v[140:143], a[112:115], v[192:207]\n\t"
    "ds_read_b64_tr_b16 v[136:137], v5 offset:54272\n\t"
    "buffer_load_dwordx4 v3, s[44:47], s68 offen offset:1024 lds\n\t"
    "ds_read_b64_tr_b16 v[138:139], v5 offset:54528\n\t"
    "v_cvt_pk_bf16_f32 v96, v66, v67\n\t"
    "v_mfma_f32_32x32x16_bf16 v[208:223], v[140:143], a[116:119], v[208:223]\n\t"
    "v_exp_f32 v72, v174\n\t"
    "v_fma_f32 v71, v71, s78, s78\n\t"
    "v_fma_f32 v70, v70, v70, 0.5\n\t"
    "v_rcp_f32 v69, v69\n\t"
    "s_waitcnt vmcnt(8) lgkmcnt(2)\n\t"
    "v_mfma_f32_32x32x16_bf16 v[192:207], v[132:135], a[120:123], v[192:207]\n\t"
    "ds_read_b64_tr_b16 v[140:141], v5 offset:55296\n\t"
    "buffer_load_dwordx4 v4, s[44:47], s68 offen offset:2048 lds\n\t"
    "s_add_u32 s68, s68, 442368\n\t"
    "ds_read_b64_tr_b16 v[142:143], v5 offset:55552\n\t"
    "v_fma_f32 v68, -v170, v68, v170\n\t"
    "v_mfma_f32_32x32x16_bf16 v[208:223], v[132:135], a[124:127], v[208:223]\n\t"
    "v_exp_f32 v73, v175\n\t"
    "v_fma_f32 v72, v72, s78, s78\n\t"
    "v_fma_f32 v71, v71, v71, 0.5\n\t"
    "v_rcp_f32 v70, v70\n\t"
    "s_waitcnt vmcnt(8) lgkmcnt(2)\n\t"
    "v_mfma_f32_32x32x16_bf16 v[192:207], v[136:139], a[128:131], v[192:207]\n\t"
    "s_add_u32 m0, s69, 53248\n\t"
    "ds_read_b64_tr_b16 v[132:133], v5 offset:56320\n\t"
    "buffer_load_dwordx4 v2, s[44:47], s68 offen offset:0 lds\n\t"
    "ds_read_b64_tr_b16 v[134:135], v5 offset:56576\n\t"
    "v_fma_f32 v69, -v171, v69, v171\n\t"
    "v_cvt_pk_bf16_f32 v97, v68, v69\n\t"
    "v_mfma_f32_32x32x16_bf16 v[208:223], v[136:139], a[132:135], v[208:223]\n\t"
    "v_exp_f32 v66, v176\n\t"
    "v_fma_f32 v73, v73, s78, s78\n\t"
    "v_fma_f32 v72, v72, v72, 0.5\n\t"
    "s_waitcnt vmcnt(8) lgkmcnt(2)\n\t"
    "v_mfma_f32_32x32x16_bf16 v[192:207], v[140:143], a[136:139], v[192:207]\n\t"
    "ds_read_b64_tr_b16 v[136:137], v5 offset:57344\n\t"
    "buffer_load_dwordx4 v3, s[44:47], s68 offen offset:1024 lds\n\t"
    "ds_read_b64_tr_b16 v[138:139], v5 offset:57600\n\t"
    "v_rcp_f32 v71, v71\n\t"
    "v_fma_f32 v70, -v172, v70, v172\n\t"
    "v_mfma_f32_32x32x16_bf16 v[208:223], v[140:143], a[140:143], v[208:223]\n\t"
    "v_exp_f32 v67, v177\n\t"
    "v_fma_f32 v66, v66, s78, s78\n\t"
    "v_fma_f32 v73, v73, v73, 0.5\n\t"
    "s_waitcnt vmcnt(8) lgkmcnt(2)\n\t"
    "v_mfma_f32_32x32x16_bf16 v[192:207], v[132:135], a[144:147], v[192:207]\n\t"
    "ds_read_b64_tr_b16 v[140:141], v5 offset:58368\n\t"
    "buffer_load_dwordx4 v4, s[44:47], s68 offen offset:2048 lds\n\t"
    "s_add_u32 s68, s68, 442368\n\t"
    "ds_read_b64_tr_b16 v[142:143], v5 offset:58624\n\t"
    "v_rcp_f32 v72, v72\n\t"
    "v_fma_f32 v71, -v173, v71, v173\n\t"
    "v_mfma_f32_32x32x16_bf16 v[208:223], v[132:135], a[148:151], v[208:223]\n\t"
    "v_cvt_pk_bf16_f32 v98, v70, v71\n\t"
    "v_exp_f32 v68, v178\n\t"
    "v_fma_f32 v67, v67, s78, s78\n\t"
    "s_waitcnt vmcnt(8) lgkmcnt(2)\n\t"
    "v_mfma_f32_32x32x16_bf16 v[192:207], v[136:139], a[152:155], v[192:207]\n\t"
    "s_add_u32 m0, s69, 56320\n\t"
    "ds_read_b64_tr_b16 v[132:133], v5 offset:59392\n\t"
    "buffer_load_dwordx4 v2, s[44:47], s68 offen offset:0 lds\n\t"
    "ds_read_b64_tr_b16 v[134:135], v5 offset:59648\n\t"
    "v_fma_f32 v66, v66, v66, 0.5\n\t"
    "v_mfma_f32_32x32x16_bf16 v[208:223], v[136:139], a[156:159], v[208:223]\n\t"
    "v_rcp_f32 v73, v73\n\t"
    "v_fma_f32 v72, -v174, v72, v174\n\t"
    "v_exp_f32 v69, v179\n\t"
    "v_fma_f32 v68, v68, s78, s78\n\t"
    "s_waitcnt vmcnt(8) lgkmcnt(2)\n\t"
    "v_mfma_f32_32x32x16_bf16 v[192:207], v[140:143], a[160:163], v[192:207]\n\t"
    "ds_read_b64_tr_b16 v[136:137], v5 offset:60416\n\t"
    "buffer_load_dwordx4 v3, s[44:47], s68 offen offset:1024 lds\n\t"
    "ds_read_b64_tr_b16 v[138:139], v5 offset:60672\n\t"
    "v_fma_f32 v67, v67, v67, 0.5\n\t"
    "v_mfma_f32_32x32x16_bf16 v[208:223], v[140:143], a[164:167], v[208:223]\n\t"
    "v_rcp_f32 v66, v66\n\t"
    "v_fma_f32 v73, -v175, v73, v175\n\t"
    "v_cvt_pk_bf16_f32 v99, v72, v73\n\t"
    "v_exp_f32 v70, v180\n\t"
    "s_waitcnt vmcnt(8) lgkmcnt(2)\n\t"
    "v_mfma_f32_32x32x16_bf16 v[192:207], v[132:135], a[168:171], v[192:207]\n\t"
    "ds_read_b64_tr_b16 v[140:141], v5 offset:61440\n\t"
    "buffer_load_dwordx4 v4, s[44:47], s68 offen offset:2048 lds\n\t"
    "s_add_u32 s68, s68, 442368\n\t"
    "ds_read_b64_tr_b16 v[142:143], v5 offset:61696\n\t"
    "v_fma_f32 v69, v69, s78, s78\n\t"
    "v_fma_f32 v68, v68, v68, 0.5\n\t"
    "v_mfma_f32_32x32x16_bf16 v[208:223], v[132:135], a[172:175], v[208:223]\n\t"
    "v_rcp_f32 v67, v67\n\t"
    "v_fma_f32 v66, -v176, v66, v176\n\t"
    "v_exp_f32 v71, v181\n\t"
    "s_waitcnt vmcnt(8) lgkmcnt(2)\n\t"
    "v_mfma_f32_32x32x16_bf16 v[192:207], v[136:139], a[176:179], v[192:207]\n\t"
    "s_add_u32 m0, s69, 59392\n\t"
    "ds_read_b64_tr_b16 v[132:133], v5 offset:50176\n\t"
    "buffer_load_dwordx4 v2, s[44:47], s68 offen offset:0 lds\n\t"
    "ds_read_b64_tr_b16 v[134:135], v5 offset:50432\n\t"
    "v_fma_f32 v70, v70, s78, s78\n\t"
    "v_fma_f32 v69, v69, v69, 0.5\n\t"
    "v_mfma_f32_32x32x16_bf16 v[208:223], v[136:139], a[180:183], v[208:223]\n\t"
    "v_rcp_f32 v68, v68\n\t"
    "v_fma_f32 v67, -v177, v67, v177\n\t"
    "v_cvt_pk_bf16_f32 v100, v66, v67\n\t"
    "s_waitcnt vmcnt(8) lgkmcnt(2)\n\t"
    "v_mfma_f32_32x32x16_bf16 v[192:207], v[140:143], a[184:187], v[192:207]\n\t"
    "ds_read_b64_tr_b16 v[136:137], v5 offset:51200\n\t"
    "buffer_load_dwordx4 v3, s[44:47], s68 offen offset:1024 lds\n\t"
    "ds_read_b64_tr_b16 v[138:139], v5 offset:51456\n\t"
    "v_exp_f32 v72, v182\n\t"
    "v_fma_f32 v71, v71, s78, s78\n\t"
    "v_mfma_f32_32x32x16_bf16 v[208:223], v[140:143], a[188:191], v[208:223]\n\t"
    "v_fma_f32 v70, v70, v70, 0.5\n\t"
    "v_rcp_f32 v69, v69\n\t"
    "v_fma_f32 v68, -v178, v68, v178\n\t"
    "s_waitcnt vmcnt(8) lgkmcnt(2)\n\t"
    "v_mfma_f32_32x32x16_bf16 v[192:207], v[132:135], a[192:195], v[192:207]\n\t"
    "ds_read_b64_tr_b16 v[140:141], v5 offset:52224\n\t"
    "buffer_load_dwordx4 v4, s[44:47], s68 offen offset:2048 lds\n\t"
    "s_mov_b32 s68, s87\n\t"
    "ds_read_b64_tr_b16 v[142:143], v5 offset:52480\n\t"
    "v_exp_f32 v73, v183\n\t"
    "v_fma_f32 v72, v72, s78, s78\n\t"
    "v_mfma_f32_32x32x16_bf16 v[208:223], v[132:135], a[196:199], v[208:223]\n\t"
    "v_fma_f32 v71, v71, v71, 0.5\n\t"
    "v_rcp_f32 v70, v70\n\t"
    "v_fma_f32 v69, -v179, v69, v179\n\t"
    "v_cvt_pk_bf16_f32 v101, v68, v69\n\t"
    "s_waitcnt vmcnt(8) lgkmcnt(2)\n\t"
    "v_mfma_f32_32x32x16_bf16 v[192:207], v[136:139], a[200:203], v[192:207]\n\t"
    "s_add_u32 m0, s69, 50176\n\t"
    "ds_read_b64_tr_b16 v[132:133], v5 offset:53248\n\t"
    "buffer_load_dwordx4 v2, s[44:47], s68 offen offset:0 lds\n\t"
    "ds_read_b64_tr_b16 v[134:135], v5 offset:53504\n\t"
    "v_exp_f32 v66, v184\n\t"
    "v_mfma_f32_32x32x16_bf16 v[208:223], v[136:139], a[204:207], v[208:223]\n\t"
    "v_fma_f32 v73, v73, s78, s78\n\t"
    "v_fma_f32 v72, v72, v72, 0.5\n\t"
    "v_rcp_f32 v71, v71\n\t"
    "s_waitcnt vmcnt(8) lgkmcnt(2)\n\t"
    "v_mfma_f32_32x32x16_bf16 v[192:207], v[140:143], a[208:211], v[192:207]\n\t"
    "ds_read_b64_tr_b16 v[136:137], v5 offset:54272\n\t"
    "buffer_load_dwordx4 v3, s[44:47], s68 offen offset:1024 lds\n\t"
    "ds_read_b64_tr_b16 v[138:139], v5 offset:54528\n\t"
    "v_fma_f32 v70, -v180, v70, v180\n\t"
    "v_mfma_f32_32x32x16_bf16 v[208:223], v[140:143], a[212:215], v[208:223]\n\t"
    "v_exp_f32 v67, v185\n\t"
    "v_fma_f32 v66, v66, s78, s78\n\t"
    "v_fma_f32 v73, v73, v73, 0.5\n\t"
    "v_rcp_f32 v72, v72\n\t"
    "s_waitcnt vmcnt(8) lgkmcnt(2)\n\t"
    "v_mfma_f32_32x32x16_bf16 v[192:207], v[132:135], a[216:219], v[192:207]\n\t"
    "ds_read_b64_tr_b16 v[140:141], v5 offset:55296\n\t"
    "buffer_load_dwordx4 v4, s[44:47], s68 offen offset:2048 lds\n\t"
    "s_add_u32 s68, s68, 442368\n\t"
    "ds_read_b64_tr_b16 v[142:143], v5 offset:55552\n\t"
    "v_fma_f32 v71, -v181, v71, v181\n\t"
    "v_cvt_pk_bf16_f32 v102, v70, v71\n\t"
    "v_mfma_f32_32x32x16_bf16 v[208:223], v[132:135], a[220:223], v[208:223]\n\t"
    "v_exp_f32 v68, v186\n\t"
    "v_fma_f32 v67, v67, s78, s78\n\t"
    "v_fma_f32 v66, v66, v66, 0.5\n\t"
    "s_waitcnt vmcnt(8) lgkmcnt(2)\n\t"
    "v_mfma_f32_32x32x16_bf16 v[192:207], v[136:139], a[224:227], v[192:207]\n\t"
    "s_add_u32 m0, s69, 53248\n\t"
    "ds_read_b64_tr_b16 v[132:133], v5 offset:56320\n\t"
    "buffer_load_dwordx4 v2, s[44:47], s68 offen offset:0 lds\n\t"
    "ds_read_b64_tr_b16 v[134:135], v5 offset:56576\n\t"
    "v_rcp_f32 v73, v73\n\t"
    "v_fma_f32 v72, -v182, v72, v182\n\t"
    "v_mfma_f32_32x32x16_bf16 v[208:223], v[136:139], a[228:231], v[208:223]\n\t"
    "v_exp_f32 v69, v187\n\t"
    "v_fma_f32 v68, v68, s78, s78\n\t"
    "v_fma_f32 v67, v67, v67, 0.5\n\t"
    "s_waitcnt vmcnt(8) lgkmcnt(2)\n\t"
    "v_mfma_f32_32x32x16_bf16 v[192:207], v[140:143], a[232:235], v[192:207]\n\t"
    "ds_read_b64_tr_b16 v[136:137], v5 offset:57344\n\t"
    "buffer_load_dwordx4 v3, s[44:47], s68 offen offset:1024 lds\n\t"
    "ds_read_b64_tr_b16 v[138:139], v5 offset:57600\n\t"
    "v_rcp_f32 v66, v66\n\t"
    "v_fma_f32 v73, -v183, v73, v183\n\t"
    "v_mfma_f32_32x32x16_bf16 v[208:223], v[140:143], a[236:239], v[208:223]\n\t"
    "v_cvt_pk_bf16_f32 v103, v72, v73\n\t"
    "v_exp_f32 v70, v188\n\t"
    "v_fma_f32 v69, v69, s78, s78\n\t"
    "v_fma_f32 v68, v68, v68, 0.5\n\t"
    "s_waitcnt vmcnt(8) lgkmcnt(2)\n\t"
    "v_mfma_f32_32x32x16_bf16 v[192:207], v[132:135], a[240:243], v[192:207]\n\t"
    "ds_read_b64_tr_b16 v[140:141], v5 offset:58368\n\t"
    "buffer_load_dwordx4 v4, s[44:47], s68 offen offset:2048 lds\n\t"
    "s_add_u32 s68, s68, 442368\n\t"
    "ds_read_b64_tr_b16 v[142:143], v5 offset:58624\n\t"
    "v_rcp_f32 v67, v67\n\t"
    "v_mfma_f32_32x32x16_bf16 v[208:223], v[132:135], a[244:247], v[208:223]\n\t"
    "v_fma_f32 v66, -v184, v66, v184\n\t"
    "v_exp_f32 v71, v189\n\t"
    "v_fma_f32 v70, v70, s78, s78\n\t"
    "v_fma_f32 v69, v69, v69, 0.5\n\t"
    "s_waitcnt vmcnt(8) lgkmcnt(2)\n\t"
    "v_mfma_f32_32x32x16_bf16 v[192:207], v[136:139], a[248:251], v[192:207]\n\t"
    "s_add_u32 m0, s69, 56320\n\t"
    "ds_read_b64_tr_b16 v[132:133], v5 offset:59392\n\t"
    "buffer_load_dwordx4 v2, s[44:47], s68 offen offset:0 lds\n\t"
    "ds_read_b64_tr_b16 v[134:135], v5 offset:59648\n\t"
    "v_rcp_f32 v68, v68\n\t"
    "v_mfma_f32_32x32x16_bf16 v[208:223], v[136:139], a[252:255], v[208:223]\n\t"
    "v_fma_f32 v67, -v185, v67, v185\n\t"
    "v_cvt_pk_bf16_f32 v104, v66, v67\n\t"
    "v_exp_f32 v72, v190\n\t"
    "v_fma_f32 v71, v71, s78, s78\n\t"
    "s_waitcnt vmcnt(8) lgkmcnt(2)\n\t"
    "v_mfma_f32_32x32x16_bf16 v[192:207], v[140:143], v[224:227], v[192:207]\n\t"
    "ds_read_b64_tr_b16 v[136:137], v5 offset:60416\n\t"
    "buffer_load_dwordx4 v3, s[44:47], s68 offen offset:1024 lds\n\t"
    "ds_read_b64_tr_b16 v[138:139], v5 offset:60672\n\t"
    "v_fma_f32 v70, v70, v70, 0.5\n\t"
    "v_mfma_f32_32x32x16_bf16 v[208:223], v[140:143], v[228:231], v[208:223]\n\t"
    "v_rcp_f32 v69, v69\n\t"
    "v_fma_f32 v68, -v186, v68, v186\n\t"
    "v_exp_f32 v73, v191\n\t"
    "s_waitcnt vmcnt(8) lgkmcnt(2)\n\t"
    "v_mfma_f32_32x32x16_bf16 v[192:207], v[132:135], v[232:235], v[192:207]\n\t"
    "ds_read_b64_tr_b16 v[140:141], v5 offset:61440\n\t"
    "buffer_load_dwordx4 v4, s[44:47], s68 offen offset:2048 lds\n\t"
    "s_add_u32 s68, s68, 442368\n\t"
    "ds_read_b64_tr_b16 v[142:143], v5 offset:61696\n\t"
    "v_fma_f32 v72, v72, s78, s78\n\t"
    "v_fma_f32 v71, v71, v71, 0.5\n\t"
    "v_mfma_f32_32x32x16_bf16 v[208:223], v[132:135], v[236:239], v[208:223]\n\t"
    "v_rcp_f32 v70, v70\n\t"
    "v_fma_f32 v69, -v187, v69, v187\n\t"
    "v_cvt_pk_bf16_f32 v105, v68, v69\n\t"
    "v_fma_f32 v73, v73, s78, s78\n\t"
    "s_waitcnt vmcnt(8) lgkmcnt(2)\n\t"
    "v_mfma_f32_32x32x16_bf16 v[192:207], v[136:139], v[240:243], v[192:207]\n\t"
    "s_add_u32 m0, s69, 59392\n\t"
    "ds_read_b64_tr_b16 v[132:133], v5 offset:50176\n\t"
    "buffer_load_dwordx4 v2, s[44:47], s68 offen offset:0 lds\n\t"
    "ds_read_b64_tr_b16 v[134:135], v5 offset:50432\n\t"
    "v_fma_f32 v72, v72, v72, 0.5\n\t"
    "v_mfma_f32_32x32x16_bf16 v[208:223], v[136:139], v[244:247], v[208:223]\n\t"
    "v_rcp_f32 v71, v71\n\t"
    "v_fma_f32 v70, -v188, v70, v188\n\t"
    "v_fma_f32 v73, v73, v73, 0.5\n\t"
    "v_rcp_f32 v72, v72\n\t"
    "s_waitcnt vmcnt(8) lgkmcnt(2)\n\t"
    "v_mfma_f32_32x32x16_bf16 v[192:207], v[140:143], v[248:251], v[192:207]\n\t"
    "ds_read_b64_tr_b16 v[136:137], v5 offset:51200\n\t"
    "buffer_load_dwordx4 v3, s[44:47], s68 offen offset:1024 lds\n\t"
    "ds_read_b64_tr_b16 v[138:139], v5 offset:51456\n\t"
    "v_fma_f32 v71, -v189, v71, v189\n\t"
    "v_cvt_pk_bf16_f32 v106, v70, v71\n\t"
    "v_mfma_f32_32x32x16_bf16 v[208:223], v[140:143], v[252:255], v[208:223]\n\t"
    "v_rcp_f32 v73, v73\n\t"
    "v_fma_f32 v72, -v190, v72, v190\n\t"
    "v_fma_f32 v73, -v191, v73, v191\n\t"
    "v_cvt_pk_bf16_f32 v107, v72, v73\n\t"
    "s_cmp_lg_u32 s88, 0\n\t"
    "s_cbranch_scc1 L_p1k1_10%=\n\t"
    "s_waitcnt vmcnt(8) lgkmcnt(2)\n\t"
    "v_mfma_f32_32x32x16_bf16 v[160:175], v[132:135], a[0:3], v[144:159]\n\t"
    "ds_read_b64_tr_b16 v[140:141], v5 offset:52224\n\t"
    "buffer_load_dwordx4 v4, s[44:47], s68 offen offset:2048 lds\n\t"
    "s_add_u32 s68, s68, 442368\n\t"
    "ds_read_b64_tr_b16 v[142:143], v5 offset:52480\n\t"
    "v_mfma_f32_32x32x16_bf16 v[176:191], v[132:135], a[4:7], v[144:159]\n\t"
    "s_waitcnt vmcnt(8) lgkmcnt(2)\n\t"
    "v_mfma_f32_32x32x16_bf16 v[160:175], v[136:139], a[8:11], v[160:175]\n\t"
    "s_add_u32 m0, s69, 50176\n\t"
    "ds_read_b64_tr_b16 v[132:133], v5 offset:53248\n\t"
    "buffer_load_dwordx4 v2, s[44:47], s68 offen offset:0 lds\n\t"
    "ds_read_b64_tr_b16 v[134:135], v5 offset:53504\n\t"
    "v_exp_f32 v66, v192\n\t"
    "v_exp_f32 v67, v193\n\t"
    "v_mfma_f32_32x32x16_bf16 v[176:191], v[136:139], a[12:15], v[176:191]\n\t"
    "v_fma_f32 v66, v66, s78, s78\n\t"
    "v_exp_f32 v68, v194\n\t"
    "v_fma_f32 v67, v67, s78, s78\n\t"
    "v_fma_f32 v66, v66, v66, 0.5\n\t"
    "v_exp_f32 v69, v195\n\t"
    "v_fma_f32 v68, v68, s78, s78\n\t"
    "v_fma_f32 v67, v67, v67, 0.5\n\t"
    "s_waitcnt vmcnt(8) lgkmcnt(2)\n\t"
    "v_mfma_f32_32x32x16_bf16 v[160:175], v[140:143], a[16:19], v[160:175]\n\t"
    "ds_read_b64_tr_b16 v[136:137], v5 offset:54272\n\t"
    "buffer_load_dwordx4 v3, s[44:47], s68 offen offset:1024 lds\n\t"
    "ds_read_b64_tr_b16 v[138:139], v5 offset:54528\n\t"
    "v_rcp_f32 v66, v66\n\t"
    "v_exp_f32 v70, v196\n\t"
    "v_fma_f32 v69, v69, s78, s78\n\t"
    "v_mfma_f32_32x32x16_bf16 v[176:191], v[140:143], a[20:23], v[176:191]\n\t"
    "v_fma_f32 v68, v68, v68, 0.5\n\t"
    "v_rcp_f32 v67, v67\n\t"
    "v_fma_f32 v66, -v192, v66, v192\n\t"
    "v_exp_f32 v71, v197\n\t"
    "v_fma_f32 v70, v70, s78, s78\n\t"
    "v_fma_f32 v69, v69, v69, 0.5\n\t"
    "v_rcp_f32 v68, v68\n\t"
    "v_mov_b32 v16, 145676\n\t"
    "s_cmp_lg_u32 s64, 0\n\t"
    "s_cbranch_scc1 L_nopoll_12%=\n\t"
    "v_and_b32 v15, 31, v1\n\t"
    "v_lshlrev_b32 v15, 2, v15\n\t"
    "s_lshl_b32 s71, s66, 7\n\t"
    "s_add_u32 s71, s71, 1280\n\t"
    "v_cmp_ne_u32 vcc, s67, v11\n\t"
    "s_mov_b32 s94, 0x40000\n\t"
    "s_cbranch_vccz L_ok_13%=\n\t"
    "L_spin_14%=:\n\t"
    "s_sleep 1\n\t"
    "buffer_load_dword v11, v15, s[40:43], s71 offen sc1\n\t"
    "s_waitcnt vmcnt(0)\n\t"
    "v_cmp_ne_u32 vcc, s67, v11\n\t"
    "s_sub_u32 s94, s94, 1\n\t"
    "s_nop 3\n\t"
    "s_cbranch_vccz L_ok_13%=\n\t"
    "s_cmp_lg_u32 s94, 0\n\t"
    "s_cbranch_scc1 L_spin_14%=\n\t"
    "L_ok_13%=:\n\t"
    "v_mov_b32 v15, 1\n\t"
    "ds_write_b32 v16, v15\n\t"
    "L_nopoll_12%=:\n\t"
    "s_mov_b32 s94, 0x400000\n\t"
    "L_ldsspin_15%=:\n\t"
    "ds_read_b32 v15, v16\n\t"
    "s_waitcnt lgkmcnt(0)\n\t"
    "v_cmp_ne_u32 vcc, 1, v15\n\t"
    "s_sub_u32 s94, s94, 1\n\t"
    "s_nop 3\n\t"
    "s_cbranch_vccz L_ldsok_16%=\n\t"
    "s_cmp_lg_u32 s94, 0\n\t"
    "s_cbranch_scc1 L_ldsspin_15%=\n\t"
    "L_ldsok_16%=:\n\t"
    "s_lshl_b32 s70, s66, 5\n\t"
    "s_add_u32 s70, s70, s65\n\t"
    "s_mul_i32 s70, s70, 12288\n\t"
    "s_add_u32 s70, s70, 3275520\n\t"
    "buffer_load_dwordx4 v[18:21], v8, s[40:43], s70 offen sc1\n\t"
    "s_add_u32 s70, s70, 4096\n\t"
    "buffer_load_dwordx4 v[22:25], v8, s[40:43], s70 offen sc1\n\t"
    "s_add_u32 s70, s70, 4096\n\t"
    "buffer_load_dwordx4 v[26:29], v8, s[40:43], s70 offen sc1\n\t"
    "s_waitcnt vmcnt(11)\n\t"
    "v_mfma_f32_32x32x16_bf16 v[160:175], v[132:135], a[24:27], v[160:175]\n\t"
    "ds_read_b64_tr_b16 v[140:141], v5 offset:55296\n\t"
    "buffer_load_dwordx4 v4, s[44:47], s68 offen offset:2048 lds\n\t"
    "s_add_u32 s68, s68, 442368\n\t"
    "ds_read_b64_tr_b16 v[142:143], v5 offset:55552\n\t"
    "v_fma_f32 v67, -v193, v67, v193\n\t"
    "v_cvt_pk_bf16_f32 v108, v66, v67\n\t"
    "v_exp_f32 v72, v198\n\t"
    "v_mfma_f32_32x32x16_bf16 v[176:191], v[132:135], a[28:31], v[176:191]\n\t"
    "v_fma_f32 v71, v71, s78, s78\n\t"
    "v_fma_f32 v70, v70, v70, 0.5\n\t"
    "v_rcp_f32 v69, v69\n\t"
    "v_fma_f32 v68, -v194, v68, v194\n\t"
    "v_exp_f32 v73, v199\n\t"
    "v_fma_f32 v72, v72, s78, s78\n\t"
    "v_fma_f32 v71, v71, v71, 0.5\n\t"
    "s_waitcnt vmcnt(11)\n\t"
    "v_mfma_f32_32x32x16_bf16 v[160:175], v[136:139], a[32:35], v[160:175]\n\t"
    "s_add_u32 m0, s69, 53248\n\t"
    "ds_read_b64_tr_b16 v[132:133], v5 offset:56320\n\t"
    "buffer_load_dwordx4 v2, s[44:47], s68 offen offset:0 lds\n\t"
    "ds_read_b64_tr_b16 v[134:135], v5 offset:56576\n\t"
    "v_rcp_f32 v70, v70\n\t"
    "v_fma_f32 v69, -v195, v69, v195\n\t"
    "v_cvt_pk_bf16_f32 v109, v68, v69\n\t"
    "v_exp_f32 v66, v200\n\t"
    "v_mfma_f32_32x32x16_bf16 v[176:191], v[136:139], a[36:39], v[176:191]\n\t"
    "v_fma_f32 v73, v73, s78, s78\n\t"
    "v_fma_f32 v72, v72, v72, 0.5\n\t"
    "v_rcp_f32 v71, v71\n\t"
    "v_fma_f32 v70, -v196, v70, v196\n\t"
    "v_exp_f32 v67, v201\n\t"
    "v_fma_f32 v66, v66, s78, s78\n\t"
    "v_fma_f32 v73, v73, v73, 0.5\n\t"
    "s_waitcnt vmcnt(11) lgkmcnt(2)\n\t"
    "v_mfma_f32_32x32x16_bf16 v[160:175], v[140:143], a[40:43], v[160:175]\n\t"
    "ds_read_b64_tr_b16 v[136:137], v5 offset:57344\n\t"
    "buffer_load_dwordx4 v3, s[44:47], s68 offen offset:1024 lds\n\t"
    "ds_read_b64_tr_b16 v[138:139], v5 offset:57600\n\t"
    "v_rcp_f32 v72, v72\n\t"
    "v_fma_f32 v71, -v197, v71, v197\n\t"
    "v_cvt_pk_bf16_f32 v110, v70, v71\n\t"
    "v_mfma_f32_32x32x16_bf16 v[176:191], v[140:143], a[44:47], v[176:191]\n\t"
    "v_exp_f32 v68, v202\n\t"
    "v_fma_f32 v67, v67, s78, s78\n\t"
    "v_fma_f32 v66, v66, v66, 0.5\n\t"
    "v_rcp_f32 v73, v73\n\t"
    "v_fma_f32 v72, -v198, v72, v198\n\t"
    "v_exp_f32 v69, v203\n\t"
    "v_fma_f32 v68, v68, s78, s78\n\t"
    "v_fma_f32 v67, v67, v67, 0.5\n\t"
    "s_waitcnt vmcnt(11) lgkmcnt(2)\n\t"
    "v_mfma_f32_32x32x16_bf16 v[160:175], v[132:135], a[48:51], v[160:175]\n\t"
    "ds_read_b64_tr_b16 v[140:141], v5 offset:58368\n\t"
    "buffer_load_dwordx4 v4, s[44:47], s68 offen offset:2048 lds\n\t"
    "ds_read_b64_tr_b16 v[142:143], v5 offset:58624\n\t"
    "v_rcp_f32 v66, v66\n\t"
    "v_fma_f32 v73, -v199, v73, v199\n\t"
    "v_cvt_pk_bf16_f32 v111, v72, v73\n\t"
    "v_mfma_f32_32x32x16_bf16 v[176:191], v[132:135], a[52:55], v[176:191]\n\t"
    "v_exp_f32 v70, v204\n\t"
    "v_fma_f32 v69, v69, s78, s78\n\t"
    "v_fma_f32 v68, v68, v68, 0.5\n\t"
    "v_rcp_f32 v67, v67\n\t"
    "v_fma_f32 v66, -v200, v66, v200\n\t"
    "v_exp_f32 v71, v205\n\t"
    "v_fma_f32 v70, v70, s78, s78\n\t"
    "s_waitcnt vmcnt(11) lgkmcnt(2)\n\t"
    "v_mfma_f32_32x32x16_bf16 v[160:175], v[136:139], a[56:59], v[160:175]\n\t"
    "ds_read_b64_tr_b16 v[132:133], v5 offset:59392\n\t"
    "ds_read_b64_tr_b16 v[134:135], v5 offset:59648\n\t"
    "v_fma_f32 v69, v69, v69, 0.5\n\t"
    "v_rcp_f32 v68, v68\n\t"
    "v_fma_f32 v67, -v201, v67, v201\n\t"
    "v_mfma_f32_32x32x16_bf16 v[176:191], v[136:139], a[60:63], v[176:191]\n\t"
    "v_cvt_pk_bf16_f32 v112, v66, v67\n\t"
    "v_exp_f32 v72, v206\n\t"
    "v_fma_f32 v71, v71, s78, s78\n\t"
    "v_fma_f32 v70, v70, v70, 0.5\n\t"
    "v_rcp_f32 v69, v69\n\t"
    "v_fma_f32 v68, -v202, v68, v202\n\t"
    "v_exp_f32 v73, v207\n\t"
    "s_waitcnt vmcnt(10) lgkmcnt(2)\n\t"
    "v_mfma_f32_32x32x16_bf16 v[160:175], v[140:143], a[64:67], v[160:175]\n\t"
    "ds_read_b64_tr_b16 v[136:137], v5 offset:60416\n\t"
    "ds_read_b64_tr_b16 v[138:139], v5 offset:60672\n\t"
    "v_fma_f32 v72, v72, s78, s78\n\t"
    "v_fma_f32 v71, v71, v71, 0.5\n\t"
    "v_rcp_f32 v70, v70\n\t"
    "v_mfma_f32_32x32x16_bf16 v[176:191], v[140:143], a[68:71], v[176:191]\n\t"
    "v_fma_f32 v69, -v203, v69, v203\n\t"
    "v_cvt_pk_bf16_f32 v113, v68, v69\n\t"
    "v_exp_f32 v66, v208\n\t"
    "v_fma_f32 v73, v73, s78, s78\n\t"
    "v_fma_f32 v72, v72, v72, 0.5\n\t"
    "v_rcp_f32 v71, v71\n\t"
    "v_fma_f32 v70, -v204, v70, v204\n\t"
    "v_add_u32 v74, s79, v7\n\t"
    "ds_write_b64 v74, v[34:35] offset:0\n\t"
    "ds_write_b64 v74, v[36:37] offset:16\n\t"
    "ds_write_b64 v74, v[38:39] offset:32\n\t"
    "ds_write_b64 v74, v[40:41] offset:48\n\t"
    "ds_write_b64 v74, v[42:43] offset:37376\n\t"
    "ds_write_b64 v74, v[44:45] offset:37392\n\t"
    "ds_write_b64 v74, v[46:47] offset:37408\n\t"
    "ds_write_b64 v74, v[48:49] offset:37424\n\t"
    "s_waitcnt vmcnt(9) lgkmcnt(10)\n\t"
    "v_mfma_f32_32x32x16_bf16 v[160:175], v[132:135], a[72:75], v[160:175]\n\t"
    "ds_read_b64_tr_b16 v[140:141], v5 offset:61440\n\t"
    "ds_read_b64_tr_b16 v[142:143], v5 offset:61696\n\t"
    "v_exp_f32 v67, v209\n\t"
    "v_fma_f32 v66, v66, s78, s78\n\t"
    "v_fma_f32 v73, v73, v73, 0.5\n\t"
    "v_mfma_f32_32x32x16_bf16 v[176:191], v[132:135], a[76:79], v[176:191]\n\t"
    "v_rcp_f32 v72, v72\n\t"
    "v_fma_f32 v71, -v205, v71, v205\n\t"
    "v_cvt_pk_bf16_f32 v114, v70, v71\n\t"
    "v_exp_f32 v68, v210\n\t"
    "v_fma_f32 v67, v67, s78, s78\n\t"
    "v_fma_f32 v66, v66, v66, 0.5\n\t"
    "v_rcp_f32 v73, v73\n\t"
    "v_fma_f32 v72, -v206, v72, v206\n\t"
    "s_waitcnt vmcnt(8) lgkmcnt(10)\n\t"
    "v_mfma_f32_32x32x16_bf16 v[160:175], v[136:139], a[80:83], v[160:175]\n\t"
    "ds_read_b64_tr_b16 v[132:133], v5 offset:50176\n\t"
    "ds_read_b64_tr_b16 v[134:135], v5 offset:50432\n\t"
    "v_exp_f32 v69, v211\n\t"
    "v_fma_f32 v68, v68, s78, s78\n\t"
    "v_fma_f32 v67, v67, v67, 0.5\n\t"
    "v_mfma_f32_32x32x16_bf16 v[176:191], v[136:139], a[84:87], v[176:191]\n\t"
    "v_rcp_f32 v66, v66\n\t"
    "v_fma_f32 v73, -v207, v73, v207\n\t"
    "v_cvt_pk_bf16_f32 v115, v72, v73\n\t"
    "v_exp_f32 v70, v212\n\t"
    "v_fma_f32 v69, v69, s78, s78\n\t"
    "v_fma_f32 v68, v68, v68, 0.5\n\t"
    "v_rcp_f32 v67, v67\n\t"
    "s_waitcnt vmcnt(7) lgkmcnt(2)\n\t"
    "v_mfma_f32_32x32x16_bf16 v[160:175], v[140:143], a[88:91], v[160:175]\n\t"
    "ds_read_b64_tr_b16 v[136:137], v5 offset:51200\n\t"
    "ds_read_b64_tr_b16 v[138:139], v5 offset:51456\n\t"
    "v_fma_f32 v66, -v208, v66, v208\n\t"
    "v_exp_f32 v71, v213\n\t"
    "v_fma_f32 v70, v70, s78, s78\n\t"
    "v_mfma_f32_32x32x16_bf16 v[176:191], v[140:143], a[92:95], v[176:191]\n\t"
    "v_fma_f32 v69, v69, v69, 0.5\n\t"
    "v_rcp_f32 v68, v68\n\t"
    "v_fma_f32 v67, -v209, v67, v209\n\t"
    "v_cvt_pk_bf16_f32 v116, v66, v67\n\t"
    "v_exp_f32 v72, v214\n\t"
    "v_fma_f32 v71, v71, s78, s78\n\t"
    "v_fma_f32 v70, v70, v70, 0.5\n\t"
    "v_add_u32 v74, s80, v7\n\t"
    "ds_write_b64 v74, v[76:77] offset:0\n\t"
    "ds_write_b64 v74, v[78:79] offset:16\n\t"
    "ds_write_b64 v74, v[80:81] offset:32\n\t"
    "ds_write_b64 v74, v[82:83] offset:48\n\t"
    "ds_write_b64 v74, v[84:85] offset:37376\n\t"
    "ds_write_b64 v74, v[86:87] offset:37392\n\t"
    "ds_write_b64 v74, v[88:89] offset:37408\n\t"
    "ds_write_b64 v74, v[90:91] offset:37424\n\t"
    "s_waitcnt vmcnt(3) lgkmcnt(10)\n\t"
    "v_mfma_f32_32x32x16_bf16 v[160:175], v[132:135], a[96:99], v[160:175]\n\t"
    "ds_read_b64_tr_b16 v[140:141], v5 offset:52224\n\t"
    "ds_read_b64_tr_b16 v[142:143], v5 offset:52480\n\t"
    "v_rcp_f32 v69, v69\n\t"
    "v_fma_f32 v68, -v210, v68, v210\n\t"
    "v_exp_f32 v73, v215\n\t"
    "v_fma_f32 v72, v72, s78, s78\n\t"
    "v_mfma_f32_32x32x16_bf16 v[176:191], v[132:135], a[100:103], v[176:191]\n\t"
    "v_fma_f32 v71, v71, v71, 0.5\n\t"
    "v_rcp_f32 v70, v70\n\t"
    "v_fma_f32 v69, -v211, v69, v211\n\t"
    "v_cvt_pk_bf16_f32 v117, v68, v69\n\t"
    "v_exp_f32 v66, v216\n\t"
    "v_fma_f32 v73, v73, s78, s78\n\t"
    "v_fma_f32 v72, v72, v72, 0.5\n\t"
    "s_waitcnt vmcnt(2) lgkmcnt(10)\n\t"
    "v_mfma_f32_32x32x16_bf16 v[160:175], v[136:139], a[104:107], v[160:175]\n\t"
    "ds_read_b64_tr_b16 v[132:133], v5 offset:53248\n\t"
    "ds_read_b64_tr_b16 v[134:135], v5 offset:53504\n\t"
    "v_rcp_f32 v71, v71\n\t"
    "v_fma_f32 v70, -v212, v70, v212\n\t"
    "v_exp_f32 v67, v217\n\t"
    "v_mfma_f32_32x32x16_bf16 v[176:191], v[136:139], a[108:111], v[176:191]\n\t"
    "v_fma_f32 v66, v66, s78, s78\n\t"
    "v_fma_f32 v73, v73, v73, 0.5\n\t"
    "v_rcp_f32 v72, v72\n\t"
    "v_fma_f32 v71, -v213, v71, v213\n\t"
    "v_cvt_pk_bf16_f32 v118, v70, v71\n\t"
    "v_exp_f32 v68, v218\n\t"
    "v_fma_f32 v67, v67, s78, s78\n\t"
    "v_fma_f32 v66, v66, v66, 0.5\n\t"
    "s_waitcnt vmcnt(1) lgkmcnt(2)\n\t"
    "v_mfma_f32_32x32x16_bf16 v[160:175], v[140:143], a[112:115], v[160:175]\n\t"
    "ds_read_b64_tr_b16 v[136:137], v5 offset:54272\n\t"
    "ds_read_b64_tr_b16 v[138:139], v5 offset:54528\n\t"
    "v_rcp_f32 v73, v73\n\t"
    "v_fma_f32 v72, -v214, v72, v214\n\t"
    "v_mfma_f32_32x32x16_bf16 v[176:191], v[140:143], a[116:119], v[176:191]\n\t"
    "v_exp_f32 v69, v219\n\t"
    "v_fma_f32 v68, v68, s78, s78\n\t"
    "v_fma_f32 v67, v67, v67, 0.5\n\t"
    "v_rcp_f32 v66, v66\n\t"
    "v_fma_f32 v73, -v215, v73, v215\n\t"
    "v_cvt_pk_bf16_f32 v119, v72, v73\n\t"
    "v_exp_f32 v70, v220\n\t"
    "v_fma_f32 v69, v69, s78, s78\n\t"
    "v_add_u32 v74, s81, v7\n\t"
    "ds_write_b64 v74, v[92:93] offset:0\n\t"
    "ds_write_b64 v74, v[94:95] offset:16\n\t"
    "ds_write_b64 v74, v[96:97] offset:32\n\t"
    "ds_write_b64 v74, v[98:99] offset:48\n\t"
    "ds_write_b64 v74, v[100:101] offset:37376\n\t"
    "ds_write_b64 v74, v[102:103] offset:37392\n\t"
    "ds_write_b64 v74, v[104:105] offset:37408\n\t"
    "ds_write_b64 v74, v[106:107] offset:37424\n\t"
    "s_waitcnt vmcnt(0) lgkmcnt(10)\n\t"
    "v_mfma_f32_32x32x16_bf16 v[160:175], v[132:135], a[120:123], v[160:175]\n\t"
    "ds_read_b64_tr_b16 v[140:141], v5 offset:55296\n\t"
    "ds_read_b64_tr_b16 v[142:143], v5 offset:55552\n\t"
    "v_fma_f32 v68, v68, v68, 0.5\n\t"
    "v_rcp_f32 v67, v67\n\t"
    "v_fma_f32 v66, -v216, v66, v216\n\t"
    "v_mfma_f32_32x32x16_bf16 v[176:191], v[132:135], a[124:127], v[176:191]\n\t"
    "v_exp_f32 v71, v221\n\t"
    "v_fma_f32 v70, v70, s78, s78\n\t"
    "v_fma_f32 v69, v69, v69, 0.5\n\t"
    "v_rcp_f32 v68, v68\n\t"
    "v_fma_f32 v67, -v217, v67, v217\n\t"
    "v_cvt_pk_bf16_f32 v120, v66, v67\n\t"
    "v_exp_f32 v72, v222\n\t"
    "s_waitcnt lgkmcnt(10)\n\t"
    "v_mfma_f32_32x32x16_bf16 v[160:175], v[136:139], a[128:131], v[160:175]\n\t"
    "v_fma_f32 v71, v71, s78, s78\n\t"
    "v_fma_f32 v70, v70, v70, 0.5\n\t"
    "v_rcp_f32 v69, v69\n\t"
    "v_mfma_f32_32x32x16_bf16 v[176:191], v[136:139], a[132:135], v[176:191]\n\t"
    "v_fma_f32 v68, -v218, v68, v218\n\t"
    "v_exp_f32 v73, v223\n\t"
    "v_fma_f32 v72, v72, s78, s78\n\t"
    "v_fma_f32 v71, v71, v71, 0.5\n\t"
    "v_rcp_f32 v70, v70\n\t"
    "v_fma_f32 v69, -v219, v69, v219\n\t"
    "v_cvt_pk_bf16_f32 v121, v68, v69\n\t"
    "v_fma_f32 v73, v73, s78, s78\n\t"
    "s_waitcnt lgkmcnt(0)\n\t"
    "v_mfma_f32_32x32x16_bf16 v[160:175], v[140:143], a[136:139], v[160:175]\n\t"
    "v_fma_f32 v72, v72, v72, 0.5\n\t"
    "v_rcp_f32 v71, v71\n\t"
    "v_fma_f32 v70, -v220, v70, v220\n\t"
    "v_mfma_f32_32x32x16_bf16 v[176:191], v[140:143], a[140:143], v[176:191]\n\t"
    "v_fma_f32 v73, v73, v73, 0.5\n\t"
    "v_rcp_f32 v72, v72\n\t"
    "v_fma_f32 v71, -v221, v71, v221\n\t"
    "v_cvt_pk_bf16_f32 v122, v70, v71\n\t"
    "v_rcp_f32 v73, v73\n\t"
    "v_fma_f32 v72, -v222, v72, v222\n\t"
    "v_fma_f32 v73, -v223, v73, v223\n\t"
    "v_cvt_pk_bf16_f32 v123, v72, v73\n\t"
    "v_lshlrev_b32 v16, 4, v1\n\t"
    "s_xor_b32 s70, s64, 1\n\t"
    "s_mul_i32 s70, s70, 12288\n\t"
    "s_add_u32 s70, s70, 74752\n\t"
    "v_add_u32 v17, s70, v16\n\t"
    "s_mul_i32 s70, s64, 12288\n\t"
    "s_add_u32 s70, s70, 74752\n\t"
    "v_add_u32 v16, s70, v16\n\t"
    "ds_write_b128 v16, v[176:179] offset:0\n\t"
    "ds_write_b128 v16, v[180:183] offset:1024\n\t"
    "ds_write_b128 v16, v[184:187] offset:2048\n\t"
    "ds_write_b128 v16, v[188:191] offset:3072\n\t"
    "ds_write_b128 v8, v[18:21] offset:0\n\t"
    "ds_write_b128 v8, v[22:25] offset:4096\n\t"
    "ds_write_b128 v8, v[26:29] offset:8192\n\t"
    "v_xor_b32 v34, 0x80008000, v58\n\t"
    "v_xor_b32 v35, 0x80008000, v59\n\t"
    "v_xor_b32 v36, 0x80008000, v60\n\t"
    "v_xor_b32 v37, 0x80008000, v61\n\t"
    "v_xor_b32 v38, 0x80008000, v62\n\t"
    "v_xor_b32 v39, 0x80008000, v63\n\t"
    "v_xor_b32 v40, 0x80008000, v64\n\t"
    "v_xor_b32 v41, 0x80008000, v65\n\t"
    "v_lshrrev_b32 v13, 4, v1\n\t"
    "v_bfe_u32 v14, v1, 2, 2\n\t"
    "v_lshl_add_u32 v14, v13, 3, v14\n\t"
    "v_mul_u32_u24 v14, 96, v14\n\t"
    "v_and_b32 v15, 3, v1\n\t"
    "v_lshl_add_u32 v14, v15, 3, v14\n\t"
    "v_add_u32 v14, 132352, v14\n\t"
    "s_lshl_b32 s70, s64, 4\n\t"
    "v_and_b32 v15, 15, v1\n\t"
    "v_add_u32 v15, s70, v15\n\t"
    "v_mul_u32_u24 v15, 192, v15\n\t"
    "v_lshl_add_u32 v15, v13, 3, v15\n\t"
    "v_add_u32 v15, 151552, v15\n\t"
    "v_cmp_gt_u32 s[74:75], 2, v13\n\t"
    "s_waitcnt lgkmcnt(0)\n\t"
    "s_barrier\n\t"
    "ds_read_b128 v[176:179], v17 offset:0\n\t"
    "ds_read_b128 v[180:183], v17 offset:1024\n\t"
    "ds_read_b128 v[184:187], v17 offset:2048\n\t"
    "ds_read_b128 v[188:191], v17 offset:3072\n\t"
    "ds_read_b64_tr_b16 v[192:193], v14 offset:0\n\t"
    "ds_read_b64_tr_b16 v[194:195], v14 offset:384\n\t"
    "ds_read_b64_tr_b16 v[208:209], v14 offset:32\n\t"
    "ds_read_b64_tr_b16 v[210:211], v14 offset:416\n\t"
    "ds_read_b64_tr_b16 v[76:77], v14 offset:64\n\t"
    "ds_read_b64_tr_b16 v[78:79], v14 offset:448\n\t"
    "ds_read_b64_tr_b16 v[196:197], v14 offset:6144\n\t"
    "ds_read_b64_tr_b16 v[198:199], v14 offset:6528\n\t"
    "ds_read_b64_tr_b16 v[212:213], v14 offset:6176\n\t"
    "ds_read_b64_tr_b16 v[214:215], v14 offset:6560\n\t"
    "ds_read_b64_tr_b16 v[80:81], v14 offset:6208\n\t"
    "ds_read_b64_tr_b16 v[82:83], v14 offset:6592\n\t"
    "ds_read_b64_tr_b16 v[200:201], v14 offset:3072\n\t"
    "ds_read_b64_tr_b16 v[202:203], v14 offset:3456\n\t"
    "ds_read_b64_tr_b16 v[216:217], v14 offset:3104\n\t"
    "ds_read_b64_tr_b16 v[218:219], v14 offset:3488\n\t"
    "ds_read_b64_tr_b16 v[84:85], v14 offset:3136\n\t"
    "ds_read_b64_tr_b16 v[86:87], v14 offset:3520\n\t"
    "ds_read_b64_tr_b16 v[204:205], v14 offset:9216\n\t"
    "ds_read_b64_tr_b16 v[206:207], v14 offset:9600\n\t"
    "ds_read_b64_tr_b16 v[220:221], v14 offset:9248\n\t"
    "ds_read_b64_tr_b16 v[222:223], v14 offset:9632\n\t"
    "ds_read_b64_tr_b16 v[88:89], v14 offset:9280\n\t"
    "ds_read_b64_tr_b16 v[90:91], v14 offset:9664\n\t"
    "s_waitcnt lgkmcnt(15)\n\t"
    "v_add_f32 v160, v176, v160\n\t"
    "v_add_f32 v161, v177, v161\n\t"
    "v_add_f32 v162, v178, v162\n\t"
    "v_add_f32 v163, v179, v163\n\t"
    "v_add_f32 v164, v180, v164\n\t"
    "v_add_f32 v165, v181, v165\n\t"
    "v_add_f32 v166, v182, v166\n\t"
    "v_add_f32 v167, v183, v167\n\t"
    "v_add_f32 v168, v184, v168\n\t"
    "v_add_f32 v169, v185, v169\n\t"
    "v_add_f32 v170, v186, v170\n\t"
    "v_add_f32 v171, v187, v171\n\t"
    "v_add_f32 v172, v188, v172\n\t"
    "v_add_f32 v173, v189, v173\n\t"
    "v_add_f32 v174, v190, v174\n\t"
    "v_add_f32 v175, v191, v175\n\t"
    "v_exp_f32 v66, v160\n\t"
    "v_exp_f32 v67, v161\n\t"
    "v_fma_f32 v66, v66, s78, s78\n\t"
    "v_exp_f32 v68, v162\n\t"
    "v_mfma_f32_16x16x32_bf16 v[144:147], v[192:195], v[50:53], 0\n\t"
    "v_fma_f32 v67, v67, s78, s78\n\t"
    "v_fma_f32 v66, v66, v66, 0.5\n\t"
    "v_exp_f32 v69, v163\n\t"
    "v_fma_f32 v68, v68, s78, s78\n\t"
    "v_mfma_f32_16x16x32_bf16 v[148:151], v[192:195], v[58:61], 0\n\t"
    "v_fma_f32 v67, v67, v67, 0.5\n\t"
    "v_rcp_f32 v66, v66\n\t"
    "v_exp_f32 v70, v164\n\t"
    "v_fma_f32 v69, v69, s78, s78\n\t"
    "v_mfma_f32_16x16x32_bf16 v[152:155], v[208:211], v[50:53], 0\n\t"
    "v_fma_f32 v68, v68, v68, 0.5\n\t"
    "v_rcp_f32 v67, v67\n\t"
    "v_fma_f32 v66, -v160, v66, v160\n\t"
    "v_exp_f32 v71, v165\n\t"
    "v_mfma_f32_16x16x32_bf16 v[156:159], v[208:211], v[58:61], 0\n\t"
    "v_fma_f32 v70, v70, s78, s78\n\t"
    "v_fma_f32 v69, v69, v69, 0.5\n\t"
    "v_rcp_f32 v68, v68\n\t"
    "v_fma_f32 v67, -v161, v67, v161\n\t"
    "v_mfma_f32_16x16x32_bf16 v[132:135], v[76:79], v[50:53], 0\n\t"
    "v_cvt_pk_bf16_f32 v124, v66, v67\n\t"
    "v_exp_f32 v72, v166\n\t"
    "v_fma_f32 v71, v71, s78, s78\n\t"
    "v_fma_f32 v70, v70, v70, 0.5\n\t"
    "v_mfma_f32_16x16x32_bf16 v[136:139], v[76:79], v[58:61], 0\n\t"
    "v_rcp_f32 v69, v69\n\t"
    "v_fma_f32 v68, -v162, v68, v162\n\t"
    "v_exp_f32 v73, v167\n\t"
    "v_fma_f32 v72, v72, s78, s78\n\t"
    "v_mfma_f32_16x16x32_bf16 v[144:147], v[196:199], v[34:37], v[144:147]\n\t"
    "v_fma_f32 v71, v71, v71, 0.5\n\t"
    "v_rcp_f32 v70, v70\n\t"
    "v_fma_f32 v69, -v163, v69, v163\n\t"
    "v_cvt_pk_bf16_f32 v125, v68, v69\n\t"
    "v_mfma_f32_16x16x32_bf16 v[148:151], v[196:199], v[50:53], v[148:151]\n\t"
    "v_exp_f32 v66, v168\n\t"
    "v_fma_f32 v73, v73, s78, s78\n\t"
    "v_fma_f32 v72, v72, v72, 0.5\n\t"
    "v_rcp_f32 v71, v71\n\t"
    "s_waitcnt lgkmcnt(14)\n\t"
    "v_mfma_f32_16x16x32_bf16 v[152:155], v[212:215], v[34:37], v[152:155]\n\t"
    "v_fma_f32 v70, -v164, v70, v164\n\t"
    "v_exp_f32 v67, v169\n\t"
    "v_fma_f32 v66, v66, s78, s78\n\t"
    "v_fma_f32 v73, v73, v73, 0.5\n\t"
    "v_mfma_f32_16x16x32_bf16 v[156:159], v[212:215], v[50:53], v[156:159]\n\t"
    "v_rcp_f32 v72, v72\n\t"
    "v_fma_f32 v71, -v165, v71, v165\n\t"
    "v_cvt_pk_bf16_f32 v126, v70, v71\n\t"
    "v_exp_f32 v68, v170\n\t"
    "s_waitcnt lgkmcnt(12)\n\t"
    "v_mfma_f32_16x16x32_bf16 v[132:135], v[80:83], v[34:37], v[132:135]\n\t"
    "v_fma_f32 v67, v67, s78, s78\n\t"
    "v_fma_f32 v66, v66, v66, 0.5\n\t"
    "v_rcp_f32 v73, v73\n\t"
    "v_fma_f32 v72, -v166, v72, v166\n\t"
    "v_mfma_f32_16x16x32_bf16 v[136:139], v[80:83], v[50:53], v[136:139]\n\t"
    "v_exp_f32 v69, v171\n\t"
    "v_fma_f32 v68, v68, s78, s78\n\t"
    "v_fma_f32 v67, v67, v67, 0.5\n\t"
    "v_rcp_f32 v66, v66\n\t"
    "s_waitcnt lgkmcnt(10)\n\t"
    "v_mfma_f32_16x16x32_bf16 v[144:147], v[200:203], v[54:57], v[144:147]\n\t"
    "v_fma_f32 v73, -v167, v73, v167\n\t"
    "v_cvt_pk_bf16_f32 v127, v72, v73\n\t"
    "v_exp_f32 v70, v172\n\t"
    "v_fma_f32 v69, v69, s78, s78\n\t"
    "v_mfma_f32_16x16x32_bf16 v[148:151], v[200:203], v[62:65], v[148:151]\n\t"
    "v_fma_f32 v68, v68, v68, 0.5\n\t"
    "v_rcp_f32 v67, v67\n\t"
    "v_fma_f32 v66, -v168, v66, v168\n\t"
    "v_exp_f32 v71, v173\n\t"
    "s_waitcnt lgkmcnt(8)\n\t"
    "v_mfma_f32_16x16x32_bf16 v[152:155], v[216:219], v[54:57], v[152:155]\n\t"
    "v_fma_f32 v70, v70, s78, s78\n\t"
    "v_fma_f32 v69, v69, v69, 0.5\n\t"
    "v_rcp_f32 v68, v68\n\t"
    "v_fma_f32 v67, -v169, v67, v169\n\t"
    "v_mfma_f32_16x16x32_bf16 v[156:159], v[216:219], v[62:65], v[156:159]\n\t"
    "v_cvt_pk_bf16_f32 v128, v66, v67\n\t"
    "v_exp_f32 v72, v174\n\t"
    "v_fma_f32 v71, v71, s78, s78\n\t"
    "v_fma_f32 v70, v70, v70, 0.5\n\t"
    "s_waitcnt lgkmcnt(6)\n\t"
    "v_mfma_f32_16x16x32_bf16 v[132:135], v[84:87], v[54:57], v[132:135]\n\t"
    "v_rcp_f32 v69, v69\n\t"
    "v_fma_f32 v68, -v170, v68, v170\n\t"
    "v_exp_f32 v73, v175\n\t"
    "v_fma_f32 v72, v72, s78, s78\n\t"
    "v_mfma_f32_16x16x32_bf16 v[136:139], v[84:87], v[62:65], v[136:139]\n\t"
    "v_fma_f32 v71, v71, v71, 0.5\n\t"
    "v_rcp_f32 v70, v70\n\t"
    "v_fma_f32 v69, -v171, v69, v171\n\t"
    "v_cvt_pk_bf16_f32 v129, v68, v69\n\t"
    "s_waitcnt lgkmcnt(4)\n\t"
    "v_mfma_f32_16x16x32_bf16 v[144:147], v[204:207], v[38:41], v[144:147]\n\t"
    "v_fma_f32 v73, v73, s78, s78\n\t"
    "v_fma_f32 v72, v72, v72, 0.5\n\t"
    "v_rcp_f32 v71, v71\n\t"
    "v_fma_f32 v70, -v172, v70, v172\n\t"
    "v_mfma_f32_16x16x32_bf16 v[148:151], v[204:207], v[54:57], v[148:151]\n\t"
    "v_fma_f32 v73, v73, v73, 0.5\n\t"
    "v_rcp_f32 v72, v72\n\t"
    "v_fma_f32 v71, -v173, v71, v173\n\t"
    "v_cvt_pk_bf16_f32 v130, v70, v71\n\t"
    "s_waitcnt lgkmcnt(2)\n\t"
    "v_mfma_f32_16x16x32_bf16 v[152:155], v[220:223], v[38:41], v[152:155]\n\t"
    "v_rcp_f32 v73, v73\n\t"
    "v_fma_f32 v72, -v174, v72, v174\n\t"
    "v_fma_f32 v73, -v175, v73, v175\n\t"
    "v_cvt_pk_bf16_f32 v131, v72, v73\n\t"
    "v_mfma_f32_16x16x32_bf16 v[156:159], v[220:223], v[54:57], v[156:159]\n\t"
    "s_waitcnt lgkmcnt(0)\n\t"
    "v_mfma_f32_16x16x32_bf16 v[132:135], v[88:91], v[38:41], v[132:135]\n\t"
    "v_mfma_f32_16x16x32_bf16 v[136:139], v[88:91], v[54:57], v[136:139]\n\t"
    "v_add_u32 v74, s82, v7\n\t"
    "ds_write_b64 v74, v[108:109] offset:0\n\t"
    "ds_write_b64 v74, v[110:111] offset:16\n\t"
    "ds_write_b64 v74, v[112:113] offset:32\n\t"
    "ds_write_b64 v74, v[114:115] offset:48\n\t"
    "ds_write_b64 v74, v[116:117] offset:37376\n\t"
    "ds_write_b64 v74, v[118:119] offset:37392\n\t"
    "ds_write_b64 v74, v[120:121] offset:37408\n\t"
    "ds_write_b64 v74, v[122:123] offset:37424\n\t"
    "v_cvt_pk_bf16_f32 v92, v144, v145\n\t"
    "v_cvt_pk_bf16_f32 v93, v146, v147\n\t"
    "ds_write_b64 v15, v[92:93] offset:0\n\t"
    "v_cvt_pk_bf16_f32 v94, v148, v149\n\t"
    "v_cvt_pk_bf16_f32 v95, v150, v151\n\t"
    "ds_write_b64 v15, v[94:95] offset:96\n\t"
    "v_cvt_pk_bf16_f32 v96, v152, v153\n\t"
    "v_cvt_pk_bf16_f32 v97, v154, v155\n\t"
    "ds_write_b64 v15, v[96:97] offset:32\n\t"
    "v_cvt_pk_bf16_f32 v98, v156, v157\n\t"
    "v_cvt_pk_bf16_f32 v99, v158, v159\n\t"
    "ds_write_b64 v15, v[98:99] offset:128\n\t"
    "v_cvt_pk_bf16_f32 v100, v132, v133\n\t"
    "v_cvt_pk_bf16_f32 v101, v134, v135\n\t"
    "v_cndmask_b32 v100, 0, v100, s[74:75]\n\t"
    "v_cndmask_b32 v101, 0, v101, s[74:75]\n\t"
    "ds_write_b64 v15, v[100:101] offset:64\n\t"
    "v_cvt_pk_bf16_f32 v102, v136, v137\n\t"
    "v_cvt_pk_bf16_f32 v103, v138, v139\n\t"
    "v_cndmask_b32 v102, 0, v102, s[74:75]\n\t"
    "v_cndmask_b32 v103, 0, v103, s[74:75]\n\t"
    "ds_write_b64 v15, v[102:103] offset:160\n\t"
    "v_add_u32 v74, s83, v7\n\t"
    "ds_write_b64 v74, v[124:125] offset:0\n\t"
    "ds_write_b64 v74, v[126:127] offset:16\n\t"
    "ds_write_b64 v74, v[128:129] offset:32\n\t"
    "ds_write_b64 v74, v[130:131] offset:48\n\t"
    "s_waitcnt lgkmcnt(0)\n\t"
    "s_branch L_p1end_11%=\n\t"
    "L_p1k1_10%=:\n\t"
    "s_waitcnt vmcnt(8) lgkmcnt(2)\n\t"
    "v_mfma_f32_32x32x16_bf16 v[160:175], v[132:135], a[144:147], 0\n\t"
    "ds_read_b64_tr_b16 v[140:141], v5 offset:52224\n\t"
    "buffer_load_dwordx4 v4, s[44:47], s68 offen offset:2048 lds\n\t"
    "s_add_u32 s68, s68, 442368\n\t"
    "ds_read_b64_tr_b16 v[142:143], v5 offset:52480\n\t"
    "v_mfma_f32_32x32x16_bf16 v[176:191], v[132:135], a[148:151], 0\n\t"
    "s_waitcnt vmcnt(8) lgkmcnt(2)\n\t"
    "v_mfma_f32_32x32x16_bf16 v[160:175], v[136:139], a[152:155], v[160:175]\n\t"
    "s_add_u32 m0, s69, 50176\n\t"
    "ds_read_b64_tr_b16 v[132:133], v5 offset:53248\n\t"
    "buffer_load_dwordx4 v2, s[44:47], s68 offen offset:0 lds\n\t"
    "ds_read_b64_tr_b16 v[134:135], v5 offset:53504\n\t"
    "v_exp_f32 v66, v192\n\t"
    "v_exp_f32 v67, v193\n\t"
    "v_mfma_f32_32x32x16_bf16 v[176:191], v[136:139], a[156:159], v[176:191]\n\t"
    "v_fma_f32 v66, v66, s78, s78\n\t"
    "v_exp_f32 v68, v194\n\t"
    "v_fma_f32 v67, v67, s78, s78\n\t"
    "v_fma_f32 v66, v66, v66, 0.5\n\t"
    "v_exp_f32 v69, v195\n\t"
    "v_fma_f32 v68, v68, s78, s78\n\t"
    "v_fma_f32 v67, v67, v67, 0.5\n\t"
    "s_waitcnt vmcnt(8) lgkmcnt(2)\n\t"
    "v_mfma_f32_32x32x16_bf16 v[160:175], v[140:143], a[160:163], v[160:175]\n\t"
    "ds_read_b64_tr_b16 v[136:137], v5 offset:54272\n\t"
    "buffer_load_dwordx4 v3, s[44:47], s68 offen offset:1024 lds\n\t"
    "ds_read_b64_tr_b16 v[138:139], v5 offset:54528\n\t"
    "v_rcp_f32 v66, v66\n\t"
    "v_exp_f32 v70, v196\n\t"
    "v_fma_f32 v69, v69, s78, s78\n\t"
    "v_mfma_f32_32x32x16_bf16 v[176:191], v[140:143], a[164:167], v[176:191]\n\t"
    "v_fma_f32 v68, v68, v68, 0.5\n\t"
    "v_rcp_f32 v67, v67\n\t"
    "v_fma_f32 v66, -v192, v66, v192\n\t"
    "v_exp_f32 v71, v197\n\t"
    "v_fma_f32 v70, v70, s78, s78\n\t"
    "v_fma_f32 v69, v69, v69, 0.5\n\t"
    "v_rcp_f32 v68, v68\n\t"
    "v_mov_b32 v16, 145676\n\t"
    "s_cmp_lg_u32 s64, 0\n\t"
    "s_cbranch_scc1 L_nopoll_17%=\n\t"
    "v_and_b32 v15, 31, v1\n\t"
    "v_lshlrev_b32 v15, 2, v15\n\t"
    "s_lshl_b32 s71, s66, 7\n\t"
    "s_add_u32 s71, s71, 1280\n\t"
    "v_cmp_ne_u32 vcc, s67, v11\n\t"
    "s_mov_b32 s94, 0x40000\n\t"
    "s_cbranch_vccz L_ok_18%=\n\t"
    "L_spin_19%=:\n\t"
    "s_sleep 1\n\t"
    "buffer_load_dword v11, v15, s[40:43], s71 offen sc1\n\t"
    "s_waitcnt vmcnt(0)\n\t"
    "v_cmp_ne_u32 vcc, s67, v11\n\t"
    "s_sub_u32 s94, s94, 1\n\t"
    "s_nop 3\n\t"
    "s_cbranch_vccz L_ok_18%=\n\t"
    "s_cmp_lg_u32 s94, 0\n\t"
    "s_cbranch_scc1 L_spin_19%=\n\t"
    "L_ok_18%=:\n\t"
    "v_mov_b32 v15, 1\n\t"
    "ds_write_b32 v16, v15\n\t"
    "L_nopoll_17%=:\n\t"
    "s_mov_b32 s94, 0x400000\n\t"
    "L_ldsspin_20%=:\n\t"
    "ds_read_b32 v15, v16\n\t"
    "s_waitcnt lgkmcnt(0)\n\t"
    "v_cmp_ne_u32 vcc, 1, v15\n\t"
    "s_sub_u32 s94, s94, 1\n\t"
    "s_nop 3\n\t"
    "s_cbranch_vccz L_ldsok_21%=\n\t"
    "s_cmp_lg_u32 s94, 0\n\t"
    "s_cbranch_scc1 L_ldsspin_20%=\n\t"
    "L_ldsok_21%=:\n\t"
    "s_lshl_b32 s70, s66, 5\n\t"
    "s_add_u32 s70, s70, s65\n\t"
    "s_mul_i32 s70, s70, 12288\n\t"
    "s_add_u32 s70, s70, 3275520\n\t"
    "buffer_load_dwordx4 v[18:21], v8, s[40:43], s70 offen sc1\n\t"
    "s_add_u32 s70, s70, 4096\n\t"
    "buffer_load_dwordx4 v[22:25], v8, s[40:43], s70 offen sc1\n\t"
    "s_add_u32 s70, s70, 4096\n\t"
    "buffer_load_dwordx4 v[26:29], v8, s[40:43], s70 offen sc1\n\t"
    "s_waitcnt vmcnt(11)\n\t"
    "v_mfma_f32_32x32x16_bf16 v[160:175], v[132:135], a[168:171], v[160:175]\n\t"
    "ds_read_b64_tr_b16 v[140:141], v5 offset:55296\n\t"
    "buffer_load_dwordx4 v4, s[44:47], s68 offen offset:2048 lds\n\t"
    "s_add_u32 s68, s68, 442368\n\t"
    "ds_read_b64_tr_b16 v[142:143], v5 offset:55552\n\t"
    "v_fma_f32 v67, -v193, v67, v193\n\t"
    "v_cvt_pk_bf16_f32 v108, v66, v67\n\t"
    "v_exp_f32 v72, v198\n\t"
    "v_mfma_f32_32x32x16_bf16 v[176:191], v[132:135], a[172:175], v[176:191]\n\t"
    "v_fma_f32 v71, v71, s78, s78\n\t"
    "v_fma_f32 v70, v70, v70, 0.5\n\t"
    "v_rcp_f32 v69, v69\n\t"
    "v_fma_f32 v68, -v194, v68, v194\n\t"
    "v_exp_f32 v73, v199\n\t"
    "v_fma_f32 v72, v72, s78, s78\n\t"
    "v_fma_f32 v71, v71, v71, 0.5\n\t"
    "s_waitcnt vmcnt(11)\n\t"
    "v_mfma_f32_32x32x16_bf16 v[160:175], v[136:139], a[176:179], v[160:175]\n\t"
    "s_add_u32 m0, s69, 53248\n\t"
    "ds_read_b64_tr_b16 v[132:133], v5 offset:56320\n\t"
    "buffer_load_dwordx4 v2, s[44:47], s68 offen offset:0 lds\n\t"
    "ds_read_b64_tr_b16 v[134:135], v5 offset:56576\n\t"
    "v_rcp_f32 v70, v70\n\t"
    "v_fma_f32 v69, -v195, v69, v195\n\t"
    "v_cvt_pk_bf16_f32 v109, v68, v69\n\t"
    "v_exp_f32 v66, v200\n\t"
    "v_mfma_f32_32x32x16_bf16 v[176:191], v[136:139], a[180:183], v[176:191]\n\t"
    "v_fma_f32 v73, v73, s78, s78\n\t"
    "v_fma_f32 v72, v72, v72, 0.5\n\t"
    "v_rcp_f32 v71, v71\n\t"
    "v_fma_f32 v70, -v196, v70, v196\n\t"
    "v_exp_f32 v67, v201\n\t"
    "v_fma_f32 v66, v66, s78, s78\n\t"
    "v_fma_f32 v73, v73, v73, 0.5\n\t"
    "s_waitcnt vmcnt(11) lgkmcnt(2)\n\t"
    "v_mfma_f32_32x32x16_bf16 v[160:175], v[140:143], a[184:187], v[160:175]\n\t"
    "ds_read_b64_tr_b16 v[136:137], v5 offset:57344\n\t"
    "buffer_load_dwordx4 v3, s[44:47], s68 offen offset:1024 lds\n\t"
    "ds_read_b64_tr_b16 v[138:139], v5 offset:57600\n\t"
    "v_rcp_f32 v72, v72\n\t"
    "v_fma_f32 v71, -v197, v71, v197\n\t"
    "v_cvt_pk_bf16_f32 v110, v70, v71\n\t"
    "v_mfma_f32_32x32x16_bf16 v[176:191], v[140:143], a[188:191], v[176:191]\n\t"
    "v_exp_f32 v68, v202\n\t"
    "v_fma_f32 v67, v67, s78, s78\n\t"
    "v_fma_f32 v66, v66, v66, 0.5\n\t"
    "v_rcp_f32 v73, v73\n\t"
    "v_fma_f32 v72, -v198, v72, v198\n\t"
    "v_exp_f32 v69, v203\n\t"
    "v_fma_f32 v68, v68, s78, s78\n\t"
    "v_fma_f32 v67, v67, v67, 0.5\n\t"
    "s_waitcnt vmcnt(11) lgkmcnt(2)\n\t"
    "v_mfma_f32_32x32x16_bf16 v[160:175], v[132:135], a[192:195], v[160:175]\n\t"
    "ds_read_b64_tr_b16 v[140:141], v5 offset:58368\n\t"
    "buffer_load_dwordx4 v4, s[44:47], s68 offen offset:2048 lds\n\t"
    "ds_read_b64_tr_b16 v[142:143], v5 offset:58624\n\t"
    "v_rcp_f32 v66, v66\n\t"
    "v_fma_f32 v73, -v199, v73, v199\n\t"
    "v_cvt_pk_bf16_f32 v111, v72, v73\n\t"
    "v_mfma_f32_32x32x16_bf16 v[176:191], v[132:135], a[196:199], v[176:191]\n\t"
    "v_exp_f32 v70, v204\n\t"
    "v_fma_f32 v69, v69, s78, s78\n\t"
    "v_fma_f32 v68, v68, v68, 0.5\n\t"
    "v_rcp_f32 v67, v67\n\t"
    "v_fma_f32 v66, -v200, v66, v200\n\t"
    "v_exp_f32 v71, v205\n\t"
    "v_fma_f32 v70, v70, s78, s78\n\t"
    "s_waitcnt vmcnt(11) lgkmcnt(2)\n\t"
    "v_mfma_f32_32x32x16_bf16 v[160:175], v[136:139], a[200:203], v[160:175]\n\t"
    "ds_read_b64_tr_b16 v[132:133], v5 offset:59392\n\t"
    "ds_read_b64_tr_b16 v[134:135], v5 offset:59648\n\t"
    "v_fma_f32 v69, v69, v69, 0.5\n\t"
    "v_rcp_f32 v68, v68\n\t"
    "v_fma_f32 v67, -v201, v67, v201\n\t"
    "v_mfma_f32_32x32x16_bf16 v[176:191], v[136:139], a[204:207], v[176:191]\n\t"
    "v_cvt_pk_bf16_f32 v112, v66, v67\n\t"
    "v_exp_f32 v72, v206\n\t"
    "v_fma_f32 v71, v71, s78, s78\n\t"
    "v_fma_f32 v70, v70, v70, 0.5\n\t"
    "v_rcp_f32 v69, v69\n\t"
    "v_fma_f32 v68, -v202, v68, v202\n\t"
    "v_exp_f32 v73, v207\n\t"
    "s_waitcnt vmcnt(10) lgkmcnt(2)\n\t"
    "v_mfma_f32_32x32x16_bf16 v[160:175], v[140:143], a[208:211], v[160:175]\n\t"
    "ds_read_b64_tr_b16 v[136:137], v5 offset:60416\n\t"
    "ds_read_b64_tr_b16 v[138:139], v5 offset:60672\n\t"
    "v_fma_f32 v72, v72, s78, s78\n\t"
    "v_fma_f32 v71, v71, v71, 0.5\n\t"
    "v_rcp_f32 v70, v70\n\t"
    "v_mfma_f32_32x32x16_bf16 v[176:191], v[140:143], a[212:215], v[176:191]\n\t"
    "v_fma_f32 v69, -v203, v69, v203\n\t"
    "v_cvt_pk_bf16_f32 v113, v68, v69\n\t"
    "v_exp_f32 v66, v208\n\t"
    "v_fma_f32 v73, v73, s78, s78\n\t"
    "v_fma_f32 v72, v72, v72, 0.5\n\t"
    "v_rcp_f32 v71, v71\n\t"
    "v_fma_f32 v70, -v204, v70, v204\n\t"
    "v_add_u32 v74, s79, v7\n\t"
    "ds_write_b64 v74, v[34:35] offset:0\n\t"
    "ds_write_b64 v74, v[36:37] offset:16\n\t"
    "ds_write_b64 v74, v[38:39] offset:32\n\t"
    "ds_write_b64 v74, v[40:41] offset:48\n\t"
    "ds_write_b64 v74, v[42:43] offset:37376\n\t"
    "ds_write_b64 v74, v[44:45] offset:37392\n\t"
    "ds_write_b64 v74, v[46:47] offset:37408\n\t"
    "ds_write_b64 v74, v[48:49] offset:37424\n\t"
    "s_waitcnt vmcnt(9) lgkmcnt(10)\n\t"
    "v_mfma_f32_32x32x16_bf16 v[160:175], v[132:135], a[216:219], v[160:175]\n\t"
    "ds_read_b64_tr_b16 v[140:141], v5 offset:61440\n\t"
    "ds_read_b64_tr_b16 v[142:143], v5 offset:61696\n\t"
    "v_exp_f32 v67, v209\n\t"
    "v_fma_f32 v66, v66, s78, s78\n\t"
    "v_fma_f32 v73, v73, v73, 0.5\n\t"
    "v_mfma_f32_32x32x16_bf16 v[176:191], v[132:135], a[220:223], v[176:191]\n\t"
    "v_rcp_f32 v72, v72\n\t"
    "v_fma_f32 v71, -v205, v71, v205\n\t"
    "v_cvt_pk_bf16_f32 v114, v70, v71\n\t"
    "v_exp_f32 v68, v210\n\t"
    "v_fma_f32 v67, v67, s78, s78\n\t"
    "v_fma_f32 v66, v66, v66, 0.5\n\t"
    "v_rcp_f32 v73, v73\n\t"
    "v_fma_f32 v72, -v206, v72, v206\n\t"
    "s_waitcnt vmcnt(8) lgkmcnt(10)\n\t"
    "v_mfma_f32_32x32x16_bf16 v[160:175], v[136:139], a[224:227], v[160:175]\n\t"
    "ds_read_b64_tr_b16 v[132:133], v5 offset:50176\n\t"
    "ds_read_b64_tr_b16 v[134:135], v5 offset:50432\n\t"
    "v_exp_f32 v69, v211\n\t"
    "v_fma_f32 v68, v68, s78, s78\n\t"
    "v_fma_f32 v67, v67, v67, 0.5\n\t"
    "v_mfma_f32_32x32x16_bf16 v[176:191], v[136:139], a[228:231], v[176:191]\n\t"
    "v_rcp_f32 v66, v66\n\t"
    "v_fma_f32 v73, -v207, v73, v207\n\t"
    "v_cvt_pk_bf16_f32 v115, v72, v73\n\t"
    "v_exp_f32 v70, v212\n\t"
    "v_fma_f32 v69, v69, s78, s78\n\t"
    "v_fma_f32 v68, v68, v68, 0.5\n\t"
    "v_rcp_f32 v67, v67\n\t"
    "s_waitcnt vmcnt(7) lgkmcnt(2)\n\t"
    "v_mfma_f32_32x32x16_bf16 v[160:175], v[140:143], a[232:235], v[160:175]\n\t"
    "ds_read_b64_tr_b16 v[136:137], v5 offset:51200\n\t"
    "ds_read_b64_tr_b16 v[138:139], v5 offset:51456\n\t"
    "v_fma_f32 v66, -v208, v66, v208\n\t"
    "v_exp_f32 v71, v213\n\t"
    "v_fma_f32 v70, v70, s78, s78\n\t"
    "v_mfma_f32_32x32x16_bf16 v[176:191], v[140:143], a[236:239], v[176:191]\n\t"
    "v_fma_f32 v69, v69, v69, 0.5\n\t"
    "v_rcp_f32 v68, v68\n\t"
    "v_fma_f32 v67, -v209, v67, v209\n\t"
    "v_cvt_pk_bf16_f32 v116, v66, v67\n\t"
    "v_exp_f32 v72, v214\n\t"
    "v_fma_f32 v71, v71, s78, s78\n\t"
    "v_fma_f32 v70, v70, v70, 0.5\n\t"
    "v_add_u32 v74, s80, v7\n\t"
    "ds_write_b64 v74, v[76:77] offset:0\n\t"
    "ds_write_b64 v74, v[78:79] offset:16\n\t"
    "ds_write_b64 v74, v[80:81] offset:32\n\t"
    "ds_write_b64 v74, v[82:83] offset:48\n\t"
    "ds_write_b64 v74, v[84:85] offset:37376\n\t"
    "ds_write_b64 v74, v[86:87] offset:37392\n\t"
    "ds_write_b64 v74, v[88:89] offset:37408\n\t"
    "ds_write_b64 v74, v[90:91] offset:37424\n\t"
    "s_waitcnt vmcnt(3) lgkmcnt(10)\n\t"
    "v_mfma_f32_32x32x16_bf16 v[160:175], v[132:135], a[240:243], v[160:175]\n\t"
    "ds_read_b64_tr_b16 v[140:141], v5 offset:52224\n\t"
    "ds_read_b64_tr_b16 v[142:143], v5 offset:52480\n\t"
    "v_rcp_f32 v69, v69\n\t"
    "v_fma_f32 v68, -v210, v68, v210\n\t"
    "v_exp_f32 v73, v215\n\t"
    "v_fma_f32 v72, v72, s78, s78\n\t"
    "v_mfma_f32_32x32x16_bf16 v[176:191], v[132:135], a[244:247], v[176:191]\n\t"
    "v_fma_f32 v71, v71, v71, 0.5\n\t"
    "v_rcp_f32 v70, v70\n\t"
    "v_fma_f32 v69, -v211, v69, v211\n\t"
    "v_cvt_pk_bf16_f32 v117, v68, v69\n\t"
    "v_exp_f32 v66, v216\n\t"
    "v_fma_f32 v73, v73, s78, s78\n\t"
    "v_fma_f32 v72, v72, v72, 0.5\n\t"
    "s_waitcnt vmcnt(2) lgkmcnt(10)\n\t"
    "v_mfma_f32_32x32x16_bf16 v[160:175], v[136:139], a[248:251], v[160:175]\n\t"
    "ds_read_b64_tr_b16 v[132:133], v5 offset:53248\n\t"
    "ds_read_b64_tr_b16 v[134:135], v5 offset:53504\n\t"
    "v_rcp_f32 v71, v71\n\t"
    "v_fma_f32 v70, -v212, v70, v212\n\t"
    "v_exp_f32 v67, v217\n\t"
    "v_mfma_f32_32x32x16_bf16 v[176:191], v[136:139], a[252:255], v[176:191]\n\t"
    "v_fma_f32 v66, v66, s78, s78\n\t"
    "v_fma_f32 v73, v73, v73, 0.5\n\t"
    "v_rcp_f32 v72, v72\n\t"
    "v_fma_f32 v71, -v213, v71, v213\n\t"
    "v_cvt_pk_bf16_f32 v118, v70, v71\n\t"
    "v_exp_f32 v68, v218\n\t"
    "v_fma_f32 v67, v67, s78, s78\n\t"
    "v_fma_f32 v66, v66, v66, 0.5\n\t"
    "s_waitcnt vmcnt(1) lgkmcnt(2)\n\t"
    "v_mfma_f32_32x32x16_bf16 v[160:175], v[140:143], v[224:227], v[160:175]\n\t"
    "ds_read_b64_tr_b16 v[136:137], v5 offset:54272\n\t"
    "ds_read_b64_tr_b16 v[138:139], v5 offset:54528\n\t"
    "v_rcp_f32 v73, v73\n\t"
    "v_fma_f32 v72, -v214, v72, v214\n\t"
    "v_mfma_f32_32x32x16_bf16 v[176:191], v[140:143], v[228:231], v[176:191]\n\t"
    "v_exp_f32 v69, v219\n\t"
    "v_fma_f32 v68, v68, s78, s78\n\t"
    "v_fma_f32 v67, v67, v67, 0.5\n\t"
    "v_rcp_f32 v66, v66\n\t"
    "v_fma_f32 v73, -v215, v73, v215\n\t"
    "v_cvt_pk_bf16_f32 v119, v72, v73\n\t"
    "v_exp_f32 v70, v220\n\t"
    "v_fma_f32 v69, v69, s78, s78\n\t"
    "v_add_u32 v74, s81, v7\n\t"
    "ds_write_b64 v74, v[92:93] offset:0\n\t"
    "ds_write_b64 v74, v[94:95] offset:16\n\t"
    "ds_write_b64 v74, v[96:97] offset:32\n\t"
    "ds_write_b64 v74, v[98:99] offset:48\n\t"
    "ds_write_b64 v74, v[100:101] offset:37376\n\t"
    "ds_write_b64 v74, v[102:103] offset:37392\n\t"
    "ds_write_b64 v74, v[104:105] offset:37408\n\t"
    "ds_write_b64 v74, v[106:107] offset:37424\n\t"
    "s_waitcnt vmcnt(0) lgkmcnt(10)\n\t"
    "v_mfma_f32_32x32x16_bf16 v[160:175], v[132:135], v[232:235], v[160:175]\n\t"
    "ds_read_b64_tr_b16 v[140:141], v5 offset:55296\n\t"
    "ds_read_b64_tr_b16 v[142:143], v5 offset:55552\n\t"
    "v_fma_f32 v68, v68, v68, 0.5\n\t"
    "v_rcp_f32 v67, v67\n\t"
    "v_fma_f32 v66, -v216, v66, v216\n\t"
    "v_mfma_f32_32x32x16_bf16 v[176:191], v[132:135], v[236:239], v[176:191]\n\t"
    "v_exp_f32 v71, v221\n\t"
    "v_fma_f32 v70, v70, s78, s78\n\t"
    "v_fma_f32 v69, v69, v69, 0.5\n\t"
    "v_rcp_f32 v68, v68\n\t"
    "v_fma_f32 v67, -v217, v67, v217\n\t"
    "v_cvt_pk_bf16_f32 v120, v66, v67\n\t"
    "v_exp_f32 v72, v222\n\t"
    "s_waitcnt lgkmcnt(10)\n\t"
    "v_mfma_f32_32x32x16_bf16 v[160:175], v[136:139], v[240:243], v[160:175]\n\t"
    "v_fma_f32 v71, v71, s78, s78\n\t"
    "v_fma_f32 v70, v70, v70, 0.5\n\t"
    "v_rcp_f32 v69, v69\n\t"
    "v_mfma_f32_32x32x16_bf16 v[176:191], v[136:139], v[244:247], v[176:191]\n\t"
    "v_fma_f32 v68, -v218, v68, v218\n\t"
    "v_exp_f32 v73, v223\n\t"
    "v_fma_f32 v72, v72, s78, s78\n\t"
    "v_fma_f32 v71, v71, v71, 0.5\n\t"
    "v_rcp_f32 v70, v70\n\t"
    "v_fma_f32 v69, -v219, v69, v219\n\t"
    "v_cvt_pk_bf16_f32 v121, v68, v69\n\t"
    "v_fma_f32 v73, v73, s78, s78\n\t"
    "s_waitcnt lgkmcnt(0)\n\t"
    "v_mfma_f32_32x32x16_bf16 v[160:175], v[140:143], v[248:251], v[160:175]\n\t"
    "v_fma_f32 v72, v72, v72, 0.5\n\t"
    "v_rcp_f32 v71, v71\n\t"
    "v_fma_f32 v70, -v220, v70, v220\n\t"
    "v_mfma_f32_32x32x16_bf16 v[176:191], v[140:143], v[252:255], v[176:191]\n\t"
    "v_fma_f32 v73, v73, v73, 0.5\n\t"
    "v_rcp_f32 v72, v72\n\t"
    "v_fma_f32 v71, -v221, v71, v221\n\t"
    "v_cvt_pk_bf16_f32 v122, v70, v71\n\t"
    "v_rcp_f32 v73, v73\n\t"
    "v_fma_f32 v72, -v222, v72, v222\n\t"
    "v_fma_f32 v73, -v223, v73, v223\n\t"
    "v_cvt_pk_bf16_f32 v123, v72, v73\n\t"
    "v_lshlrev_b32 v16, 4, v1\n\t"
    "s_xor_b32 s70, s64, 1\n\t"
    "s_mul_i32 s70, s70, 12288\n\t"
    "s_add_u32 s70, s70, 74752\n\t"
    "v_add_u32 v17, s70, v16\n\t"
    "s_mul_i32 s70, s64, 12288\n\t"
    "s_add_u32 s70, s70, 74752\n\t"
    "v_add_u32 v16, s70, v16\n\t"
    "ds_write_b128 v16, v[160:163] offset:0\n\t"
    "ds_write_b128 v16, v[164:167] offset:1024\n\t"
    "ds_write_b128 v16, v[168:171] offset:2048\n\t"
    "ds_write_b128 v16, v[172:175] offset:3072\n\t"
    "ds_write_b128 v8, v[18:21] offset:0\n\t"
    "ds_write_b128 v8, v[22:25] offset:4096\n\t"
    "ds_write_b128 v8, v[26:29] offset:8192\n\t"
    "v_xor_b32 v34, 0x80008000, v58\n\t"
    "v_xor_b32 v35, 0x80008000, v59\n\t"
    "v_xor_b32 v36, 0x80008000, v60\n\t"
    "v_xor_b32 v37, 0x80008000, v61\n\t"
    "v_xor_b32 v38, 0x80008000, v62\n\t"
    "v_xor_b32 v39, 0x80008000, v63\n\t"
    "v_xor_b32 v40, 0x80008000, v64\n\t"
    "v_xor_b32 v41, 0x80008000, v65\n\t"
    "v_lshrrev_b32 v13, 4, v1\n\t"
    "v_bfe_u32 v14, v1, 2, 2\n\t"
    "v_lshl_add_u32 v14, v13, 3, v14\n\t"
    "v_mul_u32_u24 v14, 96, v14\n\t"
    "v_and_b32 v15, 3, v1\n\t"
    "v_lshl_add_u32 v14, v15, 3, v14\n\t"
    "v_add_u32 v14, 132352, v14\n\t"
    "s_lshl_b32 s70, s64, 4\n\t"
    "v_and_b32 v15, 15, v1\n\t"
    "v_add_u32 v15, s70, v15\n\t"
    "v_mul_u32_u24 v15, 192, v15\n\t"
    "v_lshl_add_u32 v15, v13, 3, v15\n\t"
    "v_add_u32 v15, 151552, v15\n\t"
    "v_cmp_gt_u32 s[74:75], 2, v13\n\t"
    "s_waitcnt lgkmcnt(0)\n\t"
    "s_barrier\n\t"
    "ds_read_b128 v[160:163], v17 offset:0\n\t"
    "ds_read_b128 v[164:167], v17 offset:1024\n\t"
    "ds_read_b128 v[168:171], v17 offset:2048\n\t"
    "ds_read_b128 v[172:175], v17 offset:3072\n\t"
    "ds_read_b64_tr_b16 v[192:193], v14 offset:0\n\t"
    "ds_read_b64_tr_b16 v[194:195], v14 offset:384\n\t"
    "ds_read_b64_tr_b16 v[208:209], v14 offset:32\n\t"
    "ds_read_b64_tr_b16 v[210:211], v14 offset:416\n\t"
    "ds_read_b64_tr_b16 v[76:77], v14 offset:64\n\t"
    "ds_read_b64_tr_b16 v[78:79], v14 offset:448\n\t"
    "ds_read_b64_tr_b16 v[196:197], v14 offset:6144\n\t"
    "ds_read_b64_tr_b16 v[198:199], v14 offset:6528\n\t"
    "ds_read_b64_tr_b16 v[212:213], v14 offset:6176\n\t"
    "ds_read_b64_tr_b16 v[214:215], v14 offset:6560\n\t"
    "ds_read_b64_tr_b16 v[80:81], v14 offset:6208\n\t"
    "ds_read_b64_tr_b16 v[82:83], v14 offset:6592\n\t"
    "ds_read_b64_tr_b16 v[200:201], v14 offset:3072\n\t"
    "ds_read_b64_tr_b16 v[202:203], v14 offset:3456\n\t"
    "ds_read_b64_tr_b16 v[216:217], v14 offset:3104\n\t"
    "ds_read_b64_tr_b16 v[218:219], v14 offset:3488\n\t"
    "ds_read_b64_tr_b16 v[84:85], v14 offset:3136\n\t"
    "ds_read_b64_tr_b16 v[86:87], v14 offset:3520\n\t"
    "ds_read_b64_tr_b16 v[204:205], v14 offset:9216\n\t"
    "ds_read_b64_tr_b16 v[206:207], v14 offset:9600\n\t"
    "ds_read_b64_tr_b16 v[220:221], v14 offset:9248\n\t"
    "ds_read_b64_tr_b16 v[222:223], v14 offset:9632\n\t"
    "ds_read_b64_tr_b16 v[88:89], v14 offset:9280\n\t"
    "ds_read_b64_tr_b16 v[90:91], v14 offset:9664\n\t"
    "s_waitcnt lgkmcnt(15)\n\t"
    "v_add_f32 v176, v160, v176\n\t"
    "v_add_f32 v177, v161, v177\n\t"
    "v_add_f32 v178, v162, v178\n\t"
    "v_add_f32 v179, v163, v179\n\t"
    "v_add_f32 v180, v164, v180\n\t"
    "v_add_f32 v181, v165, v181\n\t"
    "v_add_f32 v182, v166, v182\n\t"
    "v_add_f32 v183, v167, v183\n\t"
    "v_add_f32 v184, v168, v184\n\t"
    "v_add_f32 v185, v169, v185\n\t"
    "v_add_f32 v186, v170, v186\n\t"
    "v_add_f32 v187, v171, v187\n\t"
    "v_add_f32 v188, v172, v188\n\t"
    "v_add_f32 v189, v173, v189\n\t"
    "v_add_f32 v190, v174, v190\n\t"
    "v_add_f32 v191, v175, v191\n\t"
    "v_exp_f32 v66, v176\n\t"
    "v_exp_f32 v67, v177\n\t"
    "v_fma_f32 v66, v66, s78, s78\n\t"
    "v_exp_f32 v68, v178\n\t"
    "v_mfma_f32_16x16x32_bf16 v[144:147], v[192:195], v[50:53], 0\n\t"
    "v_fma_f32 v67, v67, s78, s78\n\t"
    "v_fma_f32 v66, v66, v66, 0.5\n\t"
    "v_exp_f32 v69, v179\n\t"
    "v_fma_f32 v68, v68, s78, s78\n\t"
    "v_mfma_f32_16x16x32_bf16 v[148:151], v[192:195], v[58:61], 0\n\t"
    "v_fma_f32 v67, v67, v67, 0.5\n\t"
    "v_rcp_f32 v66, v66\n\t"
    "v_exp_f32 v70, v180\n\t"
    "v_fma_f32 v69, v69, s78, s78\n\t"
    "v_mfma_f32_16x16x32_bf16 v[152:155], v[208:211], v[50:53], 0\n\t"
    "v_fma_f32 v68, v68, v68, 0.5\n\t"
    "v_rcp_f32 v67, v67\n\t"
    "v_fma_f32 v66, -v176, v66, v176\n\t"
    "v_exp_f32 v71, v181\n\t"
    "v_mfma_f32_16x16x32_bf16 v[156:159], v[208:211], v[58:61], 0\n\t"
    "v_fma_f32 v70, v70, s78, s78\n\t"
    "v_fma_f32 v69, v69, v69, 0.5\n\t"
    "v_rcp_f32 v68, v68\n\t"
    "v_fma_f32 v67, -v177, v67, v177\n\t"
    "v_mfma_f32_16x16x32_bf16 v[132:135], v[76:79], v[50:53], 0\n\t"
    "v_cvt_pk_bf16_f32 v124, v66, v67\n\t"
    "v_exp_f32 v72, v182\n\t"
    "v_fma_f32 v71, v71, s78, s78\n\t"
    "v_fma_f32 v70, v70, v70, 0.5\n\t"
    "v_mfma_f32_16x16x32_bf16 v[136:139], v[76:79], v[58:61], 0\n\t"
    "v_rcp_f32 v69, v69\n\t"
    "v_fma_f32 v68, -v178, v68, v178\n\t"
    "v_exp_f32 v73, v183\n\t"
    "v_fma_f32 v72, v72, s78, s78\n\t"
    "v_mfma_f32_16x16x32_bf16 v[144:147], v[196:199], v[34:37], v[144:147]\n\t"
    "v_fma_f32 v71, v71, v71, 0.5\n\t"
    "v_rcp_f32 v70, v70\n\t"
    "v_fma_f32 v69, -v179, v69, v179\n\t"
    "v_cvt_pk_bf16_f32 v125, v68, v69\n\t"
    "v_mfma_f32_16x16x32_bf16 v[148:151], v[196:199], v[50:53], v[148:151]\n\t"
    "v_exp_f32 v66, v184\n\t"
    "v_fma_f32 v73, v73, s78, s78\n\t"
    "v_fma_f32 v72, v72, v72, 0.5\n\t"
    "v_rcp_f32 v71, v71\n\t"
    "s_waitcnt lgkmcnt(14)\n\t"
    "v_mfma_f32_16x16x32_bf16 v[152:155], v[212:215], v[34:37], v[152:155]\n\t"
    "v_fma_f32 v70, -v180, v70, v180\n\t"
    "v_exp_f32 v67, v185\n\t"
    "v_fma_f32 v66, v66, s78, s78\n\t"
    "v_fma_f32 v73, v73, v73, 0.5\n\t"
    "v_mfma_f32_16x16x32_bf16 v[156:159], v[212:215], v[50:53], v[156:159]\n\t"
    "v_rcp_f32 v72, v72\n\t"
    "v_fma_f32 v71, -v181, v71, v181\n\t"
    "v_cvt_pk_bf16_f32 v126, v70, v71\n\t"
    "v_exp_f32 v68, v186\n\t"
    "s_waitcnt lgkmcnt(12)\n\t"
    "v_mfma_f32_16x16x32_bf16 v[132:135], v[80:83], v[34:37], v[132:135]\n\t"
    "v_fma_f32 v67, v67, s78, s78\n\t"
    "v_fma_f32 v66, v66, v66, 0.5\n\t"
    "v_rcp_f32 v73, v73\n\t"
    "v_fma_f32 v72, -v182, v72, v182\n\t"
    "v_mfma_f32_16x16x32_bf16 v[136:139], v[80:83], v[50:53], v[136:139]\n\t"
    "v_exp_f32 v69, v187\n\t"
    "v_fma_f32 v68, v68, s78, s78\n\t"
    "v_fma_f32 v67, v67, v67, 0.5\n\t"
    "v_rcp_f32 v66, v66\n\t"
    "s_waitcnt lgkmcnt(10)\n\t"
    "v_mfma_f32_16x16x32_bf16 v[144:147], v[200:203], v[54:57], v[144:147]\n\t"
    "v_fma_f32 v73, -v183, v73, v183\n\t"
    "v_cvt_pk_bf16_f32 v127, v72, v73\n\t"
    "v_exp_f32 v70, v188\n\t"
    "v_fma_f32 v69, v69, s78, s78\n\t"
    "v_mfma_f32_16x16x32_bf16 v[148:151], v[200:203], v[62:65], v[148:151]\n\t"
    "v_fma_f32 v68, v68, v68, 0.5\n\t"
    "v_rcp_f32 v67, v67\n\t"
    "v_fma_f32 v66, -v184, v66, v184\n\t"
    "v_exp_f32 v71, v189\n\t"
    "s_waitcnt lgkmcnt(8)\n\t"
    "v_mfma_f32_16x16x32_bf16 v[152:155], v[216:219], v[54:57], v[152:155]\n\t"
    "v_fma_f32 v70, v70, s78, s78\n\t"
    "v_fma_f32 v69, v69, v69, 0.5\n\t"
    "v_rcp_f32 v68, v68\n\t"
    "v_fma_f32 v67, -v185, v67, v185\n\t"
    "v_mfma_f32_16x16x32_bf16 v[156:159], v[216:219], v[62:65], v[156:159]\n\t"
    "v_cvt_pk_bf16_f32 v128, v66, v67\n\t"
    "v_exp_f32 v72, v190\n\t"
    "v_fma_f32 v71, v71, s78, s78\n\t"
    "v_fma_f32 v70, v70, v70, 0.5\n\t"
    "s_waitcnt lgkmcnt(6)\n\t"
    "v_mfma_f32_16x16x32_bf16 v[132:135], v[84:87], v[54:57], v[132:135]\n\t"
    "v_rcp_f32 v69, v69\n\t"
    "v_fma_f32 v68, -v186, v68, v186\n\t"
    "v_exp_f32 v73, v191\n\t"
    "v_fma_f32 v72, v72, s78, s78\n\t"
    "v_mfma_f32_16x16x32_bf16 v[136:139], v[84:87], v[62:65], v[136:139]\n\t"
    "v_fma_f32 v71, v71, v71, 0.5\n\t"
    "v_rcp_f32 v70, v70\n\t"
    "v_fma_f32 v69, -v187, v69, v187\n\t"
    "v_cvt_pk_bf16_f32 v129, v68, v69\n\t"
    "s_waitcnt lgkmcnt(4)\n\t"
    "v_mfma_f32_16x16x32_bf16 v[144:147], v[204:207], v[38:41], v[144:147]\n\t"
    "v_fma_f32 v73, v73, s78, s78\n\t"
    "v_fma_f32 v72, v72, v72, 0.5\n\t"
    "v_rcp_f32 v71, v71\n\t"
    "v_fma_f32 v70, -v188, v70, v188\n\t"
    "v_mfma_f32_16x16x32_bf16 v[148:151], v[204:207], v[54:57], v[148:151]\n\t"
    "v_fma_f32 v73, v73, v73, 0.5\n\t"
    "v_rcp_f32 v72, v72\n\t"
    "v_fma_f32 v71, -v189, v71, v189\n\t"
    "v_cvt_pk_bf16_f32 v130, v70, v71\n\t"
    "s_waitcnt lgkmcnt(2)\n\t"
    "v_mfma_f32_16x16x32_bf16 v[152:155], v[220:223], v[38:41], v[152:155]\n\t"
    "v_rcp_f32 v73, v73\n\t"
    "v_fma_f32 v72, -v190, v72, v190\n\t"
    "v_fma_f32 v73, -v191, v73, v191\n\t"
    "v_cvt_pk_bf16_f32 v131, v72, v73\n\t"
    "v_mfma_f32_16x16x32_bf16 v[156:159], v[220:223], v[54:57], v[156:159]\n\t"
    "s_waitcnt lgkmcnt(0)\n\t"
    "v_mfma_f32_16x16x32_bf16 v[132:135], v[88:91], v[38:41], v[132:135]\n\t"
    "v_mfma_f32_16x16x32_bf16 v[136:139], v[88:91], v[54:57], v[136:139]\n\t"
    "v_add_u32 v74, s82, v7\n\t"
    "ds_write_b64 v74, v[108:109] offset:0\n\t"
    "ds_write_b64 v74, v[110:111] offset:16\n\t"
    "ds_write_b64 v74, v[112:113] offset:32\n\t"
    "ds_write_b64 v74, v[114:115] offset:48\n\t"
    "ds_write_b64 v74, v[116:117] offset:37376\n\t"
    "ds_write_b64 v74, v[118:119] offset:37392\n\t"
    "ds_write_b64 v74, v[120:121] offset:37408\n\t"
    "ds_write_b64 v74, v[122:123] offset:37424\n\t"
    "v_cvt_pk_bf16_f32 v92, v144, v145\n\t"
    "v_cvt_pk_bf16_f32 v93, v146, v147\n\t"
    "ds_write_b64 v15, v[92:93] offset:0\n\t"
    "v_cvt_pk_bf16_f32 v94, v148, v149\n\t"
    "v_cvt_pk_bf16_f32 v95, v150, v151\n\t"
    "ds_write_b64 v15, v[94:95] offset:96\n\t"
    "v_cvt_pk_bf16_f32 v96, v152, v153\n\t"
    "v_cvt_pk_bf16_f32 v97, v154, v155\n\t"
    "ds_write_b64 v15, v[96:97] offset:32\n\t"
    "v_cvt_pk_bf16_f32 v98, v156, v157\n\t"
    "v_cvt_pk_bf16_f32 v99, v158, v159\n\t"
    "ds_write_b64 v15, v[98:99] offset:128\n\t"
    "v_cvt_pk_bf16_f32 v100, v132, v133\n\t"
    "v_cvt_pk_bf16_f32 v101, v134, v135\n\t"
    "v_cndmask_b32 v100, 0, v100, s[74:75]\n\t"
    "v_cndmask_b32 v101, 0, v101, s[74:75]\n\t"
    "ds_write_b64 v15, v[100:101] offset:64\n\t"
    "v_cvt_pk_bf16_f32 v102, v136, v137\n\t"
    "v_cvt_pk_bf16_f32 v103, v138, v139\n\t"
    "v_cndmask_b32 v102, 0, v102, s[74:75]\n\t"
    "v_cndmask_b32 v103, 0, v103, s[74:75]\n\t"
    "ds_write_b64 v15, v[102:103] offset:160\n\t"
    "v_add_u32 v74, s83, v7\n\t"
    "ds_write_b64 v74, v[124:125] offset:37376\n\t"
    "ds_write_b64 v74, v[126:127] offset:37392\n\t"
    "ds_write_b64 v74, v[128:129] offset:37408\n\t"
    "ds_write_b64 v74, v[130:131] offset:37424\n\t"
    "s_waitcnt lgkmcnt(0)\n\t"
    "L_p1end_11%=:\n\t"
    "v_lshrrev_b32 v13, 2, v1\n\t"
    "v_mul_u32_u24 v2, 1152, v13\n\t"
    "v_and_b32 v14, 3, v1\n\t"
    "v_lshl_add_u32 v2, v14, 4, v2\n\t"
    "v_add_u32 v3, 17408, v2\n\t"
    "v_add_u32 v4, 34816, v2\n\t"
    "s_mul_i32 s69, s64, 12288\n\t"
    "s_add_u32 s69, s69, 74752\n\t"
    "v_lshrrev_b32 v15, 5, v1\n\t"
    "v_bfe_u32 v16, v1, 2, 2\n\t"
    "v_lshl_add_u32 v15, v15, 3, v16\n\t"
    "v_lshlrev_b32 v5, 6, v15\n\t"
    "v_bfe_u32 v16, v1, 4, 1\n\t"
    "v_lshl_add_u32 v5, v16, 5, v5\n\t"
    "v_lshl_add_u32 v5, v14, 3, v5\n\t"
    "v_add_u32 v5, s69, v5\n\t"
    "s_lshl_b32 s70, s66, 1\n\t"
    "s_add_u32 s70, s70, 2\n\t"
    "s_mov_b32 s89, 0\n\t"
    "s_add_u32 s71, s70, s64\n\t"
    "s_add_u32 s71, s71, 0\n\t"
    "s_sub_u32 s72, s71, 18\n\t"
    "s_cmp_ge_u32 s71, 18\n\t"
    "s_cselect_b32 s71, s72, s71\n\t"
    "s_sub_u32 s72, s71, 18\n\t"
    "s_cmp_ge_u32 s71, 18\n\t"
    "s_cselect_b32 s71, s72, s71\n\t"
    "s_lshl_b32 s71, s71, 0\n\t"
    "s_or_b32 s89, s89, s71\n\t"
    "s_add_u32 s71, s70, s64\n\t"
    "s_add_u32 s71, s71, 4\n\t"
    "s_sub_u32 s72, s71, 18\n\t"
    "s_cmp_ge_u32 s71, 18\n\t"
    "s_cselect_b32 s71, s72, s71\n\t"
    "s_sub_u32 s72, s71, 18\n\t"
    "s_cmp_ge_u32 s71, 18\n\t"
    "s_cselect_b32 s71, s72, s71\n\t"
    "s_lshl_b32 s71, s71, 5\n\t"
    "s_or_b32 s89, s89, s71\n\t"
    "s_add_u32 s71, s70, s64\n\t"
    "s_add_u32 s71, s71, 8\n\t"
    "s_sub_u32 s72, s71, 18\n\t"
    "s_cmp_ge_u32 s71, 18\n\t"
    "s_cselect_b32 s71, s72, s71\n\t"
    "s_sub_u32 s72, s71, 18\n\t"
    "s_cmp_ge_u32 s71, 18\n\t"
    "s_cselect_b32 s71, s72, s71\n\t"
    "s_lshl_b32 s71, s71, 10\n\t"
    "s_or_b32 s89, s89, s71\n\t"
    "s_add_u32 s71, s70, s64\n\t"
    "s_add_u32 s71, s71, 12\n\t"
    "s_sub_u32 s72, s71, 18\n\t"
    "s_cmp_ge_u32 s71, 18\n\t"
    "s_cselect_b32 s71, s72, s71\n\t"
    "s_sub_u32 s72, s71, 18\n\t"
    "s_cmp_ge_u32 s71, 18\n\t"
    "s_cselect_b32 s71, s72, s71\n\t"
    "s_lshl_b32 s71, s71, 15\n\t"
    "s_or_b32 s89, s89, s71\n\t"
    "s_lshr_b32 s71, s64, 1\n\t"
    "s_add_u32 s71, s71, s70\n\t"
    "s_add_u32 s71, s71, 16\n\t"
    "s_sub_u32 s72, s71, 18\n\t"
    "s_cmp_ge_u32 s71, 18\n\t"
    "s_cselect_b32 s71, s72, s71\n\t"
    "s_sub_u32 s72, s71, 18\n\t"
    "s_cmp_ge_u32 s71, 18\n\t"
    "s_cselect_b32 s71, s72, s71\n\t"
    "s_lshl_b32 s71, s71, 20\n\t"
    "s_or_b32 s89, s89, s71\n\t"
    "s_mul_i32 s90, s66, 663552\n\t"
    "s_bfe_u32 s70, s89, 327680\n\t"
    "s_lshl_b32 s70, s70, 6\n\t"
    "s_add_u32 s68, s90, s70\n\t"
    "s_mul_i32 s91, s65, 589824\n\t"
    "s_add_u32 s91, s91, 6553600\n\t"
    "s_mul_i32 s92, s65, 1152\n\t"
    "s_lshl_b32 s70, s66, 3\n\t"
    "s_add_u32 s92, s92, s70\n\t"
    "s_add_u32 s92, s92, 4096\n\t"
    "v_lshlrev_b32 v6, 4, v1\n\t"
    "v_lshlrev_b32 v7, 2, v1\n\t"
    "v_cmp_gt_u32 vcc, 2, v1\n\t"
    "v_mov_b32 v14, 2146435072\n\t"
    "s_nop 0\n\t"
    "v_cndmask_b32 v7, v14, v7, vcc\n\t"
    "v_lshrrev_b32 v15, 5, v1\n\t"
    "v_and_b32 v16, 31, v1\n\t"
    "v_mul_u32_u24 v20, 1168, v16\n\t"
    "v_lshl_add_u32 v20, v15, 4, v20\n\t"
    "v_mul_u32_u24 v12, 192, v16\n\t"
    "v_lshl_add_u32 v12, v15, 4, v12\n\t"
    "v_add_u32 v12, 151552, v12\n\t"
    "v_cvt_f32_u32 v18, v16\n\t"
    "s_mul_i32 s70, s66, 36\n\t"
    "v_lshlrev_b32 v15, 3, v15\n\t"
    "v_add_u32 v15, s70, v15\n\t"
    "v_cvt_f32_u32 v73, v15\n\t"
    "v_mul_f32 v73, 0x3ae38e39, v73\n\t"
    "v_add_f32 v74, 0x3ce38e39, v73\n\t"
    "v_add_f32 v75, 0x3d638e39, v73\n\t"
    "v_mov_b32 v19, s67\n\t"
    "s_barrier\n\t"
    "s_add_u32 m0, s69, 0\n\t"
    "s_nop 0\n\t"
    "buffer_load_dwordx4 v2, s[48:51], s68 offen offset:0 lds\n\t"
    "ds_read_b128 a[0:3], v20 offset:0\n\t"
    "ds_read_b128 a[4:7], v20 offset:37376\n\t"
    "buffer_load_dwordx4 v3, s[48:51], s68 offen offset:1024 lds\n\t"
    "ds_read_b128 a[8:11], v20 offset:32\n\t"
    "ds_read_b128 a[12:15], v20 offset:37408\n\t"
    "buffer_load_dwordx4 v4, s[48:51], s68 offen offset:2048 lds\n\t"
    "s_add_u32 s68, s68, 55296\n\t"
    "ds_read_b128 a[16:19], v20 offset:64\n\t"
    "ds_read_b128 a[20:23], v20 offset:37440\n\t"
    "s_add_u32 m0, s69, 3072\n\t"
    "s_nop 0\n\t"
    "buffer_load_dwordx4 v2, s[48:51], s68 offen offset:0 lds\n\t"
    "ds_read_b128 a[24:27], v20 offset:96\n\t"
    "ds_read_b128 a[28:31], v20 offset:37472\n\t"
    "buffer_load_dwordx4 v3, s[48:51], s68 offen offset:1024 lds\n\t"
    "buffer_load_dwordx4 v4, s[48:51], s68 offen offset:2048 lds\n\t"
    "s_add_u32 s68, s68, 55296\n\t"
    "s_add_u32 m0, s69, 6144\n\t"
    "s_nop 0\n\t"
    "buffer_load_dwordx4 v2, s[48:51], s68 offen offset:0 lds\n\t"
    "buffer_load_dwordx4 v3, s[48:51], s68 offen offset:1024 lds\n\t"
    "buffer_load_dwordx4 v4, s[48:51], s68 offen offset:2048 lds\n\t"
    "s_add_u32 s68, s68, 55296\n\t"
    "s_add_u32 m0, s69, 9216\n\t"
    "s_nop 0\n\t"
    "buffer_load_dwordx4 v2, s[48:51], s68 offen offset:0 lds\n\t"
    "buffer_load_dwordx4 v3, s[48:51], s68 offen offset:1024 lds\n\t"
    "buffer_load_dwordx4 v4, s[48:51], s68 offen offset:2048 lds\n\t"
    "s_add_u32 s68, s68, 55296\n\t"
    "s_bfe_u32 s70, s89, 327680\n\t"
    "s_lshl_b32 s70, s70, 5\n\t"
    "v_cvt_f32_u32 v71, s70\n\t"
    "v_add_f32 v71, v71, v18\n\t"
    "v_mul_f32 v72, 0x3ae38e39, v71\n\t"
    "v_cos_f32 v69, v72\n\t"
    "s_nop 0\n\t"
    "v_add_f32 v69, v69, v69\n\t"
    "s_waitcnt vmcnt(10)\n\t"
    "ds_read_b64_tr_b16 v[100:101], v5 offset:0\n\t"
    "ds_read_b64_tr_b16 v[102:103], v5 offset:256\n\t"
    "ds_read_b64_tr_b16 v[104:105], v5 offset:1024\n\t"
    "ds_read_b64_tr_b16 v[106:107], v5 offset:1280\n\t"
    "s_waitcnt vmcnt(9) lgkmcnt(2)\n\t"
    "v_mfma_f32_32x32x16_bf16 v[112:127], v[100:103], a[0:3], 0\n\t"
    "ds_read_b64_tr_b16 v[108:109], v5 offset:2048\n\t"
    "ds_read_b64_tr_b16 v[110:111], v5 offset:2304\n\t"
    "v_mul_f32 v70, v71, v73\n\t"
    "v_mfma_f32_32x32x16_bf16 v[128:143], v[100:103], a[4:7], 0\n\t"
    "v_fract_f32 v70, v70\n\t"
    "ds_read_b128 a[32:35], v20 offset:128\n\t"
    "ds_read_b128 a[36:39], v20 offset:37504\n\t"
    "s_waitcnt vmcnt(8) lgkmcnt(4)\n\t"
    "v_mfma_f32_32x32x16_bf16 v[112:127], v[104:107], a[8:11], v[112:127]\n\t"
    "s_add_u32 m0, s69, 0\n\t"
    "ds_read_b64_tr_b16 v[100:101], v5 offset:3072\n\t"
    "buffer_load_dwordx4 v2, s[48:51], s68 offen offset:0 lds\n\t"
    "ds_read_b64_tr_b16 v[102:103], v5 offset:3328\n\t"
    "v_cos_f32 v76, v70\n\t"
    "v_mfma_f32_32x32x16_bf16 v[128:143], v[104:107], a[12:15], v[128:143]\n\t"
    "v_add_f32 v70, v70, v72\n\t"
    "ds_read_b128 a[40:43], v20 offset:160\n\t"
    "ds_read_b128 a[44:47], v20 offset:37536\n\t"
    "ds_read_b128 v[176:179], v12 offset:0\n\t"
    "ds_read_b128 v[180:183], v12 offset:6144\n\t"
    "s_waitcnt vmcnt(8) lgkmcnt(8)\n\t"
    "v_mfma_f32_32x32x16_bf16 v[112:127], v[108:111], a[16:19], v[112:127]\n\t"
    "ds_read_b64_tr_b16 v[104:105], v5 offset:4096\n\t"
    "buffer_load_dwordx4 v3, s[48:51], s68 offen offset:1024 lds\n\t"
    "ds_read_b64_tr_b16 v[106:107], v5 offset:4352\n\t"
    "v_cos_f32 v77, v70\n\t"
    "v_mfma_f32_32x32x16_bf16 v[128:143], v[108:111], a[20:23], v[128:143]\n\t"
    "s_nop 0\n\t"
    "v_fma_f32 v78, v69, v77, -v76\n\t"
    "ds_read_b128 a[48:51], v20 offset:192\n\t"
    "ds_read_b128 a[52:55], v20 offset:37568\n\t"
    "s_waitcnt vmcnt(8) lgkmcnt(8)\n\t"
    "v_mfma_f32_32x32x16_bf16 v[112:127], v[100:103], a[24:27], v[112:127]\n\t"
    "ds_read_b64_tr_b16 v[108:109], v5 offset:5120\n\t"
    "buffer_load_dwordx4 v4, s[48:51], s68 offen offset:2048 lds\n\t"
    "s_add_u32 s68, s68, 55296\n\t"
    "ds_read_b64_tr_b16 v[110:111], v5 offset:5376\n\t"
    "v_fma_f32 v79, v69, v78, -v77\n\t"
    "v_mfma_f32_32x32x16_bf16 v[128:143], v[100:103], a[28:31], v[128:143]\n\t"
    "v_cvt_pk_bf16_f32 v92, v76, v77\n\t"
    "v_fma_f32 v80, v69, v79, -v78\n\t"
    "ds_read_b128 a[56:59], v20 offset:224\n\t"
    "ds_read_b128 a[60:63], v20 offset:37600\n\t"
    "s_waitcnt vmcnt(8) lgkmcnt(6)\n\t"
    "v_mfma_f32_32x32x16_bf16 v[112:127], v[104:107], a[32:35], v[112:127]\n\t"
    "s_add_u32 m0, s69, 3072\n\t"
    "ds_read_b64_tr_b16 v[100:101], v5 offset:6144\n\t"
    "buffer_load_dwordx4 v2, s[48:51], s68 offen offset:0 lds\n\t"
    "ds_read_b64_tr_b16 v[102:103], v5 offset:6400\n\t"
    "v_fma_f32 v81, v69, v80, -v79\n\t"
    "v_mfma_f32_32x32x16_bf16 v[128:143], v[104:107], a[36:39], v[128:143]\n\t"
    "v_cvt_pk_bf16_f32 v93, v78, v79\n\t"
    "v_fma_f32 v82, v69, v81, -v80\n\t"
    "ds_read_b128 a[64:67], v20 offset:256\n\t"
    "ds_read_b128 a[68:71], v20 offset:37632\n\t"
    "s_waitcnt vmcnt(8) lgkmcnt(6)\n\t"
    "v_mfma_f32_32x32x16_bf16 v[112:127], v[108:111], a[40:43], v[112:127]\n\t"
    "ds_read_b64_tr_b16 v[104:105], v5 offset:7168\n\t"
    "buffer_load_dwordx4 v3, s[48:51], s68 offen offset:1024 lds\n\t"
    "ds_read_b64_tr_b16 v[106:107], v5 offset:7424\n\t"
    "v_fma_f32 v83, v69, v82, -v81\n\t"
    "v_mfma_f32_32x32x16_bf16 v[128:143], v[108:111], a[44:47], v[128:143]\n\t"
    "v_cvt_pk_bf16_f32 v94, v80, v81\n\t"
    "v_cvt_pk_bf16_f32 v95, v82, v83\n\t"
    "ds_read_b128 a[72:75], v20 offset:288\n\t"
    "ds_read_b128 a[76:79], v20 offset:37664\n\t"
    "v_mfma_f32_32x32x16_bf16 v[112:127], v[92:95], v[176:179], v[112:127]\n\t"
    "v_mfma_f32_32x32x16_bf16 v[128:143], v[92:95], v[180:183], v[128:143]\n\t"
    "s_waitcnt vmcnt(8) lgkmcnt(6)\n\t"
    "v_mfma_f32_32x32x16_bf16 v[112:127], v[100:103], a[48:51], v[112:127]\n\t"
    "ds_read_b64_tr_b16 v[108:109], v5 offset:8192\n\t"
    "buffer_load_dwordx4 v4, s[48:51], s68 offen offset:2048 lds\n\t"
    "s_add_u32 s68, s68, 55296\n\t"
    "ds_read_b64_tr_b16 v[110:111], v5 offset:8448\n\t"
    "v_mul_f32 v70, v71, v74\n\t"
    "v_mfma_f32_32x32x16_bf16 v[128:143], v[100:103], a[52:55], v[128:143]\n\t"
    "v_fract_f32 v70, v70\n\t"
    "ds_read_b128 a[80:83], v20 offset:320\n\t"
    "ds_read_b128 a[84:87], v20 offset:37696\n\t"
    "s_waitcnt vmcnt(8) lgkmcnt(6)\n\t"
    "v_mfma_f32_32x32x16_bf16 v[112:127], v[104:107], a[56:59], v[112:127]\n\t"
    "s_add_u32 m0, s69, 6144\n\t"
    "ds_read_b64_tr_b16 v[100:101], v5 offset:9216\n\t"
    "buffer_load_dwordx4 v2, s[48:51], s68 offen offset:0 lds\n\t"
    "ds_read_b64_tr_b16 v[102:103], v5 offset:9472\n\t"
    "v_cos_f32 v76, v70\n\t"
    "v_mfma_f32_32x32x16_bf16 v[128:143], v[104:107], a[60:63], v[128:143]\n\t"
    "v_add_f32 v70, v70, v72\n\t"
    "ds_read_b128 a[88:91], v20 offset:352\n\t"
    "ds_read_b128 a[92:95], v20 offset:37728\n\t"
    "ds_read_b128 v[184:187], v12 offset:32\n\t"
    "ds_read_b128 v[188:191], v12 offset:6176\n\t"
    "s_waitcnt vmcnt(8) lgkmcnt(8)\n\t"
    "v_mfma_f32_32x32x16_bf16 v[112:127], v[108:111], a[64:67], v[112:127]\n\t"
    "ds_read_b64_tr_b16 v[104:105], v5 offset:10240\n\t"
    "buffer_load_dwordx4 v3, s[48:51], s68 offen offset:1024 lds\n\t"
    "ds_read_b64_tr_b16 v[106:107], v5 offset:10496\n\t"
    "v_cos_f32 v77, v70\n\t"
    "v_mfma_f32_32x32x16_bf16 v[128:143], v[108:111], a[68:71], v[128:143]\n\t"
    "s_nop 0\n\t"
    "v_fma_f32 v78, v69, v77, -v76\n\t"
    "ds_read_b128 a[96:99], v20 offset:384\n\t"
    "ds_read_b128 a[100:103], v20 offset:37760\n\t"
    "s_waitcnt vmcnt(8) lgkmcnt(8)\n\t"
    "v_mfma_f32_32x32x16_bf16 v[112:127], v[100:103], a[72:75], v[112:127]\n\t"
    "ds_read_b64_tr_b16 v[108:109], v5 offset:11264\n\t"
    "buffer_load_dwordx4 v4, s[48:51], s68 offen offset:2048 lds\n\t"
    "s_add_u32 s68, s68, 55296\n\t"
    "ds_read_b64_tr_b16 v[110:111], v5 offset:11520\n\t"
    "v_fma_f32 v79, v69, v78, -v77\n\t"
    "v_mfma_f32_32x32x16_bf16 v[128:143], v[100:103], a[76:79], v[128:143]\n\t"
    "v_cvt_pk_bf16_f32 v96, v76, v77\n\t"
    "v_fma_f32 v80, v69, v79, -v78\n\t"
    "ds_read_b128 a[104:107], v20 offset:416\n\t"
    "ds_read_b128 a[108:111], v20 offset:37792\n\t"
    "s_waitcnt vmcnt(8) lgkmcnt(6)\n\t"
    "v_mfma_f32_32x32x16_bf16 v[112:127], v[104:107], a[80:83], v[112:127]\n\t"
    "s_add_u32 m0, s69, 9216\n\t"
    "ds_read_b64_tr_b16 v[100:101], v5 offset:0\n\t"
    "buffer_load_dwordx4 v2, s[48:51], s68 offen offset:0 lds\n\t"
    "ds_read_b64_tr_b16 v[102:103], v5 offset:256\n\t"
    "v_fma_f32 v81, v69, v80, -v79\n\t"
    "v_mfma_f32_32x32x16_bf16 v[128:143], v[104:107], a[84:87], v[128:143]\n\t"
    "v_cvt_pk_bf16_f32 v97, v78, v79\n\t"
    "v_fma_f32 v82, v69, v81, -v80\n\t"
    "ds_read_b128 a[112:115], v20 offset:448\n\t"
    "ds_read_b128 a[116:119], v20 offset:37824\n\t"
    "s_waitcnt vmcnt(8) lgkmcnt(6)\n\t"
    "v_mfma_f32_32x32x16_bf16 v[112:127], v[108:111], a[88:91], v[112:127]\n\t"
    "ds_read_b64_tr_b16 v[104:105], v5 offset:1024\n\t"
    "buffer_load_dwordx4 v3, s[48:51], s68 offen offset:1024 lds\n\t"
    "ds_read_b64_tr_b16 v[106:107], v5 offset:1280\n\t"
    "v_fma_f32 v83, v69, v82, -v81\n\t"
    "v_mfma_f32_32x32x16_bf16 v[128:143], v[108:111], a[92:95], v[128:143]\n\t"
    "v_cvt_pk_bf16_f32 v98, v80, v81\n\t"
    "v_cvt_pk_bf16_f32 v99, v82, v83\n\t"
    "ds_read_b128 a[120:123], v20 offset:480\n\t"
    "ds_read_b128 a[124:127], v20 offset:37856\n\t"
    "v_mfma_f32_32x32x16_bf16 v[112:127], v[96:99], v[184:187], v[112:127]\n\t"
    "v_mfma_f32_32x32x16_bf16 v[128:143], v[96:99], v[188:191], v[128:143]\n\t"
    "s_waitcnt vmcnt(8) lgkmcnt(6)\n\t"
    "v_mfma_f32_32x32x16_bf16 v[112:127], v[100:103], a[96:99], v[112:127]\n\t"
    "ds_read_b64_tr_b16 v[108:109], v5 offset:2048\n\t"
    "buffer_load_dwordx4 v4, s[48:51], s68 offen offset:2048 lds\n\t"
    "s_add_u32 s68, s68, 55296\n\t"
    "ds_read_b64_tr_b16 v[110:111], v5 offset:2304\n\t"
    "v_mul_f32 v70, v71, v75\n\t"
    "v_mfma_f32_32x32x16_bf16 v[128:143], v[100:103], a[100:103], v[128:143]\n\t"
    "v_fract_f32 v70, v70\n\t"
    "ds_read_b128 a[128:131], v20 offset:512\n\t"
    "ds_read_b128 a[132:135], v20 offset:37888\n\t"
    "s_waitcnt vmcnt(8) lgkmcnt(6)\n\t"
    "v_mfma_f32_32x32x16_bf16 v[112:127], v[104:107], a[104:107], v[112:127]\n\t"
    "s_add_u32 m0, s69, 0\n\t"
    "ds_read_b64_tr_b16 v[100:101], v5 offset:3072\n\t"
    "buffer_load_dwordx4 v2, s[48:51], s68 offen offset:0 lds\n\t"
    "ds_read_b64_tr_b16 v[102:103], v5 offset:3328\n\t"
    "v_cos_f32 v76, v70\n\t"
    "v_mfma_f32_32x32x16_bf16 v[128:143], v[104:107], a[108:111], v[128:143]\n\t"
    "v_add_f32 v70, v70, v72\n\t"
    "ds_read_b128 a[136:139], v20 offset:544\n\t"
    "ds_read_b128 a[140:143], v20 offset:37920\n\t"
    "ds_read_b128 v[192:195], v12 offset:64\n\t"
    "ds_read_b128 v[196:199], v12 offset:6208\n\t"
    "s_waitcnt vmcnt(8) lgkmcnt(8)\n\t"
    "v_mfma_f32_32x32x16_bf16 v[112:127], v[108:111], a[112:115], v[112:127]\n\t"
    "ds_read_b64_tr_b16 v[104:105], v5 offset:4096\n\t"
    "buffer_load_dwordx4 v3, s[48:51], s68 offen offset:1024 lds\n\t"
    "ds_read_b64_tr_b16 v[106:107], v5 offset:4352\n\t"
    "v_cos_f32 v77, v70\n\t"
    "v_mfma_f32_32x32x16_bf16 v[128:143], v[108:111], a[116:119], v[128:143]\n\t"
    "s_nop 0\n\t"
    "v_fma_f32 v78, v69, v77, -v76\n\t"
    "ds_read_b128 a[144:147], v20 offset:576\n\t"
    "ds_read_b128 a[148:151], v20 offset:37952\n\t"
    "s_waitcnt vmcnt(8) lgkmcnt(8)\n\t"
    "v_mfma_f32_32x32x16_bf16 v[112:127], v[100:103], a[120:123], v[112:127]\n\t"
    "ds_read_b64_tr_b16 v[108:109], v5 offset:5120\n\t"
    "buffer_load_dwordx4 v4, s[48:51], s68 offen offset:2048 lds\n\t"
    "s_add_u32 s68, s68, 55296\n\t"
    "ds_read_b64_tr_b16 v[110:111], v5 offset:5376\n\t"
    "v_fma_f32 v79, v69, v78, -v77\n\t"
    "v_mfma_f32_32x32x16_bf16 v[128:143], v[100:103], a[124:127], v[128:143]\n\t"
    "v_cvt_pk_bf16_f32 v92, v76, v77\n\t"
    "v_fma_f32 v80, v69, v79, -v78\n\t"
    "ds_read_b128 a[152:155], v20 offset:608\n\t"
    "ds_read_b128 a[156:159], v20 offset:37984\n\t"
    "s_waitcnt vmcnt(8) lgkmcnt(6)\n\t"
    "v_mfma_f32_32x32x16_bf16 v[112:127], v[104:107], a[128:131], v[112:127]\n\t"
    "s_add_u32 m0, s69, 3072\n\t"
    "ds_read_b64_tr_b16 v[100:101], v5 offset:6144\n\t"
    "buffer_load_dwordx4 v2, s[48:51], s68 offen offset:0 lds\n\t"
    "ds_read_b64_tr_b16 v[102:103], v5 offset:6400\n\t"
    "v_fma_f32 v81, v69, v80, -v79\n\t"
    "v_mfma_f32_32x32x16_bf16 v[128:143], v[104:107], a[132:135], v[128:143]\n\t"
    "v_cvt_pk_bf16_f32 v93, v78, v79\n\t"
    "v_fma_f32 v82, v69, v81, -v80\n\t"
    "ds_read_b128 a[160:163], v20 offset:640\n\t"
    "ds_read_b128 a[164:167], v20 offset:38016\n\t"
    "s_waitcnt vmcnt(8) lgkmcnt(6)\n\t"
    "v_mfma_f32_32x32x16_bf16 v[112:127], v[108:111], a[136:139], v[112:127]\n\t"
    "ds_read_b64_tr_b16 v[104:105], v5 offset:7168\n\t"
    "buffer_load_dwordx4 v3, s[48:51], s68 offen offset:1024 lds\n\t"
    "ds_read_b64_tr_b16 v[106:107], v5 offset:7424\n\t"
    "v_fma_f32 v83, v69, v82, -v81\n\t"
    "v_mfma_f32_32x32x16_bf16 v[128:143], v[108:111], a[140:143], v[128:143]\n\t"
    "v_cvt_pk_bf16_f32 v94, v80, v81\n\t"
    "v_cvt_pk_bf16_f32 v95, v82, v83\n\t"
    "ds_read_b128 a[168:171], v20 offset:672\n\t"
    "ds_read_b128 a[172:175], v20 offset:38048\n\t"
    "v_mfma_f32_32x32x16_bf16 v[112:127], v[92:95], v[192:195], v[112:127]\n\t"
    "v_mfma_f32_32x32x16_bf16 v[128:143], v[92:95], v[196:199], v[128:143]\n\t"
    "s_waitcnt vmcnt(8) lgkmcnt(6)\n\t"
    "v_mfma_f32_32x32x16_bf16 v[112:127], v[100:103], a[144:147], v[112:127]\n\t"
    "ds_read_b64_tr_b16 v[108:109], v5 offset:8192\n\t"
    "buffer_load_dwordx4 v4, s[48:51], s68 offen offset:2048 lds\n\t"
    "s_add_u32 s68, s68, 55296\n\t"
    "ds_read_b64_tr_b16 v[110:111], v5 offset:8448\n\t"
    "v_fmaak_f32 v70, v71, v73, 0x3e800000\n\t"
    "v_mfma_f32_32x32x16_bf16 v[128:143], v[100:103], a[148:151], v[128:143]\n\t"
    "v_fract_f32 v70, v70\n\t"
    "ds_read_b128 a[176:179], v20 offset:704\n\t"
    "ds_read_b128 a[180:183], v20 offset:38080\n\t"
    "s_waitcnt vmcnt(8) lgkmcnt(6)\n\t"
    "v_mfma_f32_32x32x16_bf16 v[112:127], v[104:107], a[152:155], v[112:127]\n\t"
    "s_add_u32 m0, s69, 6144\n\t"
    "ds_read_b64_tr_b16 v[100:101], v5 offset:9216\n\t"
    "buffer_load_dwordx4 v2, s[48:51], s68 offen offset:0 lds\n\t"
    "ds_read_b64_tr_b16 v[102:103], v5 offset:9472\n\t"
    "v_cos_f32 v76, v70\n\t"
    "v_mfma_f32_32x32x16_bf16 v[128:143], v[104:107], a[156:159], v[128:143]\n\t"
    "v_add_f32 v70, v70, v72\n\t"
    "ds_read_b128 a[184:187], v20 offset:736\n\t"
    "ds_read_b128 a[188:191], v20 offset:38112\n\t"
    "ds_read_b128 v[200:203], v12 offset:96\n\t"
    "ds_read_b128 v[204:207], v12 offset:6240\n\t"
    "s_waitcnt vmcnt(8) lgkmcnt(8)\n\t"
    "v_mfma_f32_32x32x16_bf16 v[112:127], v[108:111], a[160:163], v[112:127]\n\t"
    "ds_read_b64_tr_b16 v[104:105], v5 offset:10240\n\t"
    "buffer_load_dwordx4 v3, s[48:51], s68 offen offset:1024 lds\n\t"
    "ds_read_b64_tr_b16 v[106:107], v5 offset:10496\n\t"
    "v_cos_f32 v77, v70\n\t"
    "v_mfma_f32_32x32x16_bf16 v[128:143], v[108:111], a[164:167], v[128:143]\n\t"
    "s_nop 0\n\t"
    "v_fma_f32 v78, v69, v77, -v76\n\t"
    "ds_read_b128 a[192:195], v20 offset:768\n\t"
    "ds_read_b128 a[196:199], v20 offset:38144\n\t"
    "s_waitcnt vmcnt(8) lgkmcnt(8)\n\t"
    "v_mfma_f32_32x32x16_bf16 v[112:127], v[100:103], a[168:171], v[112:127]\n\t"
    "ds_read_b64_tr_b16 v[108:109], v5 offset:11264\n\t"
    "buffer_load_dwordx4 v4, s[48:51], s68 offen offset:2048 lds\n\t"
    "s_add_u32 s68, s68, 55296\n\t"
    "ds_read_b64_tr_b16 v[110:111], v5 offset:11520\n\t"
    "v_fma_f32 v79, v69, v78, -v77\n\t"
    "v_mfma_f32_32x32x16_bf16 v[128:143], v[100:103], a[172:175], v[128:143]\n\t"
    "v_cvt_pk_bf16_f32 v96, v76, v77\n\t"
    "v_fma_f32 v80, v69, v79, -v78\n\t"
    "ds_read_b128 a[200:203], v20 offset:800\n\t"
    "ds_read_b128 a[204:207], v20 offset:38176\n\t"
    "s_waitcnt vmcnt(8) lgkmcnt(6)\n\t"
    "v_mfma_f32_32x32x16_bf16 v[112:127], v[104:107], a[176:179], v[112:127]\n\t"
    "s_add_u32 m0, s69, 9216\n\t"
    "ds_read_b64_tr_b16 v[100:101], v5 offset:0\n\t"
    "buffer_load_dwordx4 v2, s[48:51], s68 offen offset:0 lds\n\t"
    "ds_read_b64_tr_b16 v[102:103], v5 offset:256\n\t"
    "v_fma_f32 v81, v69, v80, -v79\n\t"
    "v_mfma_f32_32x32x16_bf16 v[128:143], v[104:107], a[180:183], v[128:143]\n\t"
    "v_cvt_pk_bf16_f32 v97, v78, v79\n\t"
    "v_fma_f32 v82, v69, v81, -v80\n\t"
    "ds_read_b128 a[208:211], v20 offset:832\n\t"
    "ds_read_b128 a[212:215], v20 offset:38208\n\t"
    "s_waitcnt vmcnt(8) lgkmcnt(6)\n\t"
    "v_mfma_f32_32x32x16_bf16 v[112:127], v[108:111], a[184:187], v[112:127]\n\t"
    "ds_read_b64_tr_b16 v[104:105], v5 offset:1024\n\t"
    "buffer_load_dwordx4 v3, s[48:51], s68 offen offset:1024 lds\n\t"
    "ds_read_b64_tr_b16 v[106:107], v5 offset:1280\n\t"
    "v_fma_f32 v83, v69, v82, -v81\n\t"
    "v_mfma_f32_32x32x16_bf16 v[128:143], v[108:111], a[188:191], v[128:143]\n\t"
    "v_cvt_pk_bf16_f32 v98, v80, v81\n\t"
    "v_cvt_pk_bf16_f32 v99, v82, v83\n\t"
    "ds_read_b128 a[216:219], v20 offset:864\n\t"
    "ds_read_b128 a[220:223], v20 offset:38240\n\t"
    "v_mfma_f32_32x32x16_bf16 v[112:127], v[96:99], v[200:203], v[112:127]\n\t"
    "v_mfma_f32_32x32x16_bf16 v[128:143], v[96:99], v[204:207], v[128:143]\n\t"
    "s_waitcnt vmcnt(8) lgkmcnt(6)\n\t"
    "v_mfma_f32_32x32x16_bf16 v[112:127], v[100:103], a[192:195], v[112:127]\n\t"
    "ds_read_b64_tr_b16 v[108:109], v5 offset:2048\n\t"
    "buffer_load_dwordx4 v4, s[48:51], s68 offen offset:2048 lds\n\t"
    "s_bfe_u32 s70, s89, 327685\n\t"
    "s_lshl_b32 s70, s70, 6\n\t"
    "s_add_u32 s68, s90, s70\n\t"
    "ds_read_b64_tr_b16 v[110:111], v5 offset:2304\n\t"
    "v_fmaak_f32 v70, v71, v74, 0x3e800000\n\t"
    "v_mfma_f32_32x32x16_bf16 v[128:143], v[100:103], a[196:199], v[128:143]\n\t"
    "v_fract_f32 v70, v70\n\t"
    "ds_read_b128 a[224:227], v20 offset:896\n\t"
    "ds_read_b128 a[228:231], v20 offset:38272\n\t"
    "s_waitcnt vmcnt(8) lgkmcnt(6)\n\t"
    "v_mfma_f32_32x32x16_bf16 v[112:127], v[104:107], a[200:203], v[112:127]\n\t"
    "s_add_u32 m0, s69, 0\n\t"
    "ds_read_b64_tr_b16 v[100:101], v5 offset:3072\n\t"
    "buffer_load_dwordx4 v2, s[48:51], s68 offen offset:0 lds\n\t"
    "ds_read_b64_tr_b16 v[102:103], v5 offset:3328\n\t"
    "v_cos_f32 v76, v70\n\t"
    "v_mfma_f32_32x32x16_bf16 v[128:143], v[104:107], a[204:207], v[128:143]\n\t"
    "v_add_f32 v70, v70, v72\n\t"
    "ds_read_b128 a[232:235], v20 offset:928\n\t"
    "ds_read_b128 a[236:239], v20 offset:38304\n\t"
    "ds_read_b128 v[208:211], v12 offset:128\n\t"
    "ds_read_b128 v[212:215], v12 offset:6272\n\t"
    "s_waitcnt vmcnt(8) lgkmcnt(8)\n\t"
    "v_mfma_f32_32x32x16_bf16 v[112:127], v[108:111], a[208:211], v[112:127]\n\t"
    "ds_read_b64_tr_b16 v[104:105], v5 offset:4096\n\t"
    "buffer_load_dwordx4 v3, s[48:51], s68 offen offset:1024 lds\n\t"
    "ds_read_b64_tr_b16 v[106:107], v5 offset:4352\n\t"
    "v_cos_f32 v77, v70\n\t"
    "v_mfma_f32_32x32x16_bf16 v[128:143], v[108:111], a[212:215], v[128:143]\n\t"
    "s_nop 0\n\t"
    "v_fma_f32 v78, v69, v77, -v76\n\t"
    "ds_read_b128 a[240:243], v20 offset:960\n\t"
    "ds_read_b128 a[244:247], v20 offset:38336\n\t"
    "s_waitcnt vmcnt(8) lgkmcnt(8)\n\t"
    "v_mfma_f32_32x32x16_bf16 v[112:127], v[100:103], a[216:219], v[112:127]\n\t"
    "ds_read_b64_tr_b16 v[108:109], v5 offset:5120\n\t"
    "buffer_load_dwordx4 v4, s[48:51], s68 offen offset:2048 lds\n\t"
    "s_add_u32 s68, s68, 55296\n\t"
    "ds_read_b64_tr_b16 v[110:111], v5 offset:5376\n\t"
    "v_fma_f32 v79, v69, v78, -v77\n\t"
    "v_mfma_f32_32x32x16_bf16 v[128:143], v[100:103], a[220:223], v[128:143]\n\t"
    "v_cvt_pk_bf16_f32 v92, v76, v77\n\t"
    "v_fma_f32 v80, v69, v79, -v78\n\t"
    "ds_read_b128 a[248:251], v20 offset:992\n\t"
    "ds_read_b128 a[252:255], v20 offset:38368\n\t"
    "s_waitcnt vmcnt(8) lgkmcnt(6)\n\t"
    "v_mfma_f32_32x32x16_bf16 v[112:127], v[104:107], a[224:227], v[112:127]\n\t"
    "s_add_u32 m0, s69, 3072\n\t"
    "ds_read_b64_tr_b16 v[100:101], v5 offset:6144\n\t"
    "buffer_load_dwordx4 v2, s[48:51], s68 offen offset:0 lds\n\t"
    "ds_read_b64_tr_b16 v[102:103], v5 offset:6400\n\t"
    "v_fma_f32 v81, v69, v80, -v79\n\t"
    "v_mfma_f32_32x32x16_bf16 v[128:143], v[104:107], a[228:231], v[128:143]\n\t"
    "v_cvt_pk_bf16_f32 v93, v78, v79\n\t"
    "v_fma_f32 v82, v69, v81, -v80\n\t"
    "ds_read_b128 v[224:227], v20 offset:1024\n\t"
    "ds_read_b128 v[228:231], v20 offset:38400\n\t"
    "s_waitcnt vmcnt(8) lgkmcnt(6)\n\t"
    "v_mfma_f32_32x32x16_bf16 v[112:127], v[108:111], a[232:235], v[112:127]\n\t"
    "ds_read_b64_tr_b16 v[104:105], v5 offset:7168\n\t"
    "buffer_load_dwordx4 v3, s[48:51], s68 offen offset:1024 lds\n\t"
    "ds_read_b64_tr_b16 v[106:107], v5 offset:7424\n\t"
    "v_fma_f32 v83, v69, v82, -v81\n\t"
    "v_mfma_f32_32x32x16_bf16 v[128:143], v[108:111], a[236:239], v[128:143]\n\t"
    "v_cvt_pk_bf16_f32 v94, v80, v81\n\t"
    "v_cvt_pk_bf16_f32 v95, v82, v83\n\t"
    "ds_read_b128 v[232:235], v20 offset:1056\n\t"
    "ds_read_b128 v[236:239], v20 offset:38432\n\t"
    "v_mfma_f32_32x32x16_bf16 v[112:127], v[92:95], v[208:211], v[112:127]\n\t"
    "v_mfma_f32_32x32x16_bf16 v[128:143], v[92:95], v[212:215], v[128:143]\n\t"
    "s_waitcnt vmcnt(8) lgkmcnt(6)\n\t"
    "v_mfma_f32_32x32x16_bf16 v[112:127], v[100:103], a[240:243], v[112:127]\n\t"
    "ds_read_b64_tr_b16 v[108:109], v5 offset:8192\n\t"
    "buffer_load_dwordx4 v4, s[48:51], s68 offen offset:2048 lds\n\t"
    "s_add_u32 s68, s68, 55296\n\t"
    "ds_read_b64_tr_b16 v[110:111], v5 offset:8448\n\t"
    "v_fmaak_f32 v70, v71, v75, 0x3e800000\n\t"
    "v_mfma_f32_32x32x16_bf16 v[128:143], v[100:103], a[244:247], v[128:143]\n\t"
    "v_fract_f32 v70, v70\n\t"
    "ds_read_b128 v[240:243], v20 offset:1088\n\t"
    "ds_read_b128 v[244:247], v20 offset:38464\n\t"
    "s_waitcnt vmcnt(8) lgkmcnt(6)\n\t"
    "v_mfma_f32_32x32x16_bf16 v[112:127], v[104:107], a[248:251], v[112:127]\n\t"
    "s_add_u32 m0, s69, 6144\n\t"
    "ds_read_b64_tr_b16 v[100:101], v5 offset:9216\n\t"
    "buffer_load_dwordx4 v2, s[48:51], s68 offen offset:0 lds\n\t"
    "ds_read_b64_tr_b16 v[102:103], v5 offset:9472\n\t"
    "v_cos_f32 v76, v70\n\t"
    "v_mfma_f32_32x32x16_bf16 v[128:143], v[104:107], a[252:255], v[128:143]\n\t"
    "v_add_f32 v70, v70, v72\n\t"
    "ds_read_b128 v[248:251], v20 offset:1120\n\t"
    "ds_read_b128 v[252:255], v20 offset:38496\n\t"
    "ds_read_b128 v[216:219], v12 offset:160\n\t"
    "ds_read_b128 v[220:223], v12 offset:6304\n\t"
    "s_waitcnt vmcnt(8) lgkmcnt(8)\n\t"
    "v_mfma_f32_32x32x16_bf16 v[112:127], v[108:111], v[224:227], v[112:127]\n\t"
    "ds_read_b64_tr_b16 v[104:105], v5 offset:10240\n\t"
    "buffer_load_dwordx4 v3, s[48:51], s68 offen offset:1024 lds\n\t"
    "ds_read_b64_tr_b16 v[106:107], v5 offset:10496\n\t"
    "v_cos_f32 v77, v70\n\t"
    "v_mfma_f32_32x32x16_bf16 v[128:143], v[108:111], v[228:231], v[128:143]\n\t"
    "s_nop 0\n\t"
    "v_fma_f32 v78, v69, v77, -v76\n\t"
    "s_waitcnt vmcnt(8) lgkmcnt(6)\n\t"
    "v_mfma_f32_32x32x16_bf16 v[112:127], v[100:103], v[232:235], v[112:127]\n\t"
    "ds_read_b64_tr_b16 v[108:109], v5 offset:11264\n\t"
    "buffer_load_dwordx4 v4, s[48:51], s68 offen offset:2048 lds\n\t"
    "s_add_u32 s68, s68, 55296\n\t"
    "ds_read_b64_tr_b16 v[110:111], v5 offset:11520\n\t"
    "v_fma_f32 v79, v69, v78, -v77\n\t"
    "v_mfma_f32_32x32x16_bf16 v[128:143], v[100:103], v[236:239], v[128:143]\n\t"
    "v_cvt_pk_bf16_f32 v96, v76, v77\n\t"
    "v_fma_f32 v80, v69, v79, -v78\n\t"
    "s_waitcnt vmcnt(8) lgkmcnt(2)\n\t"
    "v_mfma_f32_32x32x16_bf16 v[112:127], v[104:107], v[240:243], v[112:127]\n\t"
    "s_add_u32 m0, s69, 9216\n\t"
    "ds_read_b64_tr_b16 v[100:101], v5 offset:0\n\t"
    "buffer_load_dwordx4 v2, s[48:51], s68 offen offset:0 lds\n\t"
    "ds_read_b64_tr_b16 v[102:103], v5 offset:256\n\t"
    "v_fma_f32 v81, v69, v80, -v79\n\t"
    "v_mfma_f32_32x32x16_bf16 v[128:143], v[104:107], v[244:247], v[128:143]\n\t"
    "v_cvt_pk_bf16_f32 v97, v78, v79\n\t"
    "v_fma_f32 v82, v69, v81, -v80\n\t"
    "s_waitcnt vmcnt(8) lgkmcnt(2)\n\t"
    "v_mfma_f32_32x32x16_bf16 v[112:127], v[108:111], v[248:251], v[112:127]\n\t"
    "ds_read_b64_tr_b16 v[104:105], v5 offset:1024\n\t"
    "buffer_load_dwordx4 v3, s[48:51], s68 offen offset:1024 lds\n\t"
    "ds_read_b64_tr_b16 v[106:107], v5 offset:1280\n\t"
    "v_fma_f32 v83, v69, v82, -v81\n\t"
    "v_mfma_f32_32x32x16_bf16 v[128:143], v[108:111], v[252:255], v[128:143]\n\t"
    "v_cvt_pk_bf16_f32 v98, v80, v81\n\t"
    "v_cvt_pk_bf16_f32 v99, v82, v83\n\t"
    "s_nop 1\n\t"
    "v_mfma_f32_32x32x16_bf16 v[112:127], v[96:99], v[216:219], v[112:127]\n\t"
    "v_mfma_f32_32x32x16_bf16 v[128:143], v[96:99], v[220:223], v[128:143]\n\t"
    "s_bfe_u32 s70, s89, 327685\n\t"
    "s_lshl_b32 s70, s70, 5\n\t"
    "v_cvt_f32_u32 v71, s70\n\t"
    "v_add_f32 v71, v71, v18\n\t"
    "v_mul_f32 v72, 0x3ae38e39, v71\n\t"
    "v_cos_f32 v69, v72\n\t"
    "s_nop 0\n\t"
    "v_add_f32 v69, v69, v69\n\t"
    "s_waitcnt vmcnt(8) lgkmcnt(2)\n\t"
    "v_mfma_f32_32x32x16_bf16 v[144:159], v[100:103], a[0:3], 0\n\t"
    "ds_read_b64_tr_b16 v[108:109], v5 offset:2048\n\t"
    "buffer_load_dwordx4 v4, s[48:51], s68 offen offset:2048 lds\n\t"
    "s_add_u32 s68, s68, 55296\n\t"
    "ds_read_b64_tr_b16 v[110:111], v5 offset:2304\n\t"
    "v_mul_f32 v70, v71, v73\n\t"
    "v_mfma_f32_32x32x16_bf16 v[160:175], v[100:103], a[4:7], 0\n\t"
    "v_fract_f32 v70, v70\n\t"
    "s_waitcnt vmcnt(8) lgkmcnt(2)\n\t"
    "v_mfma_f32_32x32x16_bf16 v[144:159], v[104:107], a[8:11], v[144:159]\n\t"
    "s_add_u32 m0, s69, 0\n\t"
    "ds_read_b64_tr_b16 v[100:101], v5 offset:3072\n\t"
    "buffer_load_dwordx4 v2, s[48:51], s68 offen offset:0 lds\n\t"
    "ds_read_b64_tr_b16 v[102:103], v5 offset:3328\n\t"
    "v_cos_f32 v76, v70\n\t"
    "s_bfe_u32 s93, s89, 327680\n\t"
    "s_add_u32 s71, s93, s66\n\t"
    "v_mfma_f32_32x32x16_bf16 v[160:175], v[104:107], a[12:15], v[160:175]\n\t"
    "v_add_f32 v70, v70, v72\n\t"
    "s_add_u32 s71, s71, s65\n\t"
    "s_and_b32 s71, s71, 7\n\t"
    "s_lshl_b32 s71, s71, 12\n\t"
    "s_lshl_b32 s93, s93, 15\n\t"
    "s_waitcnt vmcnt(8) lgkmcnt(2)\n\t"
    "v_mfma_f32_32x32x16_bf16 v[144:159], v[108:111], a[16:19], v[144:159]\n\t"
    "ds_read_b64_tr_b16 v[104:105], v5 offset:4096\n\t"
    "buffer_load_dwordx4 v3, s[48:51], s68 offen offset:1024 lds\n\t"
    "ds_read_b64_tr_b16 v[106:107], v5 offset:4352\n\t"
    "v_cos_f32 v77, v70\n\t"
    "s_add_u32 s93, s93, s91\n\t"
    "s_add_u32 s93, s93, s71\n\t"
    "v_mfma_f32_32x32x16_bf16 v[160:175], v[108:111], a[20:23], v[160:175]\n\t"
    "s_nop 0\n\t"
    "v_fma_f32 v78, v69, v77, -v76\n\t"
    "v_cvt_pk_bf16_f32 v84, v112, v113\n\t"
    "v_cvt_pk_bf16_f32 v85, v114, v115\n\t"
    "v_cvt_pk_bf16_f32 v86, v116, v117\n\t"
    "v_cvt_pk_bf16_f32 v87, v118, v119\n\t"
    "s_waitcnt vmcnt(8) lgkmcnt(2)\n\t"
    "v_mfma_f32_32x32x16_bf16 v[144:159], v[100:103], a[24:27], v[144:159]\n\t"
    "ds_read_b64_tr_b16 v[108:109], v5 offset:5120\n\t"
    "buffer_load_dwordx4 v4, s[48:51], s68 offen offset:2048 lds\n\t"
    "s_add_u32 s68, s68, 55296\n\t"
    "ds_read_b64_tr_b16 v[110:111], v5 offset:5376\n\t"
    "v_fma_f32 v79, v69, v78, -v77\n\t"
    "v_mfma_f32_32x32x16_bf16 v[160:175], v[100:103], a[28:31], v[160:175]\n\t"
    "v_cvt_pk_bf16_f32 v92, v76, v77\n\t"
    "v_fma_f32 v80, v69, v79, -v78\n\t"
    "buffer_store_dwordx4 v[84:87], v6, s[40:43], s93 offen offset:0 sc1\n\t"
    "v_cvt_pk_bf16_f32 v88, v120, v121\n\t"
    "v_cvt_pk_bf16_f32 v89, v122, v123\n\t"
    "v_cvt_pk_bf16_f32 v90, v124, v125\n\t"
    "s_waitcnt vmcnt(9) lgkmcnt(2)\n\t"
    "v_mfma_f32_32x32x16_bf16 v[144:159], v[104:107], a[32:35], v[144:159]\n\t"
    "s_add_u32 m0, s69, 3072\n\t"
    "ds_read_b64_tr_b16 v[100:101], v5 offset:6144\n\t"
    "buffer_load_dwordx4 v2, s[48:51], s68 offen offset:0 lds\n\t"
    "ds_read_b64_tr_b16 v[102:103], v5 offset:6400\n\t"
    "v_fma_f32 v81, v69, v80, -v79\n\t"
    "v_cvt_pk_bf16_f32 v91, v126, v127\n\t"
    "v_mfma_f32_32x32x16_bf16 v[160:175], v[104:107], a[36:39], v[160:175]\n\t"
    "v_cvt_pk_bf16_f32 v93, v78, v79\n\t"
    "v_fma_f32 v82, v69, v81, -v80\n\t"
    "buffer_store_dwordx4 v[88:91], v6, s[40:43], s93 offen offset:1024 sc1\n\t"
    "v_cvt_pk_bf16_f32 v84, v128, v129\n\t"
    "s_waitcnt vmcnt(10) lgkmcnt(2)\n\t"
    "v_mfma_f32_32x32x16_bf16 v[144:159], v[108:111], a[40:43], v[144:159]\n\t"
    "ds_read_b64_tr_b16 v[104:105], v5 offset:7168\n\t"
    "buffer_load_dwordx4 v3, s[48:51], s68 offen offset:1024 lds\n\t"
    "ds_read_b64_tr_b16 v[106:107], v5 offset:7424\n\t"
    "v_fma_f32 v83, v69, v82, -v81\n\t"
    "v_cvt_pk_bf16_f32 v85, v130, v131\n\t"
    "v_cvt_pk_bf16_f32 v86, v132, v133\n\t"
    "v_mfma_f32_32x32x16_bf16 v[160:175], v[108:111], a[44:47], v[160:175]\n\t"
    "v_cvt_pk_bf16_f32 v94, v80, v81\n\t"
    "v_cvt_pk_bf16_f32 v95, v82, v83\n\t"
    "v_cvt_pk_bf16_f32 v87, v134, v135\n\t"
    "buffer_store_dwordx4 v[84:87], v6, s[40:43], s93 offen offset:2048 sc1\n\t"
    "v_mfma_f32_32x32x16_bf16 v[144:159], v[92:95], v[176:179], v[144:159]\n\t"
    "v_cvt_pk_bf16_f32 v88, v136, v137\n\t"
    "v_cvt_pk_bf16_f32 v89, v138, v139\n\t"
    "v_mfma_f32_32x32x16_bf16 v[160:175], v[92:95], v[180:183], v[160:175]\n\t"
    "v_cvt_pk_bf16_f32 v90, v140, v141\n\t"
    "v_cvt_pk_bf16_f32 v91, v142, v143\n\t"
    "s_waitcnt vmcnt(11) lgkmcnt(2)\n\t"
    "v_mfma_f32_32x32x16_bf16 v[144:159], v[100:103], a[48:51], v[144:159]\n\t"
    "ds_read_b64_tr_b16 v[108:109], v5 offset:8192\n\t"
    "buffer_load_dwordx4 v4, s[48:51], s68 offen offset:2048 lds\n\t"
    "s_add_u32 s68, s68, 55296\n\t"
    "ds_read_b64_tr_b16 v[110:111], v5 offset:8448\n\t"
    "v_mul_f32 v70, v71, v74\n\t"
    "buffer_store_dwordx4 v[88:91], v6, s[40:43], s93 offen offset:3072 sc1\n\t"
    "v_mfma_f32_32x32x16_bf16 v[160:175], v[100:103], a[52:55], v[160:175]\n\t"
    "v_fract_f32 v70, v70\n\t"
    "s_waitcnt vmcnt(12) lgkmcnt(2)\n\t"
    "v_mfma_f32_32x32x16_bf16 v[144:159], v[104:107], a[56:59], v[144:159]\n\t"
    "s_add_u32 m0, s69, 6144\n\t"
    "ds_read_b64_tr_b16 v[100:101], v5 offset:9216\n\t"
    "buffer_load_dwordx4 v2, s[48:51], s68 offen offset:0 lds\n\t"
    "ds_read_b64_tr_b16 v[102:103], v5 offset:9472\n\t"
    "v_cos_f32 v76, v70\n\t"
    "v_mfma_f32_32x32x16_bf16 v[160:175], v[104:107], a[60:63], v[160:175]\n\t"
    "v_add_f32 v70, v70, v72\n\t"
    "s_waitcnt vmcnt(12) lgkmcnt(2)\n\t"
    "v_mfma_f32_32x32x16_bf16 v[144:159], v[108:111], a[64:67], v[144:159]\n\t"
    "ds_read_b64_tr_b16 v[104:105], v5 offset:10240\n\t"
    "buffer_load_dwordx4 v3, s[48:51], s68 offen offset:1024 lds\n\t"
    "ds_read_b64_tr_b16 v[106:107], v5 offset:10496\n\t"
    "v_cos_f32 v77, v70\n\t"
    "v_mfma_f32_32x32x16_bf16 v[160:175], v[108:111], a[68:71], v[160:175]\n\t"
    "s_nop 0\n\t"
    "v_fma_f32 v78, v69, v77, -v76\n\t"
    "v_lshrrev_b32 v13, 5, v1\n\t"
    "v_and_b32 v14, 31, v1\n\t"
    "s_cmp_eq_u32 s66, 2\n\t"
    "s_cselect_b32 s83, 16, 17\n\t"
    "s_cselect_b32 s85, 1, 0\n\t"
    "s_cmp_eq_u32 s66, 5\n\t"
    "s_cselect_b32 s85, 1, s85\n\t"
    "s_waitcnt vmcnt(12) lgkmcnt(2)\n\t"
    "v_mfma_f32_32x32x16_bf16 v[144:159], v[100:103], a[72:75], v[144:159]\n\t"
    "ds_read_b64_tr_b16 v[108:109], v5 offset:11264\n\t"
    "buffer_load_dwordx4 v4, s[48:51], s68 offen offset:2048 lds\n\t"
    "s_add_u32 s68, s68, 55296\n\t"
    "ds_read_b64_tr_b16 v[110:111], v5 offset:11520\n\t"
    "v_fma_f32 v79, v69, v78, -v77\n\t"
    "v_mfma_f32_32x32x16_bf16 v[160:175], v[100:103], a[76:79], v[160:175]\n\t"
    "v_cvt_pk_bf16_f32 v96, v76, v77\n\t"
    "v_fma_f32 v80, v69, v79, -v78\n\t"
    "v_mov_b32 v17, 2146435072\n\t"
    "s_bfe_u32 s81, s89, 327700\n\t"
    "s_lshl_b32 s82, s88, 5\n\t"
    "s_waitcnt vmcnt(12) lgkmcnt(2)\n\t"
    "v_mfma_f32_32x32x16_bf16 v[144:159], v[104:107], a[80:83], v[144:159]\n\t"
    "s_add_u32 m0, s69, 9216\n\t"
    "ds_read_b64_tr_b16 v[100:101], v5 offset:0\n\t"
    "buffer_load_dwordx4 v2, s[48:51], s68 offen offset:0 lds\n\t"
    "ds_read_b64_tr_b16 v[102:103], v5 offset:256\n\t"
    "v_fma_f32 v81, v69, v80, -v79\n\t"
    "v_mfma_f32_32x32x16_bf16 v[160:175], v[104:107], a[84:87], v[160:175]\n\t"
    "v_cvt_pk_bf16_f32 v97, v78, v79\n\t"
    "v_fma_f32 v82, v69, v81, -v80\n\t"
    "s_lshl_b32 s81, s81, 6\n\t"
    "s_add_u32 s81, s81, 0\n\t"
    "v_add_u32 v64, s82, v14\n\t"
    "v_lshlrev_b32 v64, 5, v64\n\t"
    "s_waitcnt vmcnt(12) lgkmcnt(2)\n\t"
    "v_mfma_f32_32x32x16_bf16 v[144:159], v[108:111], a[88:91], v[144:159]\n\t"
    "ds_read_b64_tr_b16 v[104:105], v5 offset:1024\n\t"
    "buffer_load_dwordx4 v3, s[48:51], s68 offen offset:1024 lds\n\t"
    "ds_read_b64_tr_b16 v[106:107], v5 offset:1280\n\t"
    "v_fma_f32 v83, v69, v82, -v81\n\t"
    "v_mfma_f32_32x32x16_bf16 v[160:175], v[108:111], a[92:95], v[160:175]\n\t"
    "v_cvt_pk_bf16_f32 v98, v80, v81\n\t"
    "v_cvt_pk_bf16_f32 v99, v82, v83\n\t"
    "v_add_u32 v64, s65, v64\n\t"
    "v_mul_u32_u24 v64, 1152, v64\n\t"
    "v_lshl_add_u32 v64, v13, 3, v64\n\t"
    "v_mfma_f32_32x32x16_bf16 v[144:159], v[96:99], v[184:187], v[144:159]\n\t"
    "v_mfma_f32_32x32x16_bf16 v[160:175], v[96:99], v[188:191], v[160:175]\n\t"
    "s_waitcnt vmcnt(12) lgkmcnt(2)\n\t"
    "v_mfma_f32_32x32x16_bf16 v[144:159], v[100:103], a[96:99], v[144:159]\n\t"
    "ds_read_b64_tr_b16 v[108:109], v5 offset:2048\n\t"
    "buffer_load_dwordx4 v4, s[48:51], s68 offen offset:2048 lds\n\t"
    "s_add_u32 s68, s68, 55296\n\t"
    "ds_read_b64_tr_b16 v[110:111], v5 offset:2304\n\t"
    "v_mul_f32 v70, v71, v75\n\t"
    "v_mfma_f32_32x32x16_bf16 v[160:175], v[100:103], a[100:103], v[160:175]\n\t"
    "v_fract_f32 v70, v70\n\t"
    "v_add_u32 v64, s81, v64\n\t"
    "s_bfe_u32 s81, s89, 327700\n\t"
    "s_lshl_b32 s82, s88, 5\n\t"
    "s_waitcnt vmcnt(11) lgkmcnt(2)\n\t"
    "v_mfma_f32_32x32x16_bf16 v[144:159], v[104:107], a[104:107], v[144:159]\n\t"
    "s_add_u32 m0, s69, 0\n\t"
    "ds_read_b64_tr_b16 v[100:101], v5 offset:3072\n\t"
    "buffer_load_dwordx4 v2, s[48:51], s68 offen offset:0 lds\n\t"
    "ds_read_b64_tr_b16 v[102:103], v5 offset:3328\n\t"
    "v_cos_f32 v76, v70\n\t"
    "v_mfma_f32_32x32x16_bf16 v[160:175], v[104:107], a[108:111], v[160:175]\n\t"
    "v_add_f32 v70, v70, v72\n\t"
    "s_lshl_b32 s81, s81, 6\n\t"
    "s_add_u32 s81, s81, 32\n\t"
    "v_add_u32 v65, s82, v14\n\t"
    "v_lshlrev_b32 v65, 5, v65\n\t"
    "s_waitcnt vmcnt(10) lgkmcnt(2)\n\t"
    "v_mfma_f32_32x32x16_bf16 v[144:159], v[108:111], a[112:115], v[144:159]\n\t"
    "ds_read_b64_tr_b16 v[104:105], v5 offset:4096\n\t"
    "buffer_load_dwordx4 v3, s[48:51], s68 offen offset:1024 lds\n\t"
    "ds_read_b64_tr_b16 v[106:107], v5 offset:4352\n\t"
    "v_cos_f32 v77, v70\n\t"
    "v_mfma_f32_32x32x16_bf16 v[160:175], v[108:111], a[116:119], v[160:175]\n\t"
    "s_nop 0\n\t"
    "v_fma_f32 v78, v69, v77, -v76\n\t"
    "v_add_u32 v65, s65, v65\n\t"
    "v_mul_u32_u24 v65, 1152, v65\n\t"
    "v_lshl_add_u32 v65, v13, 3, v65\n\t"
    "s_waitcnt vmcnt(9) lgkmcnt(2)\n\t"
    "v_mfma_f32_32x32x16_bf16 v[144:159], v[100:103], a[120:123], v[144:159]\n\t"
    "ds_read_b64_tr_b16 v[108:109], v5 offset:5120\n\t"
    "buffer_load_dwordx4 v4, s[48:51], s68 offen offset:2048 lds\n\t"
    "s_add_u32 s68, s68, 55296\n\t"
    "ds_read_b64_tr_b16 v[110:111], v5 offset:5376\n\t"
    "v_fma_f32 v79, v69, v78, -v77\n\t"
    "v_mfma_f32_32x32x16_bf16 v[160:175], v[100:103], a[124:127], v[160:175]\n\t"
    "v_cvt_pk_bf16_f32 v92, v76, v77\n\t"
    "v_fma_f32 v80, v69, v79, -v78\n\t"
    "v_add_u32 v65, s81, v65\n\t"
    "s_lshr_b32 s82, s64, 1\n\t"
    "s_lshl_b32 s82, s82, 5\n\t"
    "s_waitcnt vmcnt(8) lgkmcnt(2)\n\t"
    "s_bfe_u32 s70, s89, 327680\n\t"
    "s_lshl_b32 s70, s70, 6\n\t"
    "s_add_u32 s70, s70, s92\n\t"
    "buffer_store_dword v19, v7, s[40:43], s70 offen sc1\n\t"
    "v_mfma_f32_32x32x16_bf16 v[144:159], v[104:107], a[128:131], v[144:159]\n\t"
    "s_add_u32 m0, s69, 3072\n\t"
    "ds_read_b64_tr_b16 v[100:101], v5 offset:6144\n\t"
    "buffer_load_dwordx4 v2, s[48:51], s68 offen offset:0 lds\n\t"
    "ds_read_b64_tr_b16 v[102:103], v5 offset:6400\n\t"
    "v_fma_f32 v81, v69, v80, -v79\n\t"
    "v_mfma_f32_32x32x16_bf16 v[160:175], v[104:107], a[132:135], v[160:175]\n\t"
    "v_cvt_pk_bf16_f32 v93, v78, v79\n\t"
    "v_fma_f32 v82, v69, v81, -v80\n\t"
    "s_lshl_b32 s81, s83, 6\n\t"
    "s_and_b32 s84, s64, 1\n\t"
    "s_lshl_b32 s84, s84, 5\n\t"
    "s_add_u32 s81, s81, s84\n\t"
    "s_waitcnt vmcnt(9) lgkmcnt(2)\n\t"
    "v_mfma_f32_32x32x16_bf16 v[144:159], v[108:111], a[136:139], v[144:159]\n\t"
    "ds_read_b64_tr_b16 v[104:105], v5 offset:7168\n\t"
    "buffer_load_dwordx4 v3, s[48:51], s68 offen offset:1024 lds\n\t"
    "ds_read_b64_tr_b16 v[106:107], v5 offset:7424\n\t"
    "v_fma_f32 v83, v69, v82, -v81\n\t"
    "v_mfma_f32_32x32x16_bf16 v[160:175], v[108:111], a[140:143], v[160:175]\n\t"
    "v_cvt_pk_bf16_f32 v94, v80, v81\n\t"
    "v_cvt_pk_bf16_f32 v95, v82, v83\n\t"
    "v_add_u32 v66, s82, v14\n\t"
    "v_lshlrev_b32 v66, 5, v66\n\t"
    "v_add_u32 v66, s65, v66\n\t"
    "v_mfma_f32_32x32x16_bf16 v[144:159], v[92:95], v[192:195], v[144:159]\n\t"
    "v_mfma_f32_32x32x16_bf16 v[160:175], v[92:95], v[196:199], v[160:175]\n\t"
    "s_waitcnt vmcnt(9) lgkmcnt(2)\n\t"
    "v_mfma_f32_32x32x16_bf16 v[144:159], v[100:103], a[144:147], v[144:159]\n\t"
    "ds_read_b64_tr_b16 v[108:109], v5 offset:8192\n\t"
    "buffer_load_dwordx4 v4, s[48:51], s68 offen offset:2048 lds\n\t"
    "s_add_u32 s68, s68, 55296\n\t"
    "ds_read_b64_tr_b16 v[110:111], v5 offset:8448\n\t"
    "v_fmaak_f32 v70, v71, v73, 0x3e800000\n\t"
    "v_mfma_f32_32x32x16_bf16 v[160:175], v[100:103], a[148:151], v[160:175]\n\t"
    "v_fract_f32 v70, v70\n\t"
    "v_mul_u32_u24 v66, 1152, v66\n\t"
    "v_lshl_add_u32 v66, v13, 3, v66\n\t"
    "v_add_u32 v66, s81, v66\n\t"
    "s_waitcnt vmcnt(9) lgkmcnt(2)\n\t"
    "v_mfma_f32_32x32x16_bf16 v[144:159], v[104:107], a[152:155], v[144:159]\n\t"
    "s_add_u32 m0, s69, 6144\n\t"
    "ds_read_b64_tr_b16 v[100:101], v5 offset:9216\n\t"
    "buffer_load_dwordx4 v2, s[48:51], s68 offen offset:0 lds\n\t"
    "ds_read_b64_tr_b16 v[102:103], v5 offset:9472\n\t"
    "v_cos_f32 v76, v70\n\t"
    "v_mfma_f32_32x32x16_bf16 v[160:175], v[104:107], a[156:159], v[160:175]\n\t"
    "v_add_f32 v70, v70, v72\n\t"
    "s_cmp_eq_u32 s85, 1\n\t"
    "s_cselect_b64 vcc, -1, 0\n\t"
    "v_cndmask_b32 v66, v17, v66, vcc\n\t"
    "v_cndmask_b32 v68, v17, v6, vcc\n\t"
    "s_bfe_u32 s81, s89, 327700\n\t"
    "s_waitcnt vmcnt(9) lgkmcnt(2)\n\t"
    "v_mfma_f32_32x32x16_bf16 v[144:159], v[108:111], a[160:163], v[144:159]\n\t"
    "ds_read_b64_tr_b16 v[104:105], v5 offset:10240\n\t"
    "buffer_load_dwordx4 v3, s[48:51], s68 offen offset:1024 lds\n\t"
    "ds_read_b64_tr_b16 v[106:107], v5 offset:10496\n\t"
    "v_cos_f32 v77, v70\n\t"
    "v_mfma_f32_32x32x16_bf16 v[160:175], v[108:111], a[164:167], v[160:175]\n\t"
    "s_nop 0\n\t"
    "v_fma_f32 v78, v69, v77, -v76\n\t"
    "s_lshl_b32 s76, s81, 15\n\t"
    "s_add_u32 s76, s76, s91\n\t"
    "s_add_u32 s79, s81, s66\n\t"
    "s_waitcnt vmcnt(9) lgkmcnt(2)\n\t"
    "v_mfma_f32_32x32x16_bf16 v[144:159], v[100:103], a[168:171], v[144:159]\n\t"
    "ds_read_b64_tr_b16 v[108:109], v5 offset:11264\n\t"
    "buffer_load_dwordx4 v4, s[48:51], s68 offen offset:2048 lds\n\t"
    "s_add_u32 s68, s68, 55296\n\t"
    "ds_read_b64_tr_b16 v[110:111], v5 offset:11520\n\t"
    "v_fma_f32 v79, v69, v78, -v77\n\t"
    "v_mfma_f32_32x32x16_bf16 v[160:175], v[100:103], a[172:175], v[160:175]\n\t"
    "v_cvt_pk_bf16_f32 v96, v76, v77\n\t"
    "v_fma_f32 v80, v69, v79, -v78\n\t"
    "s_mov_b32 s80, s83\n\t"
    "s_add_u32 s79, s79, s65\n\t"
    "s_add_u32 s80, s80, s65\n\t"
    "s_waitcnt vmcnt(9) lgkmcnt(2)\n\t"
    "v_mfma_f32_32x32x16_bf16 v[144:159], v[104:107], a[176:179], v[144:159]\n\t"
    "s_add_u32 m0, s69, 9216\n\t"
    "ds_read_b64_tr_b16 v[100:101], v5 offset:0\n\t"
    "buffer_load_dwordx4 v2, s[48:51], s68 offen offset:0 lds\n\t"
    "ds_read_b64_tr_b16 v[102:103], v5 offset:256\n\t"
    "v_fma_f32 v81, v69, v80, -v79\n\t"
    "v_mfma_f32_32x32x16_bf16 v[160:175], v[104:107], a[180:183], v[160:175]\n\t"
    "v_cvt_pk_bf16_f32 v97, v78, v79\n\t"
    "v_fma_f32 v82, v69, v81, -v80\n\t"
    "s_lshl_b32 s77, s83, 15\n\t"
    "s_add_u32 s77, s77, s91\n\t"
    "s_lshl_b32 s82, s64, 10\n\t"
    "s_add_u32 s77, s77, s82\n\t"
    "s_waitcnt vmcnt(9) lgkmcnt(2)\n\t"
    "v_mfma_f32_32x32x16_bf16 v[144:159], v[108:111], a[184:187], v[144:159]\n\t"
    "ds_read_b64_tr_b16 v[104:105], v5 offset:1024\n\t"
    "buffer_load_dwordx4 v3, s[48:51], s68 offen offset:1024 lds\n\t"
    "ds_read_b64_tr_b16 v[106:107], v5 offset:1280\n\t"
    "v_fma_f32 v83, v69, v82, -v81\n\t"
    "v_mfma_f32_32x32x16_bf16 v[160:175], v[108:111], a[188:191], v[160:175]\n\t"
    "v_cvt_pk_bf16_f32 v98, v80, v81\n\t"
    "v_cvt_pk_bf16_f32 v99, v82, v83\n\t"
    "v_and_b32 v15, 7, v1\n\t"
    "v_lshlrev_b32 v16, 3, v15\n\t"
    "s_lshl_b32 s81, s81, 6\n\t"
    "v_mfma_f32_32x32x16_bf16 v[144:159], v[96:99], v[200:203], v[144:159]\n\t"
    "v_mfma_f32_32x32x16_bf16 v[160:175], v[96:99], v[204:207], v[160:175]\n\t"
    "s_waitcnt vmcnt(9) lgkmcnt(2)\n\t"
    "v_mfma_f32_32x32x16_bf16 v[144:159], v[100:103], a[192:195], v[144:159]\n\t"
    "ds_read_b64_tr_b16 v[108:109], v5 offset:2048\n\t"
    "buffer_load_dwordx4 v4, s[48:51], s68 offen offset:2048 lds\n\t"
    "s_bfe_u32 s70, s89, 327690\n\t"
    "s_lshl_b32 s70, s70, 6\n\t"
    "s_add_u32 s68, s90, s70\n\t"
    "ds_read_b64_tr_b16 v[110:111], v5 offset:2304\n\t"
    "v_fmaak_f32 v70, v71, v74, 0x3e800000\n\t"
    "v_mfma_f32_32x32x16_bf16 v[160:175], v[100:103], a[196:199], v[160:175]\n\t"
    "v_fract_f32 v70, v70\n\t"
    "s_lshl_b32 s82, s88, 2\n\t"
    "s_add_u32 s81, s81, s82\n\t"
    "s_lshl_b32 s82, s83, 6\n\t"
    "s_waitcnt vmcnt(8) lgkmcnt(2)\n\t"
    "v_mfma_f32_32x32x16_bf16 v[144:159], v[104:107], a[200:203], v[144:159]\n\t"
    "s_add_u32 m0, s69, 0\n\t"
    "ds_read_b64_tr_b16 v[100:101], v5 offset:3072\n\t"
    "buffer_load_dwordx4 v2, s[48:51], s68 offen offset:0 lds\n\t"
    "ds_read_b64_tr_b16 v[102:103], v5 offset:3328\n\t"
    "v_cos_f32 v76, v70\n\t"
    "v_mfma_f32_32x32x16_bf16 v[160:175], v[104:107], a[204:207], v[160:175]\n\t"
    "v_add_f32 v70, v70, v72\n\t"
    "s_lshr_b32 s84, s64, 1\n\t"
    "s_lshl_b32 s84, s84, 2\n\t"
    "s_add_u32 s82, s82, s84\n\t"
    "s_mul_i32 s84, s65, 1152\n\t"
    "s_waitcnt vmcnt(8) lgkmcnt(2)\n\t"
    "v_mfma_f32_32x32x16_bf16 v[144:159], v[108:111], a[208:211], v[144:159]\n\t"
    "ds_read_b64_tr_b16 v[104:105], v5 offset:4096\n\t"
    "buffer_load_dwordx4 v3, s[48:51], s68 offen offset:1024 lds\n\t"
    "ds_read_b64_tr_b16 v[106:107], v5 offset:4352\n\t"
    "v_cos_f32 v77, v70\n\t"
    "v_mfma_f32_32x32x16_bf16 v[160:175], v[108:111], a[212:215], v[160:175]\n\t"
    "s_nop 0\n\t"
    "v_fma_f32 v78, v69, v77, -v76\n\t"
    "s_add_u32 s84, s84, 4096\n\t"
    "s_add_u32 s81, s81, s84\n\t"
    "s_add_u32 s82, s82, s84\n\t"
    "s_waitcnt vmcnt(8) lgkmcnt(2)\n\t"
    "v_mfma_f32_32x32x16_bf16 v[144:159], v[100:103], a[216:219], v[144:159]\n\t"
    "ds_read_b64_tr_b16 v[108:109], v5 offset:5120\n\t"
    "buffer_load_dwordx4 v4, s[48:51], s68 offen offset:2048 lds\n\t"
    "s_add_u32 s68, s68, 55296\n\t"
    "ds_read_b64_tr_b16 v[110:111], v5 offset:5376\n\t"
    "v_fma_f32 v79, v69, v78, -v77\n\t"
    "v_mfma_f32_32x32x16_bf16 v[160:175], v[100:103], a[220:223], v[160:175]\n\t"
    "v_cvt_pk_bf16_f32 v92, v76, v77\n\t"
    "v_fma_f32 v80, v69, v79, -v78\n\t"
    "s_lshl_b32 s84, s66, 7\n\t"
    "s_add_u32 s84, s84, 256\n\t"
    "s_lshl_b32 s83, s65, 2\n\t"
    "s_waitcnt vmcnt(8) lgkmcnt(2)\n\t"
    "v_mfma_f32_32x32x16_bf16 v[144:159], v[104:107], a[224:227], v[144:159]\n\t"
    "s_add_u32 m0, s69, 3072\n\t"
    "ds_read_b64_tr_b16 v[100:101], v5 offset:6144\n\t"
    "buffer_load_dwordx4 v2, s[48:51], s68 offen offset:0 lds\n\t"
    "ds_read_b64_tr_b16 v[102:103], v5 offset:6400\n\t"
    "v_fma_f32 v81, v69, v80, -v79\n\t"
    "v_mfma_f32_32x32x16_bf16 v[160:175], v[104:107], a[228:231], v[160:175]\n\t"
    "v_cvt_pk_bf16_f32 v93, v78, v79\n\t"
    "v_fma_f32 v82, v69, v81, -v80\n\t"
    "s_add_u32 s84, s84, s83\n\t"
    "v_add_u32 v13, s81, v16\n\t"
    "v_add_u32 v14, s82, v16\n\t"
    "v_mov_b32 v17, s84\n\t"
    "s_waitcnt vmcnt(8) lgkmcnt(2)\n\t"
    "v_mfma_f32_32x32x16_bf16 v[144:159], v[108:111], a[232:235], v[144:159]\n\t"
    "ds_read_b64_tr_b16 v[104:105], v5 offset:7168\n\t"
    "buffer_load_dwordx4 v3, s[48:51], s68 offen offset:1024 lds\n\t"
    "ds_read_b64_tr_b16 v[106:107], v5 offset:7424\n\t"
    "v_fma_f32 v83, v69, v82, -v81\n\t"
    "v_mfma_f32_32x32x16_bf16 v[160:175], v[108:111], a[236:239], v[160:175]\n\t"
    "v_cvt_pk_bf16_f32 v94, v80, v81\n\t"
    "v_cvt_pk_bf16_f32 v95, v82, v83\n\t"
    "s_cmp_eq_u32 s85, 1\n\t"
    "s_cselect_b64 vcc, -1, 0\n\t"
    "v_cndmask_b32 v14, v17, v14, vcc\n\t"
    "v_cmp_lt_u32 vcc, 1, v15\n\t"
    "v_mfma_f32_32x32x16_bf16 v[144:159], v[92:95], v[208:211], v[144:159]\n\t"
    "v_mfma_f32_32x32x16_bf16 v[160:175], v[92:95], v[212:215], v[160:175]\n\t"
    "s_waitcnt vmcnt(8) lgkmcnt(2)\n\t"
    "v_mfma_f32_32x32x16_bf16 v[144:159], v[100:103], a[240:243], v[144:159]\n\t"
    "ds_read_b64_tr_b16 v[108:109], v5 offset:8192\n\t"
    "buffer_load_dwordx4 v4, s[48:51], s68 offen offset:2048 lds\n\t"
    "s_add_u32 s68, s68, 55296\n\t"
    "ds_read_b64_tr_b16 v[110:111], v5 offset:8448\n\t"
    "v_fmaak_f32 v70, v71, v75, 0x3e800000\n\t"
    "v_mfma_f32_32x32x16_bf16 v[160:175], v[100:103], a[244:247], v[160:175]\n\t"
    "v_fract_f32 v70, v70\n\t"
    "v_cndmask_b32 v16, v17, v14, vcc\n\t"
    "v_cndmask_b32 v14, v14, v17, vcc\n\t"
    "v_subrev_u32 v15, s66, v15\n\t"
    "s_waitcnt vmcnt(8) lgkmcnt(2)\n\t"
    "v_mfma_f32_32x32x16_bf16 v[144:159], v[104:107], a[248:251], v[144:159]\n\t"
    "s_add_u32 m0, s69, 6144\n\t"
    "ds_read_b64_tr_b16 v[100:101], v5 offset:9216\n\t"
    "buffer_load_dwordx4 v2, s[48:51], s68 offen offset:0 lds\n\t"
    "ds_read_b64_tr_b16 v[102:103], v5 offset:9472\n\t"
    "v_cos_f32 v76, v70\n\t"
    "v_mfma_f32_32x32x16_bf16 v[160:175], v[104:107], a[252:255], v[160:175]\n\t"
    "v_add_f32 v70, v70, v72\n\t"
    "v_and_b32 v15, 7, v15\n\t"
    "v_cmp_lt_u32 vcc, 2, v15\n\t"
    "s_nop 1\n\t"
    "v_cndmask_b32 v12, v17, v13, vcc\n\t"
    "v_cndmask_b32 v67, v13, v17, vcc\n\t"
    "s_waitcnt vmcnt(8) lgkmcnt(2)\n\t"
    "v_mfma_f32_32x32x16_bf16 v[144:159], v[108:111], v[224:227], v[144:159]\n\t"
    "ds_read_b64_tr_b16 v[104:105], v5 offset:10240\n\t"
    "buffer_load_dwordx4 v3, s[48:51], s68 offen offset:1024 lds\n\t"
    "ds_read_b64_tr_b16 v[106:107], v5 offset:10496\n\t"
    "v_cos_f32 v77, v70\n\t"
    "v_mfma_f32_32x32x16_bf16 v[160:175], v[108:111], v[228:231], v[160:175]\n\t"
    "s_nop 0\n\t"
    "v_fma_f32 v78, v69, v77, -v76\n\t"
    "v_cmp_eq_u32 vcc, 0, v15\n\t"
    "s_nop 1\n\t"
    "v_cndmask_b32 v67, v67, v17, vcc\n\t"
    "v_bfe_u32 v15, v1, 3, 1\n\t"
    "s_waitcnt vmcnt(8) lgkmcnt(2)\n\t"
    "v_mfma_f32_32x32x16_bf16 v[144:159], v[100:103], v[232:235], v[144:159]\n\t"
    "ds_read_b64_tr_b16 v[108:109], v5 offset:11264\n\t"
    "buffer_load_dwordx4 v4, s[48:51], s68 offen offset:2048 lds\n\t"
    "s_add_u32 s68, s68, 55296\n\t"
    "ds_read_b64_tr_b16 v[110:111], v5 offset:11520\n\t"
    "v_fma_f32 v79, v69, v78, -v77\n\t"
    "v_mfma_f32_32x32x16_bf16 v[160:175], v[100:103], v[236:239], v[160:175]\n\t"
    "v_cvt_pk_bf16_f32 v96, v76, v77\n\t"
    "v_fma_f32 v80, v69, v79, -v78\n\t"
    "v_cmp_eq_u32 vcc, 1, v15\n\t"
    "s_nop 1\n\t"
    "v_cndmask_b32 v12, v12, v16, vcc\n\t"
    "v_cndmask_b32 v67, v67, v14, vcc\n\t"
    "s_waitcnt vmcnt(8) lgkmcnt(2)\n\t"
    "v_mfma_f32_32x32x16_bf16 v[144:159], v[104:107], v[240:243], v[144:159]\n\t"
    "s_add_u32 m0, s69, 9216\n\t"
    "ds_read_b64_tr_b16 v[100:101], v5 offset:0\n\t"
    "buffer_load_dwordx4 v2, s[48:51], s68 offen offset:0 lds\n\t"
    "ds_read_b64_tr_b16 v[102:103], v5 offset:256\n\t"
    "v_fma_f32 v81, v69, v80, -v79\n\t"
    "v_mfma_f32_32x32x16_bf16 v[160:175], v[104:107], v[244:247], v[160:175]\n\t"
    "v_cvt_pk_bf16_f32 v97, v78, v79\n\t"
    "v_fma_f32 v82, v69, v81, -v80\n\t"
    "s_waitcnt vmcnt(8) lgkmcnt(2)\n\t"
    "v_mfma_f32_32x32x16_bf16 v[144:159], v[108:111], v[248:251], v[144:159]\n\t"
    "ds_read_b64_tr_b16 v[104:105], v5 offset:1024\n\t"
    "buffer_load_dwordx4 v3, s[48:51], s68 offen offset:1024 lds\n\t"
    "ds_read_b64_tr_b16 v[106:107], v5 offset:1280\n\t"
    "v_fma_f32 v83, v69, v82, -v81\n\t"
    "v_mfma_f32_32x32x16_bf16 v[160:175], v[108:111], v[252:255], v[160:175]\n\t"
    "v_cvt_pk_bf16_f32 v98, v80, v81\n\t"
    "v_cvt_pk_bf16_f32 v99, v82, v83\n\t"
    "s_nop 1\n\t"
    "v_mfma_f32_32x32x16_bf16 v[144:159], v[96:99], v[216:219], v[144:159]\n\t"
    "v_mfma_f32_32x32x16_bf16 v[160:175], v[96:99], v[220:223], v[160:175]\n\t"
    "s_bfe_u32 s70, s89, 327690\n\t"
    "s_lshl_b32 s70, s70, 5\n\t"
    "v_cvt_f32_u32 v71, s70\n\t"
    "v_add_f32 v71, v71, v18\n\t"
    "v_mul_f32 v72, 0x3ae38e39, v71\n\t"
    "v_cos_f32 v69, v72\n\t"
    "s_nop 0\n\t"
    "v_add_f32 v69, v69, v69\n\t"
    "s_waitcnt vmcnt(8) lgkmcnt(2)\n\t"
    "v_mfma_f32_32x32x16_bf16 v[112:127], v[100:103], a[0:3], 0\n\t"
    "ds_read_b64_tr_b16 v[108:109], v5 offset:2048\n\t"
    "buffer_load_dwordx4 v4, s[48:51], s68 offen offset:2048 lds\n\t"
    "s_add_u32 s68, s68, 55296\n\t"
    "ds_read_b64_tr_b16 v[110:111], v5 offset:2304\n\t"
    "v_mul_f32 v70, v71, v73\n\t"
    "v_mfma_f32_32x32x16_bf16 v[128:143], v[100:103], a[4:7], 0\n\t"
    "v_fract_f32 v70, v70\n\t"
    "s_waitcnt vmcnt(8) lgkmcnt(2)\n\t"
    "v_mfma_f32_32x32x16_bf16 v[112:127], v[104:107], a[8:11], v[112:127]\n\t"
    "s_add_u32 m0, s69, 0\n\t"
    "ds_read_b64_tr_b16 v[100:101], v5 offset:3072\n\t"
    "buffer_load_dwordx4 v2, s[48:51], s68 offen offset:0 lds\n\t"
    "ds_read_b64_tr_b16 v[102:103], v5 offset:3328\n\t"
    "v_cos_f32 v76, v70\n\t"
    "s_bfe_u32 s93, s89, 327685\n\t"
    "s_add_u32 s71, s93, s66\n\t"
    "v_mfma_f32_32x32x16_bf16 v[128:143], v[104:107], a[12:15], v[128:143]\n\t"
    "v_add_f32 v70, v70, v72\n\t"
    "s_add_u32 s71, s71, s65\n\t"
    "s_and_b32 s71, s71, 7\n\t"
    "s_lshl_b32 s71, s71, 12\n\t"
    "s_lshl_b32 s93, s93, 15\n\t"
    "s_waitcnt vmcnt(8) lgkmcnt(2)\n\t"
    "v_mfma_f32_32x32x16_bf16 v[112:127], v[108:111], a[16:19], v[112:127]\n\t"
    "ds_read_b64_tr_b16 v[104:105], v5 offset:4096\n\t"
    "buffer_load_dwordx4 v3, s[48:51], s68 offen offset:1024 lds\n\t"
    "ds_read_b64_tr_b16 v[106:107], v5 offset:4352\n\t"
    "v_cos_f32 v77, v70\n\t"
    "s_add_u32 s93, s93, s91\n\t"
    "s_add_u32 s93, s93, s71\n\t"
    "v_mfma_f32_32x32x16_bf16 v[128:143], v[108:111], a[20:23], v[128:143]\n\t"
    "s_nop 0\n\t"
    "v_fma_f32 v78, v69, v77, -v76\n\t"
    "v_cvt_pk_bf16_f32 v84, v144, v145\n\t"
    "v_cvt_pk_bf16_f32 v85, v146, v147\n\t"
    "v_cvt_pk_bf16_f32 v86, v148, v149\n\t"
    "v_cvt_pk_bf16_f32 v87, v150, v151\n\t"
    "s_waitcnt vmcnt(8) lgkmcnt(2)\n\t"
    "v_mfma_f32_32x32x16_bf16 v[112:127], v[100:103], a[24:27], v[112:127]\n\t"
    "ds_read_b64_tr_b16 v[108:109], v5 offset:5120\n\t"
    "buffer_load_dwordx4 v4, s[48:51], s68 offen offset:2048 lds\n\t"
    "s_add_u32 s68, s68, 55296\n\t"
    "ds_read_b64_tr_b16 v[110:111], v5 offset:5376\n\t"
    "v_fma_f32 v79, v69, v78, -v77\n\t"
    "v_mfma_f32_32x32x16_bf16 v[128:143], v[100:103], a[28:31], v[128:143]\n\t"
    "v_cvt_pk_bf16_f32 v92, v76, v77\n\t"
    "v_fma_f32 v80, v69, v79, -v78\n\t"
    "buffer_store_dwordx4 v[84:87], v6, s[40:43], s93 offen offset:0 sc1\n\t"
    "v_cvt_pk_bf16_f32 v88, v152, v153\n\t"
    "v_cvt_pk_bf16_f32 v89, v154, v155\n\t"
    "v_cvt_pk_bf16_f32 v90, v156, v157\n\t"
    "s_waitcnt vmcnt(9) lgkmcnt(2)\n\t"
    "v_mfma_f32_32x32x16_bf16 v[112:127], v[104:107], a[32:35], v[112:127]\n\t"
    "s_add_u32 m0, s69, 3072\n\t"
    "ds_read_b64_tr_b16 v[100:101], v5 offset:6144\n\t"
    "buffer_load_dwordx4 v2, s[48:51], s68 offen offset:0 lds\n\t"
    "ds_read_b64_tr_b16 v[102:103], v5 offset:6400\n\t"
    "v_fma_f32 v81, v69, v80, -v79\n\t"
    "v_cvt_pk_bf16_f32 v91, v158, v159\n\t"
    "v_mfma_f32_32x32x16_bf16 v[128:143], v[104:107], a[36:39], v[128:143]\n\t"
    "v_cvt_pk_bf16_f32 v93, v78, v79\n\t"
    "v_fma_f32 v82, v69, v81, -v80\n\t"
    "buffer_store_dwordx4 v[88:91], v6, s[40:43], s93 offen offset:1024 sc1\n\t"
    "v_cvt_pk_bf16_f32 v84, v160, v161\n\t"
    "s_waitcnt vmcnt(10) lgkmcnt(2)\n\t"
    "v_mfma_f32_32x32x16_bf16 v[112:127], v[108:111], a[40:43], v[112:127]\n\t"
    "ds_read_b64_tr_b16 v[104:105], v5 offset:7168\n\t"
    "buffer_load_dwordx4 v3, s[48:51], s68 offen offset:1024 lds\n\t"
    "ds_read_b64_tr_b16 v[106:107], v5 offset:7424\n\t"
    "v_fma_f32 v83, v69, v82, -v81\n\t"
    "v_cvt_pk_bf16_f32 v85, v162, v163\n\t"
    "v_cvt_pk_bf16_f32 v86, v164, v165\n\t"
    "v_mfma_f32_32x32x16_bf16 v[128:143], v[108:111], a[44:47], v[128:143]\n\t"
    "v_cvt_pk_bf16_f32 v94, v80, v81\n\t"
    "v_cvt_pk_bf16_f32 v95, v82, v83\n\t"
    "v_cvt_pk_bf16_f32 v87, v166, v167\n\t"
    "buffer_store_dwordx4 v[84:87], v6, s[40:43], s93 offen offset:2048 sc1\n\t"
    "v_mfma_f32_32x32x16_bf16 v[112:127], v[92:95], v[176:179], v[112:127]\n\t"
    "v_cvt_pk_bf16_f32 v88, v168, v169\n\t"
    "v_cvt_pk_bf16_f32 v89, v170, v171\n\t"
    "v_mfma_f32_32x32x16_bf16 v[128:143], v[92:95], v[180:183], v[128:143]\n\t"
    "v_cvt_pk_bf16_f32 v90, v172, v173\n\t"
    "v_cvt_pk_bf16_f32 v91, v174, v175\n\t"
    "s_waitcnt vmcnt(11) lgkmcnt(2)\n\t"
    "v_mfma_f32_32x32x16_bf16 v[112:127], v[100:103], a[48:51], v[112:127]\n\t"
    "ds_read_b64_tr_b16 v[108:109], v5 offset:8192\n\t"
    "buffer_load_dwordx4 v4, s[48:51], s68 offen offset:2048 lds\n\t"
    "s_add_u32 s68, s68, 55296\n\t"
    "ds_read_b64_tr_b16 v[110:111], v5 offset:8448\n\t"
    "v_mul_f32 v70, v71, v74\n\t"
    "buffer_store_dwordx4 v[88:91], v6, s[40:43], s93 offen offset:3072 sc1\n\t"
    "v_mfma_f32_32x32x16_bf16 v[128:143], v[100:103], a[52:55], v[128:143]\n\t"
    "v_fract_f32 v70, v70\n\t"
    "s_waitcnt vmcnt(12) lgkmcnt(2)\n\t"
    "v_mfma_f32_32x32x16_bf16 v[112:127], v[104:107], a[56:59], v[112:127]\n\t"
    "s_add_u32 m0, s69, 6144\n\t"
    "ds_read_b64_tr_b16 v[100:101], v5 offset:9216\n\t"
    "buffer_load_dwordx4 v2, s[48:51], s68 offen offset:0 lds\n\t"
    "ds_read_b64_tr_b16 v[102:103], v5 offset:9472\n\t"
    "v_cos_f32 v76, v70\n\t"
    "v_mfma_f32_32x32x16_bf16 v[128:143], v[104:107], a[60:63], v[128:143]\n\t"
    "v_add_f32 v70, v70, v72\n\t"
    "s_waitcnt vmcnt(12) lgkmcnt(2)\n\t"
    "v_mfma_f32_32x32x16_bf16 v[112:127], v[108:111], a[64:67], v[112:127]\n\t"
    "ds_read_b64_tr_b16 v[104:105], v5 offset:10240\n\t"
    "buffer_load_dwordx4 v3, s[48:51], s68 offen offset:1024 lds\n\t"
    "ds_read_b64_tr_b16 v[106:107], v5 offset:10496\n\t"
    "v_cos_f32 v77, v70\n\t"
    "v_mfma_f32_32x32x16_bf16 v[128:143], v[108:111], a[68:71], v[128:143]\n\t"
    "s_nop 0\n\t"
    "v_fma_f32 v78, v69, v77, -v76\n\t"
    "s_waitcnt vmcnt(12) lgkmcnt(2)\n\t"
    "v_mfma_f32_32x32x16_bf16 v[112:127], v[100:103], a[72:75], v[112:127]\n\t"
    "ds_read_b64_tr_b16 v[108:109], v5 offset:11264\n\t"
    "buffer_load_dwordx4 v4, s[48:51], s68 offen offset:2048 lds\n\t"
    "s_add_u32 s68, s68, 55296\n\t"
    "ds_read_b64_tr_b16 v[110:111], v5 offset:11520\n\t"
    "v_fma_f32 v79, v69, v78, -v77\n\t"
    "v_mfma_f32_32x32x16_bf16 v[128:143], v[100:103], a[76:79], v[128:143]\n\t"
    "v_cvt_pk_bf16_f32 v96, v76, v77\n\t"
    "v_fma_f32 v80, v69, v79, -v78\n\t"
    "s_waitcnt vmcnt(12) lgkmcnt(2)\n\t"
    "v_mfma_f32_32x32x16_bf16 v[112:127], v[104:107], a[80:83], v[112:127]\n\t"
    "s_add_u32 m0, s69, 9216\n\t"
    "ds_read_b64_tr_b16 v[100:101], v5 offset:0\n\t"
    "buffer_load_dwordx4 v2, s[48:51], s68 offen offset:0 lds\n\t"
    "ds_read_b64_tr_b16 v[102:103], v5 offset:256\n\t"
    "v_fma_f32 v81, v69, v80, -v79\n\t"
    "v_mfma_f32_32x32x16_bf16 v[128:143], v[104:107], a[84:87], v[128:143]\n\t"
    "v_cvt_pk_bf16_f32 v97, v78, v79\n\t"
    "v_fma_f32 v82, v69, v81, -v80\n\t"
    "s_waitcnt vmcnt(12) lgkmcnt(2)\n\t"
    "v_mfma_f32_32x32x16_bf16 v[112:127], v[108:111], a[88:91], v[112:127]\n\t"
    "ds_read_b64_tr_b16 v[104:105], v5 offset:1024\n\t"
    "buffer_load_dwordx4 v3, s[48:51], s68 offen offset:1024 lds\n\t"
    "ds_read_b64_tr_b16 v[106:107], v5 offset:1280\n\t"
    "v_fma_f32 v83, v69, v82, -v81\n\t"
    "v_mfma_f32_32x32x16_bf16 v[128:143], v[108:111], a[92:95], v[128:143]\n\t"
    "v_cvt_pk_bf16_f32 v98, v80, v81\n\t"
    "v_cvt_pk_bf16_f32 v99, v82, v83\n\t"
    "s_nop 1\n\t"
    "v_mfma_f32_32x32x16_bf16 v[112:127], v[96:99], v[184:187], v[112:127]\n\t"
    "v_mfma_f32_32x32x16_bf16 v[128:143], v[96:99], v[188:191], v[128:143]\n\t"
    "s_waitcnt vmcnt(12) lgkmcnt(2)\n\t"
    "v_mfma_f32_32x32x16_bf16 v[112:127], v[100:103], a[96:99], v[112:127]\n\t"
    "ds_read_b64_tr_b16 v[108:109], v5 offset:2048\n\t"
    "buffer_load_dwordx4 v4, s[48:51], s68 offen offset:2048 lds\n\t"
    "s_add_u32 s68, s68, 55296\n\t"
    "ds_read_b64_tr_b16 v[110:111], v5 offset:2304\n\t"
    "v_mul_f32 v70, v71, v75\n\t"
    "v_mfma_f32_32x32x16_bf16 v[128:143], v[100:103], a[100:103], v[128:143]\n\t"
    "v_fract_f32 v70, v70\n\t"
    "s_waitcnt vmcnt(11) lgkmcnt(2)\n\t"
    "v_mfma_f32_32x32x16_bf16 v[112:127], v[104:107], a[104:107], v[112:127]\n\t"
    "s_add_u32 m0, s69, 0\n\t"
    "ds_read_b64_tr_b16 v[100:101], v5 offset:3072\n\t"
    "buffer_load_dwordx4 v2, s[48:51], s68 offen offset:0 lds\n\t"
    "ds_read_b64_tr_b16 v[102:103], v5 offset:3328\n\t"
    "v_cos_f32 v76, v70\n\t"
    "v_mfma_f32_32x32x16_bf16 v[128:143], v[104:107], a[108:111], v[128:143]\n\t"
    "v_add_f32 v70, v70, v72\n\t"
    "s_waitcnt vmcnt(10) lgkmcnt(2)\n\t"
    "v_mfma_f32_32x32x16_bf16 v[112:127], v[108:111], a[112:115], v[112:127]\n\t"
    "ds_read_b64_tr_b16 v[104:105], v5 offset:4096\n\t"
    "buffer_load_dwordx4 v3, s[48:51], s68 offen offset:1024 lds\n\t"
    "ds_read_b64_tr_b16 v[106:107], v5 offset:4352\n\t"
    "v_cos_f32 v77, v70\n\t"
    "v_mfma_f32_32x32x16_bf16 v[128:143], v[108:111], a[116:119], v[128:143]\n\t"
    "s_nop 0\n\t"
    "v_fma_f32 v78, v69, v77, -v76\n\t"
    "s_waitcnt vmcnt(9) lgkmcnt(2)\n\t"
    "v_mfma_f32_32x32x16_bf16 v[112:127], v[100:103], a[120:123], v[112:127]\n\t"
    "ds_read_b64_tr_b16 v[108:109], v5 offset:5120\n\t"
    "buffer_load_dwordx4 v4, s[48:51], s68 offen offset:2048 lds\n\t"
    "s_add_u32 s68, s68, 55296\n\t"
    "ds_read_b64_tr_b16 v[110:111], v5 offset:5376\n\t"
    "v_fma_f32 v79, v69, v78, -v77\n\t"
    "v_mfma_f32_32x32x16_bf16 v[128:143], v[100:103], a[124:127], v[128:143]\n\t"
    "v_cvt_pk_bf16_f32 v92, v76, v77\n\t"
    "v_fma_f32 v80, v69, v79, -v78\n\t"
    "s_waitcnt vmcnt(8) lgkmcnt(2)\n\t"
    "s_bfe_u32 s70, s89, 327685\n\t"
    "s_lshl_b32 s70, s70, 6\n\t"
    "s_add_u32 s70, s70, s92\n\t"
    "buffer_store_dword v19, v7, s[40:43], s70 offen sc1\n\t"
    "v_mfma_f32_32x32x16_bf16 v[112:127], v[104:107], a[128:131], v[112:127]\n\t"
    "s_add_u32 m0, s69, 3072\n\t"
    "ds_read_b64_tr_b16 v[100:101], v5 offset:6144\n\t"
    "buffer_load_dwordx4 v2, s[48:51], s68 offen offset:0 lds\n\t"
    "ds_read_b64_tr_b16 v[102:103], v5 offset:6400\n\t"
    "v_fma_f32 v81, v69, v80, -v79\n\t"
    "v_mfma_f32_32x32x16_bf16 v[128:143], v[104:107], a[132:135], v[128:143]\n\t"
    "v_cvt_pk_bf16_f32 v93, v78, v79\n\t"
    "v_fma_f32 v82, v69, v81, -v80\n\t"
    "s_waitcnt vmcnt(9) lgkmcnt(2)\n\t"
    "v_mfma_f32_32x32x16_bf16 v[112:127], v[108:111], a[136:139], v[112:127]\n\t"
    "ds_read_b64_tr_b16 v[104:105], v5 offset:7168\n\t"
    "buffer_load_dwordx4 v3, s[48:51], s68 offen offset:1024 lds\n\t"
    "ds_read_b64_tr_b16 v[106:107], v5 offset:7424\n\t"
    "v_fma_f32 v83, v69, v82, -v81\n\t"
    "v_mfma_f32_32x32x16_bf16 v[128:143], v[108:111], a[140:143], v[128:143]\n\t"
    "v_cvt_pk_bf16_f32 v94, v80, v81\n\t"
    "v_cvt_pk_bf16_f32 v95, v82, v83\n\t"
    "s_nop 1\n\t"
    "v_mfma_f32_32x32x16_bf16 v[112:127], v[92:95], v[192:195], v[112:127]\n\t"
    "v_mfma_f32_32x32x16_bf16 v[128:143], v[92:95], v[196:199], v[128:143]\n\t"
    "s_waitcnt vmcnt(9) lgkmcnt(2)\n\t"
    "v_mfma_f32_32x32x16_bf16 v[112:127], v[100:103], a[144:147], v[112:127]\n\t"
    "ds_read_b64_tr_b16 v[108:109], v5 offset:8192\n\t"
    "buffer_load_dwordx4 v4, s[48:51], s68 offen offset:2048 lds\n\t"
    "s_add_u32 s68, s68, 55296\n\t"
    "ds_read_b64_tr_b16 v[110:111], v5 offset:8448\n\t"
    "v_fmaak_f32 v70, v71, v73, 0x3e800000\n\t"
    "v_mfma_f32_32x32x16_bf16 v[128:143], v[100:103], a[148:151], v[128:143]\n\t"
    "v_fract_f32 v70, v70\n\t"
    "s_waitcnt vmcnt(9) lgkmcnt(2)\n\t"
    "v_mfma_f32_32x32x16_bf16 v[112:127], v[104:107], a[152:155], v[112:127]\n\t"
    "s_add_u32 m0, s69, 6144\n\t"
    "ds_read_b64_tr_b16 v[100:101], v5 offset:9216\n\t"
    "buffer_load_dwordx4 v2, s[48:51], s68 offen offset:0 lds\n\t"
    "ds_read_b64_tr_b16 v[102:103], v5 offset:9472\n\t"
    "v_cos_f32 v76, v70\n\t"
    "v_mfma_f32_32x32x16_bf16 v[128:143], v[104:107], a[156:159], v[128:143]\n\t"
    "v_add_f32 v70, v70, v72\n\t"
    "s_waitcnt vmcnt(9) lgkmcnt(2)\n\t"
    "v_mfma_f32_32x32x16_bf16 v[112:127], v[108:111], a[160:163], v[112:127]\n\t"
    "ds_read_b64_tr_b16 v[104:105], v5 offset:10240\n\t"
    "buffer_load_dwordx4 v3, s[48:51], s68 offen offset:1024 lds\n\t"
    "ds_read_b64_tr_b16 v[106:107], v5 offset:10496\n\t"
    "v_cos_f32 v77, v70\n\t"
    "v_mfma_f32_32x32x16_bf16 v[128:143], v[108:111], a[164:167], v[128:143]\n\t"
    "s_nop 0\n\t"
    "v_fma_f32 v78, v69, v77, -v76\n\t"
    "s_waitcnt vmcnt(9) lgkmcnt(2)\n\t"
    "v_mfma_f32_32x32x16_bf16 v[112:127], v[100:103], a[168:171], v[112:127]\n\t"
    "ds_read_b64_tr_b16 v[108:109], v5 offset:11264\n\t"
    "buffer_load_dwordx4 v4, s[48:51], s68 offen offset:2048 lds\n\t"
    "s_add_u32 s68, s68, 55296\n\t"
    "ds_read_b64_tr_b16 v[110:111], v5 offset:11520\n\t"
    "v_fma_f32 v79, v69, v78, -v77\n\t"
    "v_mfma_f32_32x32x16_bf16 v[128:143], v[100:103], a[172:175], v[128:143]\n\t"
    "v_cvt_pk_bf16_f32 v96, v76, v77\n\t"
    "v_fma_f32 v80, v69, v79, -v78\n\t"
    "s_waitcnt vmcnt(9) lgkmcnt(2)\n\t"
    "v_mfma_f32_32x32x16_bf16 v[112:127], v[104:107], a[176:179], v[112:127]\n\t"
    "s_add_u32 m0, s69, 9216\n\t"
    "ds_read_b64_tr_b16 v[100:101], v5 offset:0\n\t"
    "buffer_load_dwordx4 v2, s[48:51], s68 offen offset:0 lds\n\t"
    "ds_read_b64_tr_b16 v[102:103], v5 offset:256\n\t"
    "v_fma_f32 v81, v69, v80, -v79\n\t"
    "v_mfma_f32_32x32x16_bf16 v[128:143], v[104:107], a[180:183], v[128:143]\n\t"
    "v_cvt_pk_bf16_f32 v97, v78, v79\n\t"
    "v_fma_f32 v82, v69, v81, -v80\n\t"
    "s_waitcnt vmcnt(9) lgkmcnt(2)\n\t"
    "v_mfma_f32_32x32x16_bf16 v[112:127], v[108:111], a[184:187], v[112:127]\n\t"
    "ds_read_b64_tr_b16 v[104:105], v5 offset:1024\n\t"
    "buffer_load_dwordx4 v3, s[48:51], s68 offen offset:1024 lds\n\t"
    "ds_read_b64_tr_b16 v[106:107], v5 offset:1280\n\t"
    "v_fma_f32 v83, v69, v82, -v81\n\t"
    "v_mfma_f32_32x32x16_bf16 v[128:143], v[108:111], a[188:191], v[128:143]\n\t"
    "v_cvt_pk_bf16_f32 v98, v80, v81\n\t"
    "v_cvt_pk_bf16_f32 v99, v82, v83\n\t"
    "s_nop 1\n\t"
    "v_mfma_f32_32x32x16_bf16 v[112:127], v[96:99], v[200:203], v[112:127]\n\t"
    "v_mfma_f32_32x32x16_bf16 v[128:143], v[96:99], v[204:207], v[128:143]\n\t"
    "s_waitcnt vmcnt(9) lgkmcnt(2)\n\t"
    "v_mfma_f32_32x32x16_bf16 v[112:127], v[100:103], a[192:195], v[112:127]\n\t"
    "ds_read_b64_tr_b16 v[108:109], v5 offset:2048\n\t"
    "buffer_load_dwordx4 v4, s[48:51], s68 offen offset:2048 lds\n\t"
    "s_bfe_u32 s70, s89, 327695\n\t"
    "s_lshl_b32 s70, s70, 6\n\t"
    "s_add_u32 s68, s90, s70\n\t"
    "ds_read_b64_tr_b16 v[110:111], v5 offset:2304\n\t"
    "v_fmaak_f32 v70, v71, v74, 0x3e800000\n\t"
    "v_mfma_f32_32x32x16_bf16 v[128:143], v[100:103], a[196:199], v[128:143]\n\t"
    "v_fract_f32 v70, v70\n\t"
    "s_waitcnt vmcnt(8) lgkmcnt(2)\n\t"
    "v_mfma_f32_32x32x16_bf16 v[112:127], v[104:107], a[200:203], v[112:127]\n\t"
    "s_add_u32 m0, s69, 0\n\t"
    "ds_read_b64_tr_b16 v[100:101], v5 offset:3072\n\t"
    "buffer_load_dwordx4 v2, s[48:51], s68 offen offset:0 lds\n\t"
    "ds_read_b64_tr_b16 v[102:103], v5 offset:3328\n\t"
    "v_cos_f32 v76, v70\n\t"
    "v_mfma_f32_32x32x16_bf16 v[128:143], v[104:107], a[204:207], v[128:143]\n\t"
    "v_add_f32 v70, v70, v72\n\t"
    "s_waitcnt vmcnt(8) lgkmcnt(2)\n\t"
    "v_mfma_f32_32x32x16_bf16 v[112:127], v[108:111], a[208:211], v[112:127]\n\t"
    "ds_read_b64_tr_b16 v[104:105], v5 offset:4096\n\t"
    "buffer_load_dwordx4 v3, s[48:51], s68 offen offset:1024 lds\n\t"
    "ds_read_b64_tr_b16 v[106:107], v5 offset:4352\n\t"
    "v_cos_f32 v77, v70\n\t"
    "v_mfma_f32_32x32x16_bf16 v[128:143], v[108:111], a[212:215], v[128:143]\n\t"
    "s_nop 0\n\t"
    "v_fma_f32 v78, v69, v77, -v76\n\t"
    "s_waitcnt vmcnt(8) lgkmcnt(2)\n\t"
    "v_mfma_f32_32x32x16_bf16 v[112:127], v[100:103], a[216:219], v[112:127]\n\t"
    "ds_read_b64_tr_b16 v[108:109], v5 offset:5120\n\t"
    "buffer_load_dwordx4 v4, s[48:51], s68 offen offset:2048 lds\n\t"
    "s_add_u32 s68, s68, 55296\n\t"
    "ds_read_b64_tr_b16 v[110:111], v5 offset:5376\n\t"
    "v_fma_f32 v79, v69, v78, -v77\n\t"
    "v_mfma_f32_32x32x16_bf16 v[128:143], v[100:103], a[220:223], v[128:143]\n\t"
    "v_cvt_pk_bf16_f32 v92, v76, v77\n\t"
    "v_fma_f32 v80, v69, v79, -v78\n\t"
    "s_waitcnt vmcnt(8) lgkmcnt(2)\n\t"
    "v_mfma_f32_32x32x16_bf16 v[112:127], v[104:107], a[224:227], v[112:127]\n\t"
    "s_add_u32 m0, s69, 3072\n\t"
    "ds_read_b64_tr_b16 v[100:101], v5 offset:6144\n\t"
    "buffer_load_dwordx4 v2, s[48:51], s68 offen offset:0 lds\n\t"
    "ds_read_b64_tr_b16 v[102:103], v5 offset:6400\n\t"
    "v_fma_f32 v81, v69, v80, -v79\n\t"
    "v_mfma_f32_32x32x16_bf16 v[128:143], v[104:107], a[228:231], v[128:143]\n\t"
    "v_cvt_pk_bf16_f32 v93, v78, v79\n\t"
    "v_fma_f32 v82, v69, v81, -v80\n\t"
    "s_waitcnt vmcnt(8) lgkmcnt(2)\n\t"
    "v_mfma_f32_32x32x16_bf16 v[112:127], v[108:111], a[232:235], v[112:127]\n\t"
    "ds_read_b64_tr_b16 v[104:105], v5 offset:7168\n\t"
    "buffer_load_dwordx4 v3, s[48:51], s68 offen offset:1024 lds\n\t"
    "ds_read_b64_tr_b16 v[106:107], v5 offset:7424\n\t"
    "v_fma_f32 v83, v69, v82, -v81\n\t"
    "v_mfma_f32_32x32x16_bf16 v[128:143], v[108:111], a[236:239], v[128:143]\n\t"
    "v_cvt_pk_bf16_f32 v94, v80, v81\n\t"
    "v_cvt_pk_bf16_f32 v95, v82, v83\n\t"
    "s_nop 1\n\t"
    "v_mfma_f32_32x32x16_bf16 v[112:127], v[92:95], v[208:211], v[112:127]\n\t"
    "v_mfma_f32_32x32x16_bf16 v[128:143], v[92:95], v[212:215], v[128:143]\n\t"
    "s_waitcnt vmcnt(8) lgkmcnt(2)\n\t"
    "v_mfma_f32_32x32x16_bf16 v[112:127], v[100:103], a[240:243], v[112:127]\n\t"
    "ds_read_b64_tr_b16 v[108:109], v5 offset:8192\n\t"
    "buffer_load_dwordx4 v4, s[48:51], s68 offen offset:2048 lds\n\t"
    "s_add_u32 s68, s68, 55296\n\t"
    "ds_read_b64_tr_b16 v[110:111], v5 offset:8448\n\t"
    "v_fmaak_f32 v70, v71, v75, 0x3e800000\n\t"
    "v_mfma_f32_32x32x16_bf16 v[128:143], v[100:103], a[244:247], v[128:143]\n\t"
    "v_fract_f32 v70, v70\n\t"
    "s_waitcnt vmcnt(8) lgkmcnt(2)\n\t"
    "v_mfma_f32_32x32x16_bf16 v[112:127], v[104:107], a[248:251], v[112:127]\n\t"
    "s_add_u32 m0, s69, 6144\n\t"
    "ds_read_b64_tr_b16 v[100:101], v5 offset:9216\n\t"
    "buffer_load_dwordx4 v2, s[48:51], s68 offen offset:0 lds\n\t"
    "ds_read_b64_tr_b16 v[102:103], v5 offset:9472\n\t"
    "v_cos_f32 v76, v70\n\t"
    "v_mfma_f32_32x32x16_bf16 v[128:143], v[104:107], a[252:255], v[128:143]\n\t"
    "v_add_f32 v70, v70, v72\n\t"
    "s_waitcnt vmcnt(8) lgkmcnt(2)\n\t"
    "v_mfma_f32_32x32x16_bf16 v[112:127], v[108:111], v[224:227], v[112:127]\n\t"
    "ds_read_b64_tr_b16 v[104:105], v5 offset:10240\n\t"
    "buffer_load_dwordx4 v3, s[48:51], s68 offen offset:1024 lds\n\t"
    "ds_read_b64_tr_b16 v[106:107], v5 offset:10496\n\t"
    "v_cos_f32 v77, v70\n\t"
    "v_mfma_f32_32x32x16_bf16 v[128:143], v[108:111], v[228:231], v[128:143]\n\t"
    "s_nop 0\n\t"
    "v_fma_f32 v78, v69, v77, -v76\n\t"
    "s_waitcnt vmcnt(8) lgkmcnt(2)\n\t"
    "v_mfma_f32_32x32x16_bf16 v[112:127], v[100:103], v[232:235], v[112:127]\n\t"
    "ds_read_b64_tr_b16 v[108:109], v5 offset:11264\n\t"
    "buffer_load_dwordx4 v4, s[48:51], s68 offen offset:2048 lds\n\t"
    "s_add_u32 s68, s68, 55296\n\t"
    "ds_read_b64_tr_b16 v[110:111], v5 offset:11520\n\t"
    "v_fma_f32 v79, v69, v78, -v77\n\t"
    "v_mfma_f32_32x32x16_bf16 v[128:143], v[100:103], v[236:239], v[128:143]\n\t"
    "v_cvt_pk_bf16_f32 v96, v76, v77\n\t"
    "v_fma_f32 v80, v69, v79, -v78\n\t"
    "s_waitcnt vmcnt(8) lgkmcnt(2)\n\t"
    "v_mfma_f32_32x32x16_bf16 v[112:127], v[104:107], v[240:243], v[112:127]\n\t"
    "s_add_u32 m0, s69, 9216\n\t"
    "ds_read_b64_tr_b16 v[100:101], v5 offset:0\n\t"
    "buffer_load_dwordx4 v2, s[48:51], s68 offen offset:0 lds\n\t"
    "ds_read_b64_tr_b16 v[102:103], v5 offset:256\n\t"
    "v_fma_f32 v81, v69, v80, -v79\n\t"
    "v_mfma_f32_32x32x16_bf16 v[128:143], v[104:107], v[244:247], v[128:143]\n\t"
    "v_cvt_pk_bf16_f32 v97, v78, v79\n\t"
    "v_fma_f32 v82, v69, v81, -v80\n\t"
    "s_waitcnt vmcnt(8) lgkmcnt(2)\n\t"
    "v_mfma_f32_32x32x16_bf16 v[112:127], v[108:111], v[248:251], v[112:127]\n\t"
    "ds_read_b64_tr_b16 v[104:105], v5 offset:1024\n\t"
    "buffer_load_dwordx4 v3, s[48:51], s68 offen offset:1024 lds\n\t"
    "ds_read_b64_tr_b16 v[106:107], v5 offset:1280\n\t"
    "v_fma_f32 v83, v69, v82, -v81\n\t"
    "v_mfma_f32_32x32x16_bf16 v[128:143], v[108:111], v[252:255], v[128:143]\n\t"
    "v_cvt_pk_bf16_f32 v98, v80, v81\n\t"
    "v_cvt_pk_bf16_f32 v99, v82, v83\n\t"
    "s_nop 1\n\t"
    "v_mfma_f32_32x32x16_bf16 v[112:127], v[96:99], v[216:219], v[112:127]\n\t"
    "v_mfma_f32_32x32x16_bf16 v[128:143], v[96:99], v[220:223], v[128:143]\n\t"
    "s_bfe_u32 s70, s89, 327695\n\t"
    "s_lshl_b32 s70, s70, 5\n\t"
    "v_cvt_f32_u32 v71, s70\n\t"
    "v_add_f32 v71, v71, v18\n\t"
    "v_mul_f32 v72, 0x3ae38e39, v71\n\t"
    "v_cos_f32 v69, v72\n\t"
    "s_nop 0\n\t"
    "v_add_f32 v69, v69, v69\n\t"
    "s_waitcnt vmcnt(8) lgkmcnt(2)\n\t"
    "v_mfma_f32_32x32x16_bf16 v[144:159], v[100:103], a[0:3], 0\n\t"
    "ds_read_b64_tr_b16 v[108:109], v5 offset:2048\n\t"
    "buffer_load_dwordx4 v4, s[48:51], s68 offen offset:2048 lds\n\t"
    "s_add_u32 s68, s68, 55296\n\t"
    "ds_read_b64_tr_b16 v[110:111], v5 offset:2304\n\t"
    "v_mul_f32 v70, v71, v73\n\t"
    "v_mfma_f32_32x32x16_bf16 v[160:175], v[100:103], a[4:7], 0\n\t"
    "v_fract_f32 v70, v70\n\t"
    "s_waitcnt vmcnt(8) lgkmcnt(2)\n\t"
    "v_mfma_f32_32x32x16_bf16 v[144:159], v[104:107], a[8:11], v[144:159]\n\t"
    "s_add_u32 m0, s69, 0\n\t"
    "ds_read_b64_tr_b16 v[100:101], v5 offset:3072\n\t"
    "buffer_load_dwordx4 v2, s[48:51], s68 offen offset:0 lds\n\t"
    "ds_read_b64_tr_b16 v[102:103], v5 offset:3328\n\t"
    "v_cos_f32 v76, v70\n\t"
    "s_bfe_u32 s93, s89, 327690\n\t"
    "s_add_u32 s71, s93, s66\n\t"
    "v_mfma_f32_32x32x16_bf16 v[160:175], v[104:107], a[12:15], v[160:175]\n\t"
    "v_add_f32 v70, v70, v72\n\t"
    "s_add_u32 s71, s71, s65\n\t"
    "s_and_b32 s71, s71, 7\n\t"
    "s_lshl_b32 s71, s71, 12\n\t"
    "s_lshl_b32 s93, s93, 15\n\t"
    "s_waitcnt vmcnt(8) lgkmcnt(2)\n\t"
    "v_mfma_f32_32x32x16_bf16 v[144:159], v[108:111], a[16:19], v[144:159]\n\t"
    "ds_read_b64_tr_b16 v[104:105], v5 offset:4096\n\t"
    "buffer_load_dwordx4 v3, s[48:51], s68 offen offset:1024 lds\n\t"
    "ds_read_b64_tr_b16 v[106:107], v5 offset:4352\n\t"
    "v_cos_f32 v77, v70\n\t"
    "s_add_u32 s93, s93, s91\n\t"
    "s_add_u32 s93, s93, s71\n\t"
    "v_mfma_f32_32x32x16_bf16 v[160:175], v[108:111], a[20:23], v[160:175]\n\t"
    "s_nop 0\n\t"
    "v_fma_f32 v78, v69, v77, -v76\n\t"
    "v_cvt_pk_bf16_f32 v84, v112, v113\n\t"
    "v_cvt_pk_bf16_f32 v85, v114, v115\n\t"
    "v_cvt_pk_bf16_f32 v86, v116, v117\n\t"
    "v_cvt_pk_bf16_f32 v87, v118, v119\n\t"
    "s_waitcnt vmcnt(8) lgkmcnt(2)\n\t"
    "v_mfma_f32_32x32x16_bf16 v[144:159], v[100:103], a[24:27], v[144:159]\n\t"
    "ds_read_b64_tr_b16 v[108:109], v5 offset:5120\n\t"
    "buffer_load_dwordx4 v4, s[48:51], s68 offen offset:2048 lds\n\t"
    "s_add_u32 s68, s68, 55296\n\t"
    "ds_read_b64_tr_b16 v[110:111], v5 offset:5376\n\t"
    "v_fma_f32 v79, v69, v78, -v77\n\t"
    "v_mfma_f32_32x32x16_bf16 v[160:175], v[100:103], a[28:31], v[160:175]\n\t"
    "v_cvt_pk_bf16_f32 v92, v76, v77\n\t"
    "v_fma_f32 v80, v69, v79, -v78\n\t"
    "buffer_store_dwordx4 v[84:87], v6, s[40:43], s93 offen offset:0 sc1\n\t"
    "v_cvt_pk_bf16_f32 v88, v120, v121\n\t"
    "v_cvt_pk_bf16_f32 v89, v122, v123\n\t"
    "v_cvt_pk_bf16_f32 v90, v124, v125\n\t"
    "s_waitcnt vmcnt(9) lgkmcnt(2)\n\t"
    "v_mfma_f32_32x32x16_bf16 v[144:159], v[104:107], a[32:35], v[144:159]\n\t"
    "s_add_u32 m0, s69, 3072\n\t"
    "ds_read_b64_tr_b16 v[100:101], v5 offset:6144\n\t"
    "buffer_load_dwordx4 v2, s[48:51], s68 offen offset:0 lds\n\t"
    "ds_read_b64_tr_b16 v[102:103], v5 offset:6400\n\t"
    "v_fma_f32 v81, v69, v80, -v79\n\t"
    "v_cvt_pk_bf16_f32 v91, v126, v127\n\t"
    "v_mfma_f32_32x32x16_bf16 v[160:175], v[104:107], a[36:39], v[160:175]\n\t"
    "v_cvt_pk_bf16_f32 v93, v78, v79\n\t"
    "v_fma_f32 v82, v69, v81, -v80\n\t"
    "buffer_store_dwordx4 v[88:91], v6, s[40:43], s93 offen offset:1024 sc1\n\t"
    "v_cvt_pk_bf16_f32 v84, v128, v129\n\t"
    "s_waitcnt vmcnt(10) lgkmcnt(2)\n\t"
    "v_mfma_f32_32x32x16_bf16 v[144:159], v[108:111], a[40:43], v[144:159]\n\t"
    "ds_read_b64_tr_b16 v[104:105], v5 offset:7168\n\t"
    "buffer_load_dwordx4 v3, s[48:51], s68 offen offset:1024 lds\n\t"
    "ds_read_b64_tr_b16 v[106:107], v5 offset:7424\n\t"
    "v_fma_f32 v83, v69, v82, -v81\n\t"
    "v_cvt_pk_bf16_f32 v85, v130, v131\n\t"
    "v_cvt_pk_bf16_f32 v86, v132, v133\n\t"
    "v_mfma_f32_32x32x16_bf16 v[160:175], v[108:111], a[44:47], v[160:175]\n\t"
    "v_cvt_pk_bf16_f32 v94, v80, v81\n\t"
    "v_cvt_pk_bf16_f32 v95, v82, v83\n\t"
    "v_cvt_pk_bf16_f32 v87, v134, v135\n\t"
    "buffer_store_dwordx4 v[84:87], v6, s[40:43], s93 offen offset:2048 sc1\n\t"
    "v_mfma_f32_32x32x16_bf16 v[144:159], v[92:95], v[176:179], v[144:159]\n\t"
    "v_cvt_pk_bf16_f32 v88, v136, v137\n\t"
    "v_cvt_pk_bf16_f32 v89, v138, v139\n\t"
    "v_mfma_f32_32x32x16_bf16 v[160:175], v[92:95], v[180:183], v[160:175]\n\t"
    "v_cvt_pk_bf16_f32 v90, v140, v141\n\t"
    "v_cvt_pk_bf16_f32 v91, v142, v143\n\t"
    "s_waitcnt vmcnt(11) lgkmcnt(2)\n\t"
    "v_mfma_f32_32x32x16_bf16 v[144:159], v[100:103], a[48:51], v[144:159]\n\t"
    "ds_read_b64_tr_b16 v[108:109], v5 offset:8192\n\t"
    "buffer_load_dwordx4 v4, s[48:51], s68 offen offset:2048 lds\n\t"
    "s_add_u32 s68, s68, 55296\n\t"
    "ds_read_b64_tr_b16 v[110:111], v5 offset:8448\n\t"
    "v_mul_f32 v70, v71, v74\n\t"
    "buffer_store_dwordx4 v[88:91], v6, s[40:43], s93 offen offset:3072 sc1\n\t"
    "v_mfma_f32_32x32x16_bf16 v[160:175], v[100:103], a[52:55], v[160:175]\n\t"
    "v_fract_f32 v70, v70\n\t"
    "s_waitcnt vmcnt(12) lgkmcnt(2)\n\t"
    "v_mfma_f32_32x32x16_bf16 v[144:159], v[104:107], a[56:59], v[144:159]\n\t"
    "s_add_u32 m0, s69, 6144\n\t"
    "ds_read_b64_tr_b16 v[100:101], v5 offset:9216\n\t"
    "buffer_load_dwordx4 v2, s[48:51], s68 offen offset:0 lds\n\t"
    "ds_read_b64_tr_b16 v[102:103], v5 offset:9472\n\t"
    "v_cos_f32 v76, v70\n\t"
    "v_mfma_f32_32x32x16_bf16 v[160:175], v[104:107], a[60:63], v[160:175]\n\t"
    "v_add_f32 v70, v70, v72\n\t"
    "s_waitcnt vmcnt(12) lgkmcnt(2)\n\t"
    "v_mfma_f32_32x32x16_bf16 v[144:159], v[108:111], a[64:67], v[144:159]\n\t"
    "ds_read_b64_tr_b16 v[104:105], v5 offset:10240\n\t"
    "buffer_load_dwordx4 v3, s[48:51], s68 offen offset:1024 lds\n\t"
    "ds_read_b64_tr_b16 v[106:107], v5 offset:10496\n\t"
    "v_cos_f32 v77, v70\n\t"
    "v_mfma_f32_32x32x16_bf16 v[160:175], v[108:111], a[68:71], v[160:175]\n\t"
    "s_nop 0\n\t"
    "v_fma_f32 v78, v69, v77, -v76\n\t"
    "s_waitcnt vmcnt(12) lgkmcnt(2)\n\t"
    "v_mfma_f32_32x32x16_bf16 v[144:159], v[100:103], a[72:75], v[144:159]\n\t"
    "ds_read_b64_tr_b16 v[108:109], v5 offset:11264\n\t"
    "buffer_load_dwordx4 v4, s[48:51], s68 offen offset:2048 lds\n\t"
    "s_add_u32 s68, s68, 55296\n\t"
    "ds_read_b64_tr_b16 v[110:111], v5 offset:11520\n\t"
    "v_fma_f32 v79, v69, v78, -v77\n\t"
    "v_mfma_f32_32x32x16_bf16 v[160:175], v[100:103], a[76:79], v[160:175]\n\t"
    "v_cvt_pk_bf16_f32 v96, v76, v77\n\t"
    "v_fma_f32 v80, v69, v79, -v78\n\t"
    "s_waitcnt vmcnt(12) lgkmcnt(2)\n\t"
    "v_mfma_f32_32x32x16_bf16 v[144:159], v[104:107], a[80:83], v[144:159]\n\t"
    "s_add_u32 m0, s69, 9216\n\t"
    "ds_read_b64_tr_b16 v[100:101], v5 offset:0\n\t"
    "buffer_load_dwordx4 v2, s[48:51], s68 offen offset:0 lds\n\t"
    "ds_read_b64_tr_b16 v[102:103], v5 offset:256\n\t"
    "v_fma_f32 v81, v69, v80, -v79\n\t"
    "v_mfma_f32_32x32x16_bf16 v[160:175], v[104:107], a[84:87], v[160:175]\n\t"
    "v_cvt_pk_bf16_f32 v97, v78, v79\n\t"
    "v_fma_f32 v82, v69, v81, -v80\n\t"
    "s_waitcnt vmcnt(12) lgkmcnt(2)\n\t"
    "v_mfma_f32_32x32x16_bf16 v[144:159], v[108:111], a[88:91], v[144:159]\n\t"
    "ds_read_b64_tr_b16 v[104:105], v5 offset:1024\n\t"
    "buffer_load_dwordx4 v3, s[48:51], s68 offen offset:1024 lds\n\t"
    "ds_read_b64_tr_b16 v[106:107], v5 offset:1280\n\t"
    "v_fma_f32 v83, v69, v82, -v81\n\t"
    "v_mfma_f32_32x32x16_bf16 v[160:175], v[108:111], a[92:95], v[160:175]\n\t"
    "v_cvt_pk_bf16_f32 v98, v80, v81\n\t"
    "v_cvt_pk_bf16_f32 v99, v82, v83\n\t"
    "s_nop 1\n\t"
    "v_mfma_f32_32x32x16_bf16 v[144:159], v[96:99], v[184:187], v[144:159]\n\t"
    "v_mfma_f32_32x32x16_bf16 v[160:175], v[96:99], v[188:191], v[160:175]\n\t"
    "s_waitcnt vmcnt(12) lgkmcnt(2)\n\t"
    "v_mfma_f32_32x32x16_bf16 v[144:159], v[100:103], a[96:99], v[144:159]\n\t"
    "ds_read_b64_tr_b16 v[108:109], v5 offset:2048\n\t"
    "buffer_load_dwordx4 v4, s[48:51], s68 offen offset:2048 lds\n\t"
    "s_add_u32 s68, s68, 55296\n\t"
    "ds_read_b64_tr_b16 v[110:111], v5 offset:2304\n\t"
    "v_mul_f32 v70, v71, v75\n\t"
    "v_mfma_f32_32x32x16_bf16 v[160:175], v[100:103], a[100:103], v[160:175]\n\t"
    "v_fract_f32 v70, v70\n\t"
    "s_waitcnt vmcnt(11) lgkmcnt(2)\n\t"
    "v_mfma_f32_32x32x16_bf16 v[144:159], v[104:107], a[104:107], v[144:159]\n\t"
    "s_add_u32 m0, s69, 0\n\t"
    "ds_read_b64_tr_b16 v[100:101], v5 offset:3072\n\t"
    "buffer_load_dwordx4 v2, s[48:51], s68 offen offset:0 lds\n\t"
    "ds_read_b64_tr_b16 v[102:103], v5 offset:3328\n\t"
    "v_cos_f32 v76, v70\n\t"
    "v_mfma_f32_32x32x16_bf16 v[160:175], v[104:107], a[108:111], v[160:175]\n\t"
    "v_add_f32 v70, v70, v72\n\t"
    "s_waitcnt vmcnt(10) lgkmcnt(2)\n\t"
    "v_mfma_f32_32x32x16_bf16 v[144:159], v[108:111], a[112:115], v[144:159]\n\t"
    "ds_read_b64_tr_b16 v[104:105], v5 offset:4096\n\t"
    "buffer_load_dwordx4 v3, s[48:51], s68 offen offset:1024 lds\n\t"
    "ds_read_b64_tr_b16 v[106:107], v5 offset:4352\n\t"
    "v_cos_f32 v77, v70\n\t"
    "v_mfma_f32_32x32x16_bf16 v[160:175], v[108:111], a[116:119], v[160:175]\n\t"
    "s_nop 0\n\t"
    "v_fma_f32 v78, v69, v77, -v76\n\t"
    "s_waitcnt vmcnt(9) lgkmcnt(2)\n\t"
    "v_mfma_f32_32x32x16_bf16 v[144:159], v[100:103], a[120:123], v[144:159]\n\t"
    "ds_read_b64_tr_b16 v[108:109], v5 offset:5120\n\t"
    "buffer_load_dwordx4 v4, s[48:51], s68 offen offset:2048 lds\n\t"
    "s_add_u32 s68, s68, 55296\n\t"
    "ds_read_b64_tr_b16 v[110:111], v5 offset:5376\n\t"
    "v_fma_f32 v79, v69, v78, -v77\n\t"
    "v_mfma_f32_32x32x16_bf16 v[160:175], v[100:103], a[124:127], v[160:175]\n\t"
    "v_cvt_pk_bf16_f32 v92, v76, v77\n\t"
    "v_fma_f32 v80, v69, v79, -v78\n\t"
    "s_waitcnt vmcnt(8) lgkmcnt(2)\n\t"
    "s_bfe_u32 s70, s89, 327690\n\t"
    "s_lshl_b32 s70, s70, 6\n\t"
    "s_add_u32 s70, s70, s92\n\t"
    "buffer_store_dword v19, v7, s[40:43], s70 offen sc1\n\t"
    "v_mfma_f32_32x32x16_bf16 v[144:159], v[104:107], a[128:131], v[144:159]\n\t"
    "s_add_u32 m0, s69, 3072\n\t"
    "ds_read_b64_tr_b16 v[100:101], v5 offset:6144\n\t"
    "buffer_load_dwordx4 v2, s[48:51], s68 offen offset:0 lds\n\t"
    "ds_read_b64_tr_b16 v[102:103], v5 offset:6400\n\t"
    "v_fma_f32 v81, v69, v80, -v79\n\t"
    "v_mfma_f32_32x32x16_bf16 v[160:175], v[104:107], a[132:135], v[160:175]\n\t"
    "v_cvt_pk_bf16_f32 v93, v78, v79\n\t"
    "v_fma_f32 v82, v69, v81, -v80\n\t"
    "s_waitcnt vmcnt(9) lgkmcnt(2)\n\t"
    "v_mfma_f32_32x32x16_bf16 v[144:159], v[108:111], a[136:139], v[144:159]\n\t"
    "ds_read_b64_tr_b16 v[104:105], v5 offset:7168\n\t"
    "buffer_load_dwordx4 v3, s[48:51], s68 offen offset:1024 lds\n\t"
    "ds_read_b64_tr_b16 v[106:107], v5 offset:7424\n\t"
    "v_fma_f32 v83, v69, v82, -v81\n\t"
    "v_mfma_f32_32x32x16_bf16 v[160:175], v[108:111], a[140:143], v[160:175]\n\t"
    "v_cvt_pk_bf16_f32 v94, v80, v81\n\t"
    "v_cvt_pk_bf16_f32 v95, v82, v83\n\t"
    "s_nop 1\n\t"
    "v_mfma_f32_32x32x16_bf16 v[144:159], v[92:95], v[192:195], v[144:159]\n\t"
    "v_mfma_f32_32x32x16_bf16 v[160:175], v[92:95], v[196:199], v[160:175]\n\t"
    "s_waitcnt vmcnt(9) lgkmcnt(2)\n\t"
    "v_mfma_f32_32x32x16_bf16 v[144:159], v[100:103], a[144:147], v[144:159]\n\t"
    "ds_read_b64_tr_b16 v[108:109], v5 offset:8192\n\t"
    "buffer_load_dwordx4 v4, s[48:51], s68 offen offset:2048 lds\n\t"
    "s_add_u32 s68, s68, 55296\n\t"
    "ds_read_b64_tr_b16 v[110:111], v5 offset:8448\n\t"
    "v_fmaak_f32 v70, v71, v73, 0x3e800000\n\t"
    "v_mfma_f32_32x32x16_bf16 v[160:175], v[100:103], a[148:151], v[160:175]\n\t"
    "v_fract_f32 v70, v70\n\t"
    "s_waitcnt vmcnt(9) lgkmcnt(2)\n\t"
    "v_mfma_f32_32x32x16_bf16 v[144:159], v[104:107], a[152:155], v[144:159]\n\t"
    "s_add_u32 m0, s69, 6144\n\t"
    "ds_read_b64_tr_b16 v[100:101], v5 offset:9216\n\t"
    "buffer_load_dwordx4 v2, s[48:51], s68 offen offset:0 lds\n\t"
    "ds_read_b64_tr_b16 v[102:103], v5 offset:9472\n\t"
    "v_cos_f32 v76, v70\n\t"
    "v_mfma_f32_32x32x16_bf16 v[160:175], v[104:107], a[156:159], v[160:175]\n\t"
    "v_add_f32 v70, v70, v72\n\t"
    "s_waitcnt vmcnt(9) lgkmcnt(2)\n\t"
    "v_mfma_f32_32x32x16_bf16 v[144:159], v[108:111], a[160:163], v[144:159]\n\t"
    "ds_read_b64_tr_b16 v[104:105], v5 offset:10240\n\t"
    "buffer_load_dwordx4 v3, s[48:51], s68 offen offset:1024 lds\n\t"
    "ds_read_b64_tr_b16 v[106:107], v5 offset:10496\n\t"
    "v_cos_f32 v77, v70\n\t"
    "v_mfma_f32_32x32x16_bf16 v[160:175], v[108:111], a[164:167], v[160:175]\n\t"
    "s_nop 0\n\t"
    "v_fma_f32 v78, v69, v77, -v76\n\t"
    "s_waitcnt vmcnt(9) lgkmcnt(2)\n\t"
    "v_mfma_f32_32x32x16_bf16 v[144:159], v[100:103], a[168:171], v[144:159]\n\t"
    "ds_read_b64_tr_b16 v[108:109], v5 offset:11264\n\t"
    "buffer_load_dwordx4 v4, s[48:51], s68 offen offset:2048 lds\n\t"
    "s_add_u32 s68, s68, 55296\n\t"
    "ds_read_b64_tr_b16 v[110:111], v5 offset:11520\n\t"
    "v_fma_f32 v79, v69, v78, -v77\n\t"
    "v_mfma_f32_32x32x16_bf16 v[160:175], v[100:103], a[172:175], v[160:175]\n\t"
    "v_cvt_pk_bf16_f32 v96, v76, v77\n\t"
    "v_fma_f32 v80, v69, v79, -v78\n\t"
    "s_waitcnt vmcnt(9) lgkmcnt(2)\n\t"
    "v_mfma_f32_32x32x16_bf16 v[144:159], v[104:107], a[176:179], v[144:159]\n\t"
    "s_add_u32 m0, s69, 9216\n\t"
    "ds_read_b64_tr_b16 v[100:101], v5 offset:0\n\t"
    "buffer_load_dwordx4 v2, s[48:51], s68 offen offset:0 lds\n\t"
    "ds_read_b64_tr_b16 v[102:103], v5 offset:256\n\t"
    "v_fma_f32 v81, v69, v80, -v79\n\t"
    "v_mfma_f32_32x32x16_bf16 v[160:175], v[104:107], a[180:183], v[160:175]\n\t"
    "v_cvt_pk_bf16_f32 v97, v78, v79\n\t"
    "v_fma_f32 v82, v69, v81, -v80\n\t"
    "s_waitcnt vmcnt(9) lgkmcnt(2)\n\t"
    "v_mfma_f32_32x32x16_bf16 v[144:159], v[108:111], a[184:187], v[144:159]\n\t"
    "ds_read_b64_tr_b16 v[104:105], v5 offset:1024\n\t"
    "buffer_load_dwordx4 v3, s[48:51], s68 offen offset:1024 lds\n\t"
    "ds_read_b64_tr_b16 v[106:107], v5 offset:1280\n\t"
    "v_fma_f32 v83, v69, v82, -v81\n\t"
    "v_mfma_f32_32x32x16_bf16 v[160:175], v[108:111], a[188:191], v[160:175]\n\t"
    "v_cvt_pk_bf16_f32 v98, v80, v81\n\t"
    "v_cvt_pk_bf16_f32 v99, v82, v83\n\t"
    "s_nop 1\n\t"
    "v_mfma_f32_32x32x16_bf16 v[144:159], v[96:99], v[200:203], v[144:159]\n\t"
    "v_mfma_f32_32x32x16_bf16 v[160:175], v[96:99], v[204:207], v[160:175]\n\t"
    "s_waitcnt vmcnt(9) lgkmcnt(2)\n\t"
    "v_mfma_f32_32x32x16_bf16 v[144:159], v[100:103], a[192:195], v[144:159]\n\t"
    "ds_read_b64_tr_b16 v[108:109], v5 offset:2048\n\t"
    "buffer_load_dwordx4 v4, s[48:51], s68 offen offset:2048 lds\n\t"
    "s_bfe_u32 s70, s89, 327700\n\t"
    "s_lshl_b32 s70, s70, 6\n\t"
    "s_add_u32 s68, s90, s70\n\t"
    "s_mul_i32 s70, s88, 331776\n\t"
    "s_add_u32 s68, s68, s70\n\t"
    "ds_read_b64_tr_b16 v[110:111], v5 offset:2304\n\t"
    "v_fmaak_f32 v70, v71, v74, 0x3e800000\n\t"
    "v_mfma_f32_32x32x16_bf16 v[160:175], v[100:103], a[196:199], v[160:175]\n\t"
    "v_fract_f32 v70, v70\n\t"
    "s_waitcnt vmcnt(8) lgkmcnt(2)\n\t"
    "v_mfma_f32_32x32x16_bf16 v[144:159], v[104:107], a[200:203], v[144:159]\n\t"
    "s_add_u32 m0, s69, 0\n\t"
    "ds_read_b64_tr_b16 v[100:101], v5 offset:3072\n\t"
    "buffer_load_dwordx4 v2, s[48:51], s68 offen offset:0 lds\n\t"
    "ds_read_b64_tr_b16 v[102:103], v5 offset:3328\n\t"
    "v_cos_f32 v76, v70\n\t"
    "v_mfma_f32_32x32x16_bf16 v[160:175], v[104:107], a[204:207], v[160:175]\n\t"
    "v_add_f32 v70, v70, v72\n\t"
    "s_waitcnt vmcnt(8) lgkmcnt(2)\n\t"
    "v_mfma_f32_32x32x16_bf16 v[144:159], v[108:111], a[208:211], v[144:159]\n\t"
    "ds_read_b64_tr_b16 v[104:105], v5 offset:4096\n\t"
    "buffer_load_dwordx4 v3, s[48:51], s68 offen offset:1024 lds\n\t"
    "ds_read_b64_tr_b16 v[106:107], v5 offset:4352\n\t"
    "v_cos_f32 v77, v70\n\t"
    "v_mfma_f32_32x32x16_bf16 v[160:175], v[108:111], a[212:215], v[160:175]\n\t"
    "s_nop 0\n\t"
    "v_fma_f32 v78, v69, v77, -v76\n\t"
    "s_waitcnt vmcnt(8) lgkmcnt(2)\n\t"
    "v_mfma_f32_32x32x16_bf16 v[144:159], v[100:103], a[216:219], v[144:159]\n\t"
    "ds_read_b64_tr_b16 v[108:109], v5 offset:5120\n\t"
    "buffer_load_dwordx4 v4, s[48:51], s68 offen offset:2048 lds\n\t"
    "s_add_u32 s68, s68, 55296\n\t"
    "ds_read_b64_tr_b16 v[110:111], v5 offset:5376\n\t"
    "v_fma_f32 v79, v69, v78, -v77\n\t"
    "v_mfma_f32_32x32x16_bf16 v[160:175], v[100:103], a[220:223], v[160:175]\n\t"
    "v_cvt_pk_bf16_f32 v92, v76, v77\n\t"
    "v_fma_f32 v80, v69, v79, -v78\n\t"
    "s_waitcnt vmcnt(8) lgkmcnt(2)\n\t"
    "v_mfma_f32_32x32x16_bf16 v[144:159], v[104:107], a[224:227], v[144:159]\n\t"
    "s_add_u32 m0, s69, 3072\n\t"
    "ds_read_b64_tr_b16 v[100:101], v5 offset:6144\n\t"
    "buffer_load_dwordx4 v2, s[48:51], s68 offen offset:0 lds\n\t"
    "ds_read_b64_tr_b16 v[102:103], v5 offset:6400\n\t"
    "v_fma_f32 v81, v69, v80, -v79\n\t"
    "v_mfma_f32_32x32x16_bf16 v[160:175], v[104:107], a[228:231], v[160:175]\n\t"
    "v_cvt_pk_bf16_f32 v93, v78, v79\n\t"
    "v_fma_f32 v82, v69, v81, -v80\n\t"
    "s_mov_b32 s73, 0\n\t"
    "buffer_load_dword v11, v12, s[40:43], s73 offen sc1\n\t"
    "s_waitcnt vmcnt(9) lgkmcnt(2)\n\t"
    "v_mfma_f32_32x32x16_bf16 v[144:159], v[108:111], a[232:235], v[144:159]\n\t"
    "ds_read_b64_tr_b16 v[104:105], v5 offset:7168\n\t"
    "buffer_load_dwordx4 v3, s[48:51], s68 offen offset:1024 lds\n\t"
    "ds_read_b64_tr_b16 v[106:107], v5 offset:7424\n\t"
    "v_fma_f32 v83, v69, v82, -v81\n\t"
    "v_mfma_f32_32x32x16_bf16 v[160:175], v[108:111], a[236:239], v[160:175]\n\t"
    "v_cvt_pk_bf16_f32 v94, v80, v81\n\t"
    "v_cvt_pk_bf16_f32 v95, v82, v83\n\t"
    "s_nop 1\n\t"
    "v_mfma_f32_32x32x16_bf16 v[144:159], v[92:95], v[208:211], v[144:159]\n\t"
    "v_mfma_f32_32x32x16_bf16 v[160:175], v[92:95], v[212:215], v[160:175]\n\t"
    "s_waitcnt vmcnt(9) lgkmcnt(2)\n\t"
    "v_mfma_f32_32x32x16_bf16 v[144:159], v[100:103], a[240:243], v[144:159]\n\t"
    "ds_read_b64_tr_b16 v[108:109], v5 offset:8192\n\t"
    "buffer_load_dwordx4 v4, s[48:51], s68 offen offset:2048 lds\n\t"
    "s_add_u32 s68, s68, 55296\n\t"
    "ds_read_b64_tr_b16 v[110:111], v5 offset:8448\n\t"
    "v_fmaak_f32 v70, v71, v75, 0x3e800000\n\t"
    "v_mfma_f32_32x32x16_bf16 v[160:175], v[100:103], a[244:247], v[160:175]\n\t"
    "v_fract_f32 v70, v70\n\t"
    "s_waitcnt vmcnt(9) lgkmcnt(2)\n\t"
    "v_mfma_f32_32x32x16_bf16 v[144:159], v[104:107], a[248:251], v[144:159]\n\t"
    "s_add_u32 m0, s69, 6144\n\t"
    "ds_read_b64_tr_b16 v[100:101], v5 offset:9216\n\t"
    "buffer_load_dwordx4 v2, s[48:51], s68 offen offset:0 lds\n\t"
    "ds_read_b64_tr_b16 v[102:103], v5 offset:9472\n\t"
    "v_cos_f32 v76, v70\n\t"
    "v_mfma_f32_32x32x16_bf16 v[160:175], v[104:107], a[252:255], v[160:175]\n\t"
    "v_add_f32 v70, v70, v72\n\t"
    "s_waitcnt vmcnt(9) lgkmcnt(2)\n\t"
    "v_mfma_f32_32x32x16_bf16 v[144:159], v[108:111], v[224:227], v[144:159]\n\t"
    "ds_read_b64_tr_b16 v[104:105], v5 offset:10240\n\t"
    "buffer_load_dwordx4 v3, s[48:51], s68 offen offset:1024 lds\n\t"
    "ds_read_b64_tr_b16 v[106:107], v5 offset:10496\n\t"
    "v_cos_f32 v77, v70\n\t"
    "v_mfma_f32_32x32x16_bf16 v[160:175], v[108:111], v[228:231], v[160:175]\n\t"
    "s_nop 0\n\t"
    "v_fma_f32 v78, v69, v77, -v76\n\t"
    "s_waitcnt vmcnt(9) lgkmcnt(2)\n\t"
    "v_mfma_f32_32x32x16_bf16 v[144:159], v[100:103], v[232:235], v[144:159]\n\t"
    "ds_read_b64_tr_b16 v[108:109], v5 offset:11264\n\t"
    "buffer_load_dwordx4 v4, s[48:51], s68 offen offset:2048 lds\n\t"
    "s_add_u32 s68, s68, 55296\n\t"
    "ds_read_b64_tr_b16 v[110:111], v5 offset:11520\n\t"
    "v_fma_f32 v79, v69, v78, -v77\n\t"
    "v_mfma_f32_32x32x16_bf16 v[160:175], v[100:103], v[236:239], v[160:175]\n\t"
    "v_cvt_pk_bf16_f32 v96, v76, v77\n\t"
    "v_fma_f32 v80, v69, v79, -v78\n\t"
    "s_waitcnt vmcnt(9) lgkmcnt(2)\n\t"
    "v_mfma_f32_32x32x16_bf16 v[144:159], v[104:107], v[240:243], v[144:159]\n\t"
    "s_add_u32 m0, s69, 9216\n\t"
    "ds_read_b64_tr_b16 v[100:101], v5 offset:0\n\t"
    "buffer_load_dwordx4 v2, s[48:51], s68 offen offset:0 lds\n\t"
    "ds_read_b64_tr_b16 v[102:103], v5 offset:256\n\t"
    "v_fma_f32 v81, v69, v80, -v79\n\t"
    "v_mfma_f32_32x32x16_bf16 v[160:175], v[104:107], v[244:247], v[160:175]\n\t"
    "v_cvt_pk_bf16_f32 v97, v78, v79\n\t"
    "v_fma_f32 v82, v69, v81, -v80\n\t"
    "s_waitcnt vmcnt(9) lgkmcnt(2)\n\t"
    "v_mfma_f32_32x32x16_bf16 v[144:159], v[108:111], v[248:251], v[144:159]\n\t"
    "ds_read_b64_tr_b16 v[104:105], v5 offset:1024\n\t"
    "buffer_load_dwordx4 v3, s[48:51], s68 offen offset:1024 lds\n\t"
    "ds_read_b64_tr_b16 v[106:107], v5 offset:1280\n\t"
    "v_fma_f32 v83, v69, v82, -v81\n\t"
    "v_mfma_f32_32x32x16_bf16 v[160:175], v[108:111], v[252:255], v[160:175]\n\t"
    "v_cvt_pk_bf16_f32 v98, v80, v81\n\t"
    "v_cvt_pk_bf16_f32 v99, v82, v83\n\t"
    "s_nop 1\n\t"
    "v_mfma_f32_32x32x16_bf16 v[144:159], v[96:99], v[216:219], v[144:159]\n\t"
    "v_mfma_f32_32x32x16_bf16 v[160:175], v[96:99], v[220:223], v[160:175]\n\t"
    "s_bfe_u32 s70, s89, 327700\n\t"
    "s_lshl_b32 s70, s70, 5\n\t"
    "v_cvt_f32_u32 v71, s70\n\t"
    "v_add_f32 v71, v71, v18\n\t"
    "v_mul_f32 v72, 0x3ae38e39, v71\n\t"
    "v_cos_f32 v69, v72\n\t"
    "s_nop 0\n\t"
    "v_add_f32 v69, v69, v69\n\t"
    "s_mov_b64 s[60:61], %6\n\t"
    "s_and_b32 s61, s61, 0xffff\n\t"
    "s_mov_b32 s62, 2359296\n\t"
    "s_mov_b32 s63, 0x20000\n\t"
    "s_cmp_lg_u32 s88, 0\n\t"
    "s_cbranch_scc1 L_p2k1_22%=\n\t"
    "s_waitcnt vmcnt(9) lgkmcnt(2)\n\t"
    "v_mfma_f32_32x32x16_bf16 v[112:127], v[100:103], a[0:3], 0\n\t"
    "ds_read_b64_tr_b16 v[108:109], v5 offset:2048\n\t"
    "buffer_load_dwordx4 v4, s[48:51], s68 offen offset:2048 lds\n\t"
    "s_add_u32 s68, s68, 55296\n\t"
    "ds_read_b64_tr_b16 v[110:111], v5 offset:2304\n\t"
    "v_mul_f32 v70, v71, v73\n\t"
    "v_mfma_f32_32x32x16_bf16 v[128:143], v[100:103], a[4:7], 0\n\t"
    "v_fract_f32 v70, v70\n\t"
    "s_waitcnt vmcnt(9) lgkmcnt(2)\n\t"
    "v_mfma_f32_32x32x16_bf16 v[112:127], v[104:107], a[8:11], v[112:127]\n\t"
    "s_add_u32 m0, s69, 0\n\t"
    "ds_read_b64_tr_b16 v[100:101], v5 offset:3072\n\t"
    "buffer_load_dwordx4 v2, s[48:51], s68 offen offset:0 lds\n\t"
    "ds_read_b64_tr_b16 v[102:103], v5 offset:3328\n\t"
    "v_cos_f32 v76, v70\n\t"
    "s_bfe_u32 s93, s89, 327695\n\t"
    "s_add_u32 s71, s93, s66\n\t"
    "s_add_u32 s71, s71, s65\n\t"
    "v_mfma_f32_32x32x16_bf16 v[128:143], v[104:107], a[12:15], v[128:143]\n\t"
    "v_add_f32 v70, v70, v72\n\t"
    "s_and_b32 s71, s71, 7\n\t"
    "s_lshl_b32 s71, s71, 12\n\t"
    "s_lshl_b32 s93, s93, 15\n\t"
    "s_add_u32 s93, s93, s91\n\t"
    "s_add_u32 s93, s93, s71\n\t"
    "v_cvt_pk_bf16_f32 v84, v144, v145\n\t"
    "v_cvt_pk_bf16_f32 v85, v146, v147\n\t"
    "s_waitcnt vmcnt(8) lgkmcnt(2)\n\t"
    "v_mfma_f32_32x32x16_bf16 v[112:127], v[108:111], a[16:19], v[112:127]\n\t"
    "ds_read_b64_tr_b16 v[104:105], v5 offset:4096\n\t"
    "buffer_load_dwordx4 v3, s[48:51], s68 offen offset:1024 lds\n\t"
    "ds_read_b64_tr_b16 v[106:107], v5 offset:4352\n\t"
    "v_cos_f32 v77, v70\n\t"
    "v_cvt_pk_bf16_f32 v86, v148, v149\n\t"
    "v_cvt_pk_bf16_f32 v87, v150, v151\n\t"
    "v_mfma_f32_32x32x16_bf16 v[128:143], v[108:111], a[20:23], v[128:143]\n\t"
    "s_nop 0\n\t"
    "v_fma_f32 v78, v69, v77, -v76\n\t"
    "buffer_store_dwordx4 v[84:87], v6, s[40:43], s93 offen offset:0 sc1\n\t"
    "v_cvt_pk_bf16_f32 v88, v152, v153\n\t"
    "v_cvt_pk_bf16_f32 v89, v154, v155\n\t"
    "v_cvt_pk_bf16_f32 v90, v156, v157\n\t"
    "s_waitcnt vmcnt(9) lgkmcnt(2)\n\t"
    "v_mfma_f32_32x32x16_bf16 v[112:127], v[100:103], a[24:27], v[112:127]\n\t"
    "ds_read_b64_tr_b16 v[108:109], v5 offset:5120\n\t"
    "buffer_load_dwordx4 v4, s[48:51], s68 offen offset:2048 lds\n\t"
    "s_add_u32 s68, s68, 55296\n\t"
    "ds_read_b64_tr_b16 v[110:111], v5 offset:5376\n\t"
    "v_fma_f32 v79, v69, v78, -v77\n\t"
    "v_cvt_pk_bf16_f32 v91, v158, v159\n\t"
    "v_mfma_f32_32x32x16_bf16 v[128:143], v[100:103], a[28:31], v[128:143]\n\t"
    "v_cvt_pk_bf16_f32 v92, v76, v77\n\t"
    "v_fma_f32 v80, v69, v79, -v78\n\t"
    "buffer_store_dwordx4 v[88:91], v6, s[40:43], s93 offen offset:1024 sc1\n\t"
    "v_cvt_pk_bf16_f32 v84, v160, v161\n\t"
    "v_cvt_pk_bf16_f32 v85, v162, v163\n\t"
    "v_cvt_pk_bf16_f32 v86, v164, v165\n\t"
    "v_cvt_pk_bf16_f32 v87, v166, v167\n\t"
    "s_waitcnt vmcnt(10) lgkmcnt(2)\n\t"
    "v_mfma_f32_32x32x16_bf16 v[112:127], v[104:107], a[32:35], v[112:127]\n\t"
    "s_add_u32 m0, s69, 3072\n\t"
    "ds_read_b64_tr_b16 v[100:101], v5 offset:6144\n\t"
    "buffer_load_dwordx4 v2, s[48:51], s68 offen offset:0 lds\n\t"
    "ds_read_b64_tr_b16 v[102:103], v5 offset:6400\n\t"
    "v_fma_f32 v81, v69, v80, -v79\n\t"
    "v_mfma_f32_32x32x16_bf16 v[128:143], v[104:107], a[36:39], v[128:143]\n\t"
    "v_cvt_pk_bf16_f32 v93, v78, v79\n\t"
    "v_fma_f32 v82, v69, v81, -v80\n\t"
    "buffer_store_dwordx4 v[84:87], v6, s[40:43], s93 offen offset:2048 sc1\n\t"
    "v_cvt_pk_bf16_f32 v88, v168, v169\n\t"
    "v_cvt_pk_bf16_f32 v89, v170, v171\n\t"
    "v_cvt_pk_bf16_f32 v90, v172, v173\n\t"
    "v_cvt_pk_bf16_f32 v91, v174, v175\n\t"
    "s_waitcnt vmcnt(11) lgkmcnt(2)\n\t"
    "v_mfma_f32_32x32x16_bf16 v[112:127], v[108:111], a[40:43], v[112:127]\n\t"
    "ds_read_b64_tr_b16 v[104:105], v5 offset:7168\n\t"
    "buffer_load_dwordx4 v3, s[48:51], s68 offen offset:1024 lds\n\t"
    "ds_read_b64_tr_b16 v[106:107], v5 offset:7424\n\t"
    "v_fma_f32 v83, v69, v82, -v81\n\t"
    "buffer_store_dwordx4 v[88:91], v6, s[40:43], s93 offen offset:3072 sc1\n\t"
    "v_mfma_f32_32x32x16_bf16 v[128:143], v[108:111], a[44:47], v[128:143]\n\t"
    "v_cvt_pk_bf16_f32 v94, v80, v81\n\t"
    "v_cvt_pk_bf16_f32 v95, v82, v83\n\t"
    "s_nop 1\n\t"
    "v_mfma_f32_32x32x16_bf16 v[112:127], v[92:95], v[176:179], v[112:127]\n\t"
    "v_mfma_f32_32x32x16_bf16 v[128:143], v[92:95], v[180:183], v[128:143]\n\t"
    "s_waitcnt vmcnt(12) lgkmcnt(2)\n\t"
    "v_mfma_f32_32x32x16_bf16 v[112:127], v[100:103], a[48:51], v[112:127]\n\t"
    "ds_read_b64_tr_b16 v[108:109], v5 offset:8192\n\t"
    "buffer_load_dwordx4 v4, s[48:51], s68 offen offset:2048 lds\n\t"
    "ds_read_b64_tr_b16 v[110:111], v5 offset:8448\n\t"
    "v_mul_f32 v70, v71, v74\n\t"
    "v_mfma_f32_32x32x16_bf16 v[128:143], v[100:103], a[52:55], v[128:143]\n\t"
    "v_fract_f32 v70, v70\n\t"
    "buffer_load_dwordx2 v[52:53], v64, s[36:39], 0 offen\n\t"
    "buffer_load_dwordx2 v[54:55], v64, s[36:39], 0 offen offset:16\n\t"
    "buffer_load_dwordx2 v[56:57], v65, s[36:39], 0 offen\n\t"
    "buffer_load_dwordx2 v[58:59], v65, s[36:39], 0 offen offset:16\n\t"
    "buffer_load_dwordx2 v[60:61], v66, s[36:39], 0 offen\n\t"
    "buffer_load_dwordx2 v[62:63], v66, s[36:39], 0 offen offset:16\n\t"
    "v_cmp_ne_u32 vcc, s67, v11\n\t"
    "s_mov_b32 s94, 0x40000\n\t"
    "s_cbranch_vccz L_ok_24%=\n\t"
    "L_spin_25%=:\n\t"
    "s_sleep 1\n\t"
    "buffer_load_dword v11, v12, s[40:43], s73 offen sc1\n\t"
    "s_waitcnt vmcnt(0)\n\t"
    "v_cmp_ne_u32 vcc, s67, v11\n\t"
    "s_sub_u32 s94, s94, 1\n\t"
    "s_nop 3\n\t"
    "s_cbranch_vccz L_ok_24%=\n\t"
    "s_cmp_lg_u32 s94, 0\n\t"
    "s_cbranch_scc1 L_spin_25%=\n\t"
    "L_ok_24%=:\n\t"
    "s_add_u32 s71, s79, 3\n\t"
    "s_and_b32 s71, s71, 7\n\t"
    "s_lshl_b32 s71, s71, 12\n\t"
    "s_add_u32 s71, s71, s76\n\t"
    "buffer_load_dwordx4 v[20:23], v6, s[40:43], s71 offen offset:0 sc1\n\t"
    "s_add_u32 s71, s79, 4\n\t"
    "s_and_b32 s71, s71, 7\n\t"
    "s_lshl_b32 s71, s71, 12\n\t"
    "s_add_u32 s71, s71, s76\n\t"
    "buffer_load_dwordx4 v[24:27], v6, s[40:43], s71 offen offset:0 sc1\n\t"
    "s_waitcnt vmcnt(20) lgkmcnt(2)\n\t"
    "v_mfma_f32_32x32x16_bf16 v[112:127], v[104:107], a[56:59], v[112:127]\n\t"
    "ds_read_b64_tr_b16 v[100:101], v5 offset:9216\n\t"
    "ds_read_b64_tr_b16 v[102:103], v5 offset:9472\n\t"
    "v_cos_f32 v76, v70\n\t"
    "v_mfma_f32_32x32x16_bf16 v[128:143], v[104:107], a[60:63], v[128:143]\n\t"
    "v_add_f32 v70, v70, v72\n\t"
    "s_add_u32 s71, s79, 5\n\t"
    "s_and_b32 s71, s71, 7\n\t"
    "s_lshl_b32 s71, s71, 12\n\t"
    "s_add_u32 s71, s71, s76\n\t"
    "buffer_load_dwordx4 v[28:31], v6, s[40:43], s71 offen offset:0 sc1\n\t"
    "s_add_u32 s71, s79, 6\n\t"
    "s_and_b32 s71, s71, 7\n\t"
    "s_lshl_b32 s71, s71, 12\n\t"
    "s_add_u32 s71, s71, s76\n\t"
    "buffer_load_dwordx4 v[32:35], v6, s[40:43], s71 offen offset:0 sc1\n\t"
    "s_waitcnt vmcnt(21) lgkmcnt(2)\n\t"
    "v_mfma_f32_32x32x16_bf16 v[112:127], v[108:111], a[64:67], v[112:127]\n\t"
    "ds_read_b64_tr_b16 v[104:105], v5 offset:10240\n\t"
    "ds_read_b64_tr_b16 v[106:107], v5 offset:10496\n\t"
    "v_cos_f32 v77, v70\n\t"
    "v_mfma_f32_32x32x16_bf16 v[128:143], v[108:111], a[68:71], v[128:143]\n\t"
    "s_nop 0\n\t"
    "v_fma_f32 v78, v69, v77, -v76\n\t"
    "s_add_u32 s71, s79, 7\n\t"
    "s_and_b32 s71, s71, 7\n\t"
    "s_lshl_b32 s71, s71, 12\n\t"
    "s_add_u32 s71, s71, s76\n\t"
    "buffer_load_dwordx4 v[36:39], v6, s[40:43], s71 offen offset:0 sc1\n\t"
    "s_add_u32 s71, s80, 2\n\t"
    "s_and_b32 s71, s71, 7\n\t"
    "s_lshl_b32 s71, s71, 12\n\t"
    "s_add_u32 s71, s71, s77\n\t"
    "buffer_load_dwordx4 v[40:43], v68, s[40:43], s71 offen sc1\n\t"
    "s_waitcnt vmcnt(22) lgkmcnt(2)\n\t"
    "v_mfma_f32_32x32x16_bf16 v[112:127], v[100:103], a[72:75], v[112:127]\n\t"
    "ds_read_b64_tr_b16 v[108:109], v5 offset:11264\n\t"
    "ds_read_b64_tr_b16 v[110:111], v5 offset:11520\n\t"
    "v_fma_f32 v79, v69, v78, -v77\n\t"
    "v_mfma_f32_32x32x16_bf16 v[128:143], v[100:103], a[76:79], v[128:143]\n\t"
    "v_cvt_pk_bf16_f32 v96, v76, v77\n\t"
    "v_fma_f32 v80, v69, v79, -v78\n\t"
    "s_add_u32 s71, s80, 3\n\t"
    "s_and_b32 s71, s71, 7\n\t"
    "s_lshl_b32 s71, s71, 12\n\t"
    "s_add_u32 s71, s71, s77\n\t"
    "buffer_load_dwordx4 v[44:47], v68, s[40:43], s71 offen sc1\n\t"
    "s_add_u32 s71, s80, 4\n\t"
    "s_and_b32 s71, s71, 7\n\t"
    "s_lshl_b32 s71, s71, 12\n\t"
    "s_add_u32 s71, s71, s77\n\t"
    "buffer_load_dwordx4 v[48:51], v68, s[40:43], s71 offen sc1\n\t"
    "s_waitcnt vmcnt(23) lgkmcnt(2)\n\t"
    "v_mfma_f32_32x32x16_bf16 v[112:127], v[104:107], a[80:83], v[112:127]\n\t"
    "ds_read_b64_tr_b16 v[100:101], v5 offset:0\n\t"
    "ds_read_b64_tr_b16 v[102:103], v5 offset:256\n\t"
    "v_fma_f32 v81, v69, v80, -v79\n\t"
    "v_mfma_f32_32x32x16_bf16 v[128:143], v[104:107], a[84:87], v[128:143]\n\t"
    "v_cvt_pk_bf16_f32 v97, v78, v79\n\t"
    "v_fma_f32 v82, v69, v81, -v80\n\t"
    "s_add_u32 s71, s79, 3\n\t"
    "s_and_b32 s71, s71, 7\n\t"
    "s_lshl_b32 s71, s71, 12\n\t"
    "s_add_u32 s71, s71, s76\n\t"
    "buffer_load_dwordx4 v[144:147], v6, s[40:43], s71 offen offset:1024 sc1\n\t"
    "s_add_u32 s71, s79, 4\n\t"
    "s_and_b32 s71, s71, 7\n\t"
    "s_lshl_b32 s71, s71, 12\n\t"
    "s_add_u32 s71, s71, s76\n\t"
    "buffer_load_dwordx4 v[148:151], v6, s[40:43], s71 offen offset:1024 sc1\n\t"
    "s_waitcnt vmcnt(24) lgkmcnt(2)\n\t"
    "v_mfma_f32_32x32x16_bf16 v[112:127], v[108:111], a[88:91], v[112:127]\n\t"
    "ds_read_b64_tr_b16 v[104:105], v5 offset:1024\n\t"
    "ds_read_b64_tr_b16 v[106:107], v5 offset:1280\n\t"
    "v_fma_f32 v83, v69, v82, -v81\n\t"
    "v_mfma_f32_32x32x16_bf16 v[128:143], v[108:111], a[92:95], v[128:143]\n\t"
    "v_cvt_pk_bf16_f32 v98, v80, v81\n\t"
    "v_cvt_pk_bf16_f32 v99, v82, v83\n\t"
    "s_add_u32 s71, s79, 5\n\t"
    "s_and_b32 s71, s71, 7\n\t"
    "s_lshl_b32 s71, s71, 12\n\t"
    "s_add_u32 s71, s71, s76\n\t"
    "buffer_load_dwordx4 v[152:155], v6, s[40:43], s71 offen offset:1024 sc1\n\t"
    "s_add_u32 s71, s79, 6\n\t"
    "s_and_b32 s71, s71, 7\n\t"
    "s_lshl_b32 s71, s71, 12\n\t"
    "s_add_u32 s71, s71, s76\n\t"
    "buffer_load_dwordx4 v[156:159], v6, s[40:43], s71 offen offset:1024 sc1\n\t"
    "v_mfma_f32_32x32x16_bf16 v[112:127], v[96:99], v[184:187], v[112:127]\n\t"
    "v_mfma_f32_32x32x16_bf16 v[128:143], v[96:99], v[188:191], v[128:143]\n\t"
    "s_waitcnt vmcnt(24) lgkmcnt(2)\n\t"
    "v_mfma_f32_32x32x16_bf16 v[112:127], v[100:103], a[96:99], v[112:127]\n\t"
    "ds_read_b64_tr_b16 v[108:109], v5 offset:2048\n\t"
    "ds_read_b64_tr_b16 v[110:111], v5 offset:2304\n\t"
    "v_mul_f32 v70, v71, v75\n\t"
    "v_mfma_f32_32x32x16_bf16 v[128:143], v[100:103], a[100:103], v[128:143]\n\t"
    "v_fract_f32 v70, v70\n\t"
    "s_add_u32 s71, s79, 7\n\t"
    "s_and_b32 s71, s71, 7\n\t"
    "s_lshl_b32 s71, s71, 12\n\t"
    "s_add_u32 s71, s71, s76\n\t"
    "buffer_load_dwordx4 v[160:163], v6, s[40:43], s71 offen offset:1024 sc1\n\t"
    "s_add_u32 s71, s80, 5\n\t"
    "s_and_b32 s71, s71, 7\n\t"
    "s_lshl_b32 s71, s71, 12\n\t"
    "s_add_u32 s71, s71, s77\n\t"
    "buffer_load_dwordx4 v[164:167], v68, s[40:43], s71 offen sc1\n\t"
    "s_waitcnt vmcnt(24) lgkmcnt(2)\n\t"
    "v_mfma_f32_32x32x16_bf16 v[112:127], v[104:107], a[104:107], v[112:127]\n\t"
    "ds_read_b64_tr_b16 v[100:101], v5 offset:3072\n\t"
    "ds_read_b64_tr_b16 v[102:103], v5 offset:3328\n\t"
    "v_cos_f32 v76, v70\n\t"
    "v_mfma_f32_32x32x16_bf16 v[128:143], v[104:107], a[108:111], v[128:143]\n\t"
    "v_add_f32 v70, v70, v72\n\t"
    "s_add_u32 s71, s80, 6\n\t"
    "s_and_b32 s71, s71, 7\n\t"
    "s_lshl_b32 s71, s71, 12\n\t"
    "s_add_u32 s71, s71, s77\n\t"
    "buffer_load_dwordx4 v[168:171], v68, s[40:43], s71 offen sc1\n\t"
    "s_add_u32 s71, s80, 7\n\t"
    "s_and_b32 s71, s71, 7\n\t"
    "s_lshl_b32 s71, s71, 12\n\t"
    "s_add_u32 s71, s71, s77\n\t"
    "buffer_load_dwordx4 v[172:175], v68, s[40:43], s71 offen sc1\n\t"
    "s_waitcnt vmcnt(24) lgkmcnt(2)\n\t"
    "v_mfma_f32_32x32x16_bf16 v[112:127], v[108:111], a[112:115], v[112:127]\n\t"
    "ds_read_b64_tr_b16 v[104:105], v5 offset:4096\n\t"
    "ds_read_b64_tr_b16 v[106:107], v5 offset:4352\n\t"
    "v_cos_f32 v77, v70\n\t"
    "v_mfma_f32_32x32x16_bf16 v[128:143], v[108:111], a[116:119], v[128:143]\n\t"
    "s_nop 0\n\t"
    "v_fma_f32 v78, v69, v77, -v76\n\t"
    "s_waitcnt vmcnt(22) lgkmcnt(2)\n\t"
    "s_bfe_u32 s70, s89, 327695\n\t"
    "s_lshl_b32 s70, s70, 6\n\t"
    "s_add_u32 s70, s70, s92\n\t"
    "buffer_store_dword v19, v7, s[40:43], s70 offen sc1\n\t"
    "v_mfma_f32_32x32x16_bf16 v[112:127], v[100:103], a[120:123], v[112:127]\n\t"
    "ds_read_b64_tr_b16 v[108:109], v5 offset:5120\n\t"
    "ds_read_b64_tr_b16 v[110:111], v5 offset:5376\n\t"
    "v_fma_f32 v79, v69, v78, -v77\n\t"
    "v_mfma_f32_32x32x16_bf16 v[128:143], v[100:103], a[124:127], v[128:143]\n\t"
    "v_cvt_pk_bf16_f32 v92, v76, v77\n\t"
    "v_fma_f32 v80, v69, v79, -v78\n\t"
    "s_waitcnt lgkmcnt(2)\n\t"
    "v_mfma_f32_32x32x16_bf16 v[112:127], v[104:107], a[128:131], v[112:127]\n\t"
    "v_fma_f32 v81, v69, v80, -v79\n\t"
    "v_mfma_f32_32x32x16_bf16 v[128:143], v[104:107], a[132:135], v[128:143]\n\t"
    "v_cvt_pk_bf16_f32 v93, v78, v79\n\t"
    "v_fma_f32 v82, v69, v81, -v80\n\t"
    "s_waitcnt lgkmcnt(0)\n\t"
    "v_mfma_f32_32x32x16_bf16 v[112:127], v[108:111], a[136:139], v[112:127]\n\t"
    "v_fma_f32 v83, v69, v82, -v81\n\t"
    "v_mfma_f32_32x32x16_bf16 v[128:143], v[108:111], a[140:143], v[128:143]\n\t"
    "v_cvt_pk_bf16_f32 v94, v80, v81\n\t"
    "v_cvt_pk_bf16_f32 v95, v82, v83\n\t"
    "s_nop 1\n\t"
    "v_mfma_f32_32x32x16_bf16 v[112:127], v[92:95], v[192:195], v[112:127]\n\t"
    "v_mfma_f32_32x32x16_bf16 v[128:143], v[92:95], v[196:199], v[128:143]\n\t"
    "s_mov_b32 s73, 0\n\t"
    "buffer_load_dword v11, v67, s[40:43], s73 offen sc1\n\t"
    "v_lshlrev_b32 v16, 4, v1\n\t"
    "s_xor_b32 s70, s64, 1\n\t"
    "s_mul_i32 s70, s70, 12288\n\t"
    "s_add_u32 s70, s70, 74752\n\t"
    "v_add_u32 v17, s70, v16\n\t"
    "s_mul_i32 s70, s64, 12288\n\t"
    "s_add_u32 s70, s70, 74752\n\t"
    "v_add_u32 v16, s70, v16\n\t"
    "s_nop 1\n\t"
    "ds_write_b128 v16, v[128:131] offset:0\n\t"
    "ds_write_b128 v16, v[132:135] offset:1024\n\t"
    "ds_write_b128 v16, v[136:139] offset:2048\n\t"
    "ds_write_b128 v16, v[140:143] offset:3072\n\t"
    "s_waitcnt lgkmcnt(0)\n\t"
    "s_barrier\n\t"
    "ds_read_b128 v[128:131], v17 offset:0\n\t"
    "ds_read_b128 v[132:135], v17 offset:1024\n\t"
    "ds_read_b128 v[136:139], v17 offset:2048\n\t"
    "ds_read_b128 v[140:143], v17 offset:3072\n\t"
    "v_mov_b32 v76, 0\n\t"
    "v_mov_b32 v77, 0\n\t"
    "v_mov_b32 v78, 0\n\t"
    "v_mov_b32 v79, 0\n\t"
    "v_mov_b32 v80, 0\n\t"
    "v_mov_b32 v81, 0\n\t"
    "v_mov_b32 v82, 0\n\t"
    "v_mov_b32 v83, 0\n\t"
    "s_mov_b32 s74, 0x00003f80\n\t"
    "s_mov_b32 s75, 0x3f800000\n\t"
    "s_waitcnt lgkmcnt(3)\n\t"
    "v_add_f32 v112, v128, v112\n\t"
    "v_add_f32 v113, v129, v113\n\t"
    "v_add_f32 v114, v130, v114\n\t"
    "v_add_f32 v115, v131, v115\n\t"
    "s_waitcnt lgkmcnt(2)\n\t"
    "v_add_f32 v116, v132, v116\n\t"
    "v_add_f32 v117, v133, v117\n\t"
    "v_add_f32 v118, v134, v118\n\t"
    "v_add_f32 v119, v135, v119\n\t"
    "s_waitcnt lgkmcnt(1)\n\t"
    "v_add_f32 v120, v136, v120\n\t"
    "v_add_f32 v121, v137, v121\n\t"
    "v_add_f32 v122, v138, v122\n\t"
    "v_add_f32 v123, v139, v123\n\t"
    "s_waitcnt lgkmcnt(0)\n\t"
    "v_add_f32 v124, v140, v124\n\t"
    "v_add_f32 v125, v141, v125\n\t"
    "v_add_f32 v126, v142, v126\n\t"
    "v_add_f32 v127, v143, v127\n\t"
    "s_waitcnt vmcnt(17)\n\t"
    "v_dot2c_f32_bf16 v112, s74, v20\n\t"
    "v_dot2c_f32_bf16 v113, s75, v20\n\t"
    "v_dot2c_f32_bf16 v114, s74, v21\n\t"
    "v_dot2c_f32_bf16 v115, s75, v21\n\t"
    "v_dot2c_f32_bf16 v116, s74, v22\n\t"
    "v_dot2c_f32_bf16 v117, s75, v22\n\t"
    "v_dot2c_f32_bf16 v118, s74, v23\n\t"
    "v_dot2c_f32_bf16 v119, s75, v23\n\t"
    "s_waitcnt vmcnt(16)\n\t"
    "v_dot2c_f32_bf16 v112, s74, v24\n\t"
    "v_dot2c_f32_bf16 v113, s75, v24\n\t"
    "v_dot2c_f32_bf16 v114, s74, v25\n\t"
    "v_dot2c_f32_bf16 v115, s75, v25\n\t"
    "v_dot2c_f32_bf16 v116, s74, v26\n\t"
    "v_dot2c_f32_bf16 v117, s75, v26\n\t"
    "v_dot2c_f32_bf16 v118, s74, v27\n\t"
    "v_dot2c_f32_bf16 v119, s75, v27\n\t"
    "s_waitcnt vmcnt(15)\n\t"
    "v_dot2c_f32_bf16 v112, s74, v28\n\t"
    "v_dot2c_f32_bf16 v113, s75, v28\n\t"
    "v_dot2c_f32_bf16 v114, s74, v29\n\t"
    "v_dot2c_f32_bf16 v115, s75, v29\n\t"
    "v_dot2c_f32_bf16 v116, s74, v30\n\t"
    "v_dot2c_f32_bf16 v117, s75, v30\n\t"
    "v_dot2c_f32_bf16 v118, s74, v31\n\t"
    "v_dot2c_f32_bf16 v119, s75, v31\n\t"
    "s_waitcnt vmcnt(14)\n\t"
    "v_dot2c_f32_bf16 v112, s74, v32\n\t"
    "v_dot2c_f32_bf16 v113, s75, v32\n\t"
    "v_dot2c_f32_bf16 v114, s74, v33\n\t"
    "v_dot2c_f32_bf16 v115, s75, v33\n\t"
    "v_dot2c_f32_bf16 v116, s74, v34\n\t"
    "v_dot2c_f32_bf16 v117, s75, v34\n\t"
    "v_dot2c_f32_bf16 v118, s74, v35\n\t"
    "v_dot2c_f32_bf16 v119, s75, v35\n\t"
    "s_waitcnt vmcnt(13)\n\t"
    "v_dot2c_f32_bf16 v112, s74, v36\n\t"
    "v_dot2c_f32_bf16 v113, s75, v36\n\t"
    "v_dot2c_f32_bf16 v114, s74, v37\n\t"
    "v_dot2c_f32_bf16 v115, s75, v37\n\t"
    "v_dot2c_f32_bf16 v116, s74, v38\n\t"
    "v_dot2c_f32_bf16 v117, s75, v38\n\t"
    "v_dot2c_f32_bf16 v118, s74, v39\n\t"
    "v_dot2c_f32_bf16 v119, s75, v39\n\t"
    "s_waitcnt vmcnt(9)\n\t"
    "v_dot2c_f32_bf16 v120, s74, v144\n\t"
    "v_dot2c_f32_bf16 v121, s75, v144\n\t"
    "v_dot2c_f32_bf16 v122, s74, v145\n\t"
    "v_dot2c_f32_bf16 v123, s75, v145\n\t"
    "v_dot2c_f32_bf16 v124, s74, v146\n\t"
    "v_dot2c_f32_bf16 v125, s75, v146\n\t"
    "v_dot2c_f32_bf16 v126, s74, v147\n\t"
    "v_dot2c_f32_bf16 v127, s75, v147\n\t"
    "s_waitcnt vmcnt(8)\n\t"
    "v_dot2c_f32_bf16 v120, s74, v148\n\t"
    "v_dot2c_f32_bf16 v121, s75, v148\n\t"
    "v_dot2c_f32_bf16 v122, s74, v149\n\t"
    "v_dot2c_f32_bf16 v123, s75, v149\n\t"
    "v_dot2c_f32_bf16 v124, s74, v150\n\t"
    "v_dot2c_f32_bf16 v125, s75, v150\n\t"
    "v_dot2c_f32_bf16 v126, s74, v151\n\t"
    "v_dot2c_f32_bf16 v127, s75, v151\n\t"
    "s_waitcnt vmcnt(7)\n\t"
    "v_dot2c_f32_bf16 v120, s74, v152\n\t"
    "v_dot2c_f32_bf16 v121, s75, v152\n\t"
    "v_dot2c_f32_bf16 v122, s74, v153\n\t"
    "v_dot2c_f32_bf16 v123, s75, v153\n\t"
    "v_dot2c_f32_bf16 v124, s74, v154\n\t"
    "v_dot2c_f32_bf16 v125, s75, v154\n\t"
    "v_dot2c_f32_bf16 v126, s74, v155\n\t"
    "v_dot2c_f32_bf16 v127, s75, v155\n\t"
    "s_waitcnt vmcnt(6)\n\t"
    "v_dot2c_f32_bf16 v120, s74, v156\n\t"
    "v_dot2c_f32_bf16 v121, s75, v156\n\t"
    "v_dot2c_f32_bf16 v122, s74, v157\n\t"
    "v_dot2c_f32_bf16 v123, s75, v157\n\t"
    "v_dot2c_f32_bf16 v124, s74, v158\n\t"
    "v_dot2c_f32_bf16 v125, s75, v158\n\t"
    "v_dot2c_f32_bf16 v126, s74, v159\n\t"
    "v_dot2c_f32_bf16 v127, s75, v159\n\t"
    "s_waitcnt vmcnt(5)\n\t"
    "v_dot2c_f32_bf16 v120, s74, v160\n\t"
    "v_dot2c_f32_bf16 v121, s75, v160\n\t"
    "v_dot2c_f32_bf16 v122, s74, v161\n\t"
    "v_dot2c_f32_bf16 v123, s75, v161\n\t"
    "v_dot2c_f32_bf16 v124, s74, v162\n\t"
    "v_dot2c_f32_bf16 v125, s75, v162\n\t"
    "v_dot2c_f32_bf16 v126, s74, v163\n\t"
    "v_dot2c_f32_bf16 v127, s75, v163\n\t"
    "v_dot2c_f32_bf16 v76, s74, v40\n\t"
    "v_dot2c_f32_bf16 v77, s75, v40\n\t"
    "v_dot2c_f32_bf16 v78, s74, v41\n\t"
    "v_dot2c_f32_bf16 v79, s75, v41\n\t"
    "v_dot2c_f32_bf16 v80, s74, v42\n\t"
    "v_dot2c_f32_bf16 v81, s75, v42\n\t"
    "v_dot2c_f32_bf16 v82, s74, v43\n\t"
    "v_dot2c_f32_bf16 v83, s75, v43\n\t"
    "v_dot2c_f32_bf16 v76, s74, v44\n\t"
    "v_dot2c_f32_bf16 v77, s75, v44\n\t"
    "v_dot2c_f32_bf16 v78, s74, v45\n\t"
    "v_dot2c_f32_bf16 v79, s75, v45\n\t"
    "v_dot2c_f32_bf16 v80, s74, v46\n\t"
    "v_dot2c_f32_bf16 v81, s75, v46\n\t"
    "v_dot2c_f32_bf16 v82, s74, v47\n\t"
    "v_dot2c_f32_bf16 v83, s75, v47\n\t"
    "v_dot2c_f32_bf16 v76, s74, v48\n\t"
    "v_dot2c_f32_bf16 v77, s75, v48\n\t"
    "v_dot2c_f32_bf16 v78, s74, v49\n\t"
    "v_dot2c_f32_bf16 v79, s75, v49\n\t"
    "v_dot2c_f32_bf16 v80, s74, v50\n\t"
    "v_dot2c_f32_bf16 v81, s75, v50\n\t"
    "v_dot2c_f32_bf16 v82, s74, v51\n\t"
    "v_dot2c_f32_bf16 v83, s75, v51\n\t"
    "s_waitcnt vmcnt(4)\n\t"
    "v_dot2c_f32_bf16 v76, s74, v164\n\t"
    "v_dot2c_f32_bf16 v77, s75, v164\n\t"
    "v_dot2c_f32_bf16 v78, s74, v165\n\t"
    "v_dot2c_f32_bf16 v79, s75, v165\n\t"
    "v_dot2c_f32_bf16 v80, s74, v166\n\t"
    "v_dot2c_f32_bf16 v81, s75, v166\n\t"
    "v_dot2c_f32_bf16 v82, s74, v167\n\t"
    "v_dot2c_f32_bf16 v83, s75, v167\n\t"
    "s_waitcnt vmcnt(3)\n\t"
    "v_dot2c_f32_bf16 v76, s74, v168\n\t"
    "v_dot2c_f32_bf16 v77, s75, v168\n\t"
    "v_dot2c_f32_bf16 v78, s74, v169\n\t"
    "v_dot2c_f32_bf16 v79, s75, v169\n\t"
    "v_dot2c_f32_bf16 v80, s74, v170\n\t"
    "v_dot2c_f32_bf16 v81, s75, v170\n\t"
    "v_dot2c_f32_bf16 v82, s74, v171\n\t"
    "v_dot2c_f32_bf16 v83, s75, v171\n\t"
    "s_waitcnt vmcnt(2)\n\t"
    "v_dot2c_f32_bf16 v76, s74, v172\n\t"
    "v_dot2c_f32_bf16 v77, s75, v172\n\t"
    "v_dot2c_f32_bf16 v78, s74, v173\n\t"
    "v_dot2c_f32_bf16 v79, s75, v173\n\t"
    "v_dot2c_f32_bf16 v80, s74, v174\n\t"
    "v_dot2c_f32_bf16 v81, s75, v174\n\t"
    "v_dot2c_f32_bf16 v82, s74, v175\n\t"
    "v_dot2c_f32_bf16 v83, s75, v175\n\t"
    "s_waitcnt vmcnt(0)\n\t"
    "v_cmp_ne_u32 vcc, s67, v11\n\t"
    "s_mov_b32 s94, 0x40000\n\t"
    "s_cbranch_vccz L_ok_26%=\n\t"
    "L_spin_27%=:\n\t"
    "s_sleep 1\n\t"
    "buffer_load_dword v11, v67, s[40:43], s73 offen sc1\n\t"
    "s_waitcnt vmcnt(0)\n\t"
    "v_cmp_ne_u32 vcc, s67, v11\n\t"
    "s_sub_u32 s94, s94, 1\n\t"
    "s_nop 3\n\t"
    "s_cbranch_vccz L_ok_26%=\n\t"
    "s_cmp_lg_u32 s94, 0\n\t"
    "s_cbranch_scc1 L_spin_27%=\n\t"
    "L_ok_26%=:\n\t"
    "s_add_u32 s71, s79, 1\n\t"
    "s_and_b32 s71, s71, 7\n\t"
    "s_lshl_b32 s71, s71, 12\n\t"
    "s_add_u32 s71, s71, s76\n\t"
    "buffer_load_dwordx4 v[176:179], v6, s[40:43], s71 offen offset:0 sc1\n\t"
    "s_add_u32 s71, s79, 1\n\t"
    "s_and_b32 s71, s71, 7\n\t"
    "s_lshl_b32 s71, s71, 12\n\t"
    "s_add_u32 s71, s71, s76\n\t"
    "buffer_load_dwordx4 v[184:187], v6, s[40:43], s71 offen offset:1024 sc1\n\t"
    "s_add_u32 s71, s80, 0\n\t"
    "s_and_b32 s71, s71, 7\n\t"
    "s_lshl_b32 s71, s71, 12\n\t"
    "s_add_u32 s71, s71, s77\n\t"
    "buffer_load_dwordx4 v[192:195], v68, s[40:43], s71 offen sc1\n\t"
    "s_add_u32 s71, s79, 2\n\t"
    "s_and_b32 s71, s71, 7\n\t"
    "s_lshl_b32 s71, s71, 12\n\t"
    "s_add_u32 s71, s71, s76\n\t"
    "buffer_load_dwordx4 v[180:183], v6, s[40:43], s71 offen offset:0 sc1\n\t"
    "s_add_u32 s71, s79, 2\n\t"
    "s_and_b32 s71, s71, 7\n\t"
    "s_lshl_b32 s71, s71, 12\n\t"
    "s_add_u32 s71, s71, s76\n\t"
    "buffer_load_dwordx4 v[188:191], v6, s[40:43], s71 offen offset:1024 sc1\n\t"
    "s_add_u32 s71, s80, 1\n\t"
    "s_and_b32 s71, s71, 7\n\t"
    "s_lshl_b32 s71, s71, 12\n\t"
    "s_add_u32 s71, s71, s77\n\t"
    "buffer_load_dwordx4 v[196:199], v68, s[40:43], s71 offen sc1\n\t"
    "s_mov_b32 s73, 0x3f317218\n\t"
    "s_waitcnt vmcnt(5)\n\t"
    "v_dot2c_f32_bf16 v112, s74, v176\n\t"
    "v_dot2c_f32_bf16 v113, s75, v176\n\t"
    "v_dot2c_f32_bf16 v114, s74, v177\n\t"
    "v_dot2c_f32_bf16 v115, s75, v177\n\t"
    "v_dot2c_f32_bf16 v116, s74, v178\n\t"
    "v_dot2c_f32_bf16 v117, s75, v178\n\t"
    "v_dot2c_f32_bf16 v118, s74, v179\n\t"
    "v_dot2c_f32_bf16 v119, s75, v179\n\t"
    "s_waitcnt vmcnt(2)\n\t"
    "v_dot2c_f32_bf16 v112, s74, v180\n\t"
    "v_dot2c_f32_bf16 v113, s75, v180\n\t"
    "v_dot2c_f32_bf16 v114, s74, v181\n\t"
    "v_dot2c_f32_bf16 v115, s75, v181\n\t"
    "v_dot2c_f32_bf16 v116, s74, v182\n\t"
    "v_dot2c_f32_bf16 v117, s75, v182\n\t"
    "v_dot2c_f32_bf16 v118, s74, v183\n\t"
    "v_dot2c_f32_bf16 v119, s75, v183\n\t"
    "v_lshlrev_b32 v88, 16, v52\n\t"
    "v_and_b32 v89, 0xffff0000, v52\n\t"
    "v_fma_f32 v88, v112, s73, v88\n\t"
    "v_fma_f32 v89, v113, s73, v89\n\t"
    "v_cvt_pk_bf16_f32 v84, v88, v89\n\t"
    "v_lshlrev_b32 v88, 16, v53\n\t"
    "v_and_b32 v89, 0xffff0000, v53\n\t"
    "v_fma_f32 v88, v114, s73, v88\n\t"
    "v_fma_f32 v89, v115, s73, v89\n\t"
    "v_cvt_pk_bf16_f32 v85, v88, v89\n\t"
    "v_lshlrev_b32 v88, 16, v54\n\t"
    "v_and_b32 v89, 0xffff0000, v54\n\t"
    "v_fma_f32 v88, v116, s73, v88\n\t"
    "v_fma_f32 v89, v117, s73, v89\n\t"
    "v_cvt_pk_bf16_f32 v86, v88, v89\n\t"
    "v_lshlrev_b32 v88, 16, v55\n\t"
    "v_and_b32 v89, 0xffff0000, v55\n\t"
    "v_fma_f32 v88, v118, s73, v88\n\t"
    "v_fma_f32 v89, v119, s73, v89\n\t"
    "v_cvt_pk_bf16_f32 v87, v88, v89\n\t"
    "buffer_store_dwordx2 v[84:85], v64, s[60:63], 0 offen\n\t"
    "buffer_store_dwordx2 v[86:87], v64, s[60:63], 0 offen offset:16\n\t"
    "v_dot2c_f32_bf16 v120, s74, v184\n\t"
    "v_dot2c_f32_bf16 v121, s75, v184\n\t"
    "v_dot2c_f32_bf16 v122, s74, v185\n\t"
    "v_dot2c_f32_bf16 v123, s75, v185\n\t"
    "v_dot2c_f32_bf16 v124, s74, v186\n\t"
    "v_dot2c_f32_bf16 v125, s75, v186\n\t"
    "v_dot2c_f32_bf16 v126, s74, v187\n\t"
    "v_dot2c_f32_bf16 v127, s75, v187\n\t"
    "s_waitcnt vmcnt(3)\n\t"
    "v_dot2c_f32_bf16 v120, s74, v188\n\t"
    "v_dot2c_f32_bf16 v121, s75, v188\n\t"
    "v_dot2c_f32_bf16 v122, s74, v189\n\t"
    "v_dot2c_f32_bf16 v123, s75, v189\n\t"
    "v_dot2c_f32_bf16 v124, s74, v190\n\t"
    "v_dot2c_f32_bf16 v125, s75, v190\n\t"
    "v_dot2c_f32_bf16 v126, s74, v191\n\t"
    "v_dot2c_f32_bf16 v127, s75, v191\n\t"
    "v_lshlrev_b32 v88, 16, v56\n\t"
    "v_and_b32 v89, 0xffff0000, v56\n\t"
    "v_fma_f32 v88, v120, s73, v88\n\t"
    "v_fma_f32 v89, v121, s73, v89\n\t"
    "v_cvt_pk_bf16_f32 v84, v88, v89\n\t"
    "v_lshlrev_b32 v88, 16, v57\n\t"
    "v_and_b32 v89, 0xffff0000, v57\n\t"
    "v_fma_f32 v88, v122, s73, v88\n\t"
    "v_fma_f32 v89, v123, s73, v89\n\t"
    "v_cvt_pk_bf16_f32 v85, v88, v89\n\t"
    "v_lshlrev_b32 v88, 16, v58\n\t"
    "v_and_b32 v89, 0xffff0000, v58\n\t"
    "v_fma_f32 v88, v124, s73, v88\n\t"
    "v_fma_f32 v89, v125, s73, v89\n\t"
    "v_cvt_pk_bf16_f32 v86, v88, v89\n\t"
    "v_lshlrev_b32 v88, 16, v59\n\t"
    "v_and_b32 v89, 0xffff0000, v59\n\t"
    "v_fma_f32 v88, v126, s73, v88\n\t"
    "v_fma_f32 v89, v127, s73, v89\n\t"
    "v_cvt_pk_bf16_f32 v87, v88, v89\n\t"
    "buffer_store_dwordx2 v[84:85], v65, s[60:63], 0 offen\n\t"
    "buffer_store_dwordx2 v[86:87], v65, s[60:63], 0 offen offset:16\n\t"
    "v_dot2c_f32_bf16 v76, s74, v192\n\t"
    "v_dot2c_f32_bf16 v77, s75, v192\n\t"
    "v_dot2c_f32_bf16 v78, s74, v193\n\t"
    "v_dot2c_f32_bf16 v79, s75, v193\n\t"
    "v_dot2c_f32_bf16 v80, s74, v194\n\t"
    "v_dot2c_f32_bf16 v81, s75, v194\n\t"
    "v_dot2c_f32_bf16 v82, s74, v195\n\t"
    "v_dot2c_f32_bf16 v83, s75, v195\n\t"
    "s_waitcnt vmcnt(4)\n\t"
    "v_dot2c_f32_bf16 v76, s74, v196\n\t"
    "v_dot2c_f32_bf16 v77, s75, v196\n\t"
    "v_dot2c_f32_bf16 v78, s74, v197\n\t"
    "v_dot2c_f32_bf16 v79, s75, v197\n\t"
    "v_dot2c_f32_bf16 v80, s74, v198\n\t"
    "v_dot2c_f32_bf16 v81, s75, v198\n\t"
    "v_dot2c_f32_bf16 v82, s74, v199\n\t"
    "v_dot2c_f32_bf16 v83, s75, v199\n\t"
    "v_lshlrev_b32 v88, 16, v60\n\t"
    "v_and_b32 v89, 0xffff0000, v60\n\t"
    "v_fma_f32 v88, v76, s73, v88\n\t"
    "v_fma_f32 v89, v77, s73, v89\n\t"
    "v_cvt_pk_bf16_f32 v84, v88, v89\n\t"
    "v_lshlrev_b32 v88, 16, v61\n\t"
    "v_and_b32 v89, 0xffff0000, v61\n\t"
    "v_fma_f32 v88, v78, s73, v88\n\t"
    "v_fma_f32 v89, v79, s73, v89\n\t"
    "v_cvt_pk_bf16_f32 v85, v88, v89\n\t"
    "v_lshlrev_b32 v88, 16, v62\n\t"
    "v_and_b32 v89, 0xffff0000, v62\n\t"
    "v_fma_f32 v88, v80, s73, v88\n\t"
    "v_fma_f32 v89, v81, s73, v89\n\t"
    "v_cvt_pk_bf16_f32 v86, v88, v89\n\t"
    "v_lshlrev_b32 v88, 16, v63\n\t"
    "v_and_b32 v89, 0xffff0000, v63\n\t"
    "v_fma_f32 v88, v82, s73, v88\n\t"
    "v_fma_f32 v89, v83, s73, v89\n\t"
    "v_cvt_pk_bf16_f32 v87, v88, v89\n\t"
    "buffer_store_dwordx2 v[84:85], v66, s[60:63], 0 offen\n\t"
    "buffer_store_dwordx2 v[86:87], v66, s[60:63], 0 offen offset:16\n\t"
    "s_branch L_p2end_23%=\n\t"
    "L_p2k1_22%=:\n\t"
    "s_waitcnt vmcnt(9) lgkmcnt(2)\n\t"
    "v_mfma_f32_32x32x16_bf16 v[112:127], v[100:103], a[144:147], 0\n\t"
    "ds_read_b64_tr_b16 v[108:109], v5 offset:2048\n\t"
    "buffer_load_dwordx4 v4, s[48:51], s68 offen offset:2048 lds\n\t"
    "s_add_u32 s68, s68, 55296\n\t"
    "ds_read_b64_tr_b16 v[110:111], v5 offset:2304\n\t"
    "v_fmaak_f32 v70, v71, v73, 0x3e800000\n\t"
    "v_mfma_f32_32x32x16_bf16 v[128:143], v[100:103], a[148:151], 0\n\t"
    "v_fract_f32 v70, v70\n\t"
    "s_waitcnt vmcnt(9) lgkmcnt(2)\n\t"
    "v_mfma_f32_32x32x16_bf16 v[112:127], v[104:107], a[152:155], v[112:127]\n\t"
    "s_add_u32 m0, s69, 0\n\t"
    "ds_read_b64_tr_b16 v[100:101], v5 offset:3072\n\t"
    "buffer_load_dwordx4 v2, s[48:51], s68 offen offset:0 lds\n\t"
    "ds_read_b64_tr_b16 v[102:103], v5 offset:3328\n\t"
    "v_cos_f32 v76, v70\n\t"
    "s_bfe_u32 s93, s89, 327695\n\t"
    "s_add_u32 s71, s93, s66\n\t"
    "s_add_u32 s71, s71, s65\n\t"
    "v_mfma_f32_32x32x16_bf16 v[128:143], v[104:107], a[156:159], v[128:143]\n\t"
    "v_add_f32 v70, v70, v72\n\t"
    "s_and_b32 s71, s71, 7\n\t"
    "s_lshl_b32 s71, s71, 12\n\t"
    "s_lshl_b32 s93, s93, 15\n\t"
    "s_add_u32 s93, s93, s91\n\t"
    "s_add_u32 s93, s93, s71\n\t"
    "v_cvt_pk_bf16_f32 v84, v144, v145\n\t"
    "v_cvt_pk_bf16_f32 v85, v146, v147\n\t"
    "s_waitcnt vmcnt(8) lgkmcnt(2)\n\t"
    "v_mfma_f32_32x32x16_bf16 v[112:127], v[108:111], a[160:163], v[112:127]\n\t"
    "ds_read_b64_tr_b16 v[104:105], v5 offset:4096\n\t"
    "buffer_load_dwordx4 v3, s[48:51], s68 offen offset:1024 lds\n\t"
    "ds_read_b64_tr_b16 v[106:107], v5 offset:4352\n\t"
    "v_cos_f32 v77, v70\n\t"
    "v_cvt_pk_bf16_f32 v86, v148, v149\n\t"
    "v_cvt_pk_bf16_f32 v87, v150, v151\n\t"
    "v_mfma_f32_32x32x16_bf16 v[128:143], v[108:111], a[164:167], v[128:143]\n\t"
    "s_nop 0\n\t"
    "v_fma_f32 v78, v69, v77, -v76\n\t"
    "buffer_store_dwordx4 v[84:87], v6, s[40:43], s93 offen offset:0 sc1\n\t"
    "v_cvt_pk_bf16_f32 v88, v152, v153\n\t"
    "v_cvt_pk_bf16_f32 v89, v154, v155\n\t"
    "v_cvt_pk_bf16_f32 v90, v156, v157\n\t"
    "s_waitcnt vmcnt(9) lgkmcnt(2)\n\t"
    "v_mfma_f32_32x32x16_bf16 v[112:127], v[100:103], a[168:171], v[112:127]\n\t"
    "ds_read_b64_tr_b16 v[108:109], v5 offset:5120\n\t"
    "buffer_load_dwordx4 v4, s[48:51], s68 offen offset:2048 lds\n\t"
    "s_add_u32 s68, s68, 55296\n\t"
    "ds_read_b64_tr_b16 v[110:111], v5 offset:5376\n\t"
    "v_fma_f32 v79, v69, v78, -v77\n\t"
    "v_cvt_pk_bf16_f32 v91, v158, v159\n\t"
    "v_mfma_f32_32x32x16_bf16 v[128:143], v[100:103], a[172:175], v[128:143]\n\t"
    "v_cvt_pk_bf16_f32 v92, v76, v77\n\t"
    "v_fma_f32 v80, v69, v79, -v78\n\t"
    "buffer_store_dwordx4 v[88:91], v6, s[40:43], s93 offen offset:1024 sc1\n\t"
    "v_cvt_pk_bf16_f32 v84, v160, v161\n\t"
    "v_cvt_pk_bf16_f32 v85, v162, v163\n\t"
    "v_cvt_pk_bf16_f32 v86, v164, v165\n\t"
    "v_cvt_pk_bf16_f32 v87, v166, v167\n\t"
    "s_waitcnt vmcnt(10) lgkmcnt(2)\n\t"
    "v_mfma_f32_32x32x16_bf16 v[112:127], v[104:107], a[176:179], v[112:127]\n\t"
    "s_add_u32 m0, s69, 3072\n\t"
    "ds_read_b64_tr_b16 v[100:101], v5 offset:6144\n\t"
    "buffer_load_dwordx4 v2, s[48:51], s68 offen offset:0 lds\n\t"
    "ds_read_b64_tr_b16 v[102:103], v5 offset:6400\n\t"
    "v_fma_f32 v81, v69, v80, -v79\n\t"
    "v_mfma_f32_32x32x16_bf16 v[128:143], v[104:107], a[180:183], v[128:143]\n\t"
    "v_cvt_pk_bf16_f32 v93, v78, v79\n\t"
    "v_fma_f32 v82, v69, v81, -v80\n\t"
    "buffer_store_dwordx4 v[84:87], v6, s[40:43], s93 offen offset:2048 sc1\n\t"
    "v_cvt_pk_bf16_f32 v88, v168, v169\n\t"
    "v_cvt_pk_bf16_f32 v89, v170, v171\n\t"
    "v_cvt_pk_bf16_f32 v90, v172, v173\n\t"
    "v_cvt_pk_bf16_f32 v91, v174, v175\n\t"
    "s_waitcnt vmcnt(11) lgkmcnt(2)\n\t"
    "v_mfma_f32_32x32x16_bf16 v[112:127], v[108:111], a[184:187], v[112:127]\n\t"
    "ds_read_b64_tr_b16 v[104:105], v5 offset:7168\n\t"
    "buffer_load_dwordx4 v3, s[48:51], s68 offen offset:1024 lds\n\t"
    "ds_read_b64_tr_b16 v[106:107], v5 offset:7424\n\t"
    "v_fma_f32 v83, v69, v82, -v81\n\t"
    "buffer_store_dwordx4 v[88:91], v6, s[40:43], s93 offen offset:3072 sc1\n\t"
    "v_mfma_f32_32x32x16_bf16 v[128:143], v[108:111], a[188:191], v[128:143]\n\t"
    "v_cvt_pk_bf16_f32 v94, v80, v81\n\t"
    "v_cvt_pk_bf16_f32 v95, v82, v83\n\t"
    "s_nop 1\n\t"
    "v_mfma_f32_32x32x16_bf16 v[112:127], v[92:95], v[200:203], v[112:127]\n\t"
    "v_mfma_f32_32x32x16_bf16 v[128:143], v[92:95], v[204:207], v[128:143]\n\t"
    "s_waitcnt vmcnt(12) lgkmcnt(2)\n\t"
    "v_mfma_f32_32x32x16_bf16 v[112:127], v[100:103], a[192:195], v[112:127]\n\t"
    "ds_read_b64_tr_b16 v[108:109], v5 offset:8192\n\t"
    "buffer_load_dwordx4 v4, s[48:51], s68 offen offset:2048 lds\n\t"
    "ds_read_b64_tr_b16 v[110:111], v5 offset:8448\n\t"
    "v_fmaak_f32 v70, v71, v74, 0x3e800000\n\t"
    "v_mfma_f32_32x32x16_bf16 v[128:143], v[100:103], a[196:199], v[128:143]\n\t"
    "v_fract_f32 v70, v70\n\t"
    "buffer_load_dwordx2 v[52:53], v64, s[36:39], 0 offen\n\t"
    "buffer_load_dwordx2 v[54:55], v64, s[36:39], 0 offen offset:16\n\t"
    "buffer_load_dwordx2 v[56:57], v65, s[36:39], 0 offen\n\t"
    "buffer_load_dwordx2 v[58:59], v65, s[36:39], 0 offen offset:16\n\t"
    "buffer_load_dwordx2 v[60:61], v66, s[36:39], 0 offen\n\t"
    "buffer_load_dwordx2 v[62:63], v66, s[36:39], 0 offen offset:16\n\t"
    "v_cmp_ne_u32 vcc, s67, v11\n\t"
    "s_mov_b32 s94, 0x40000\n\t"
    "s_cbranch_vccz L_ok_28%=\n\t"
    "L_spin_29%=:\n\t"
    "s_sleep 1\n\t"
    "buffer_load_dword v11, v12, s[40:43], s73 offen sc1\n\t"
    "s_waitcnt vmcnt(0)\n\t"
    "v_cmp_ne_u32 vcc, s67, v11\n\t"
    "s_sub_u32 s94, s94, 1\n\t"
    "s_nop 3\n\t"
    "s_cbranch_vccz L_ok_28%=\n\t"
    "s_cmp_lg_u32 s94, 0\n\t"
    "s_cbranch_scc1 L_spin_29%=\n\t"
    "L_ok_28%=:\n\t"
    "s_add_u32 s71, s79, 3\n\t"
    "s_and_b32 s71, s71, 7\n\t"
    "s_lshl_b32 s71, s71, 12\n\t"
    "s_add_u32 s71, s71, s76\n\t"
    "buffer_load_dwordx4 v[20:23], v6, s[40:43], s71 offen offset:2048 sc1\n\t"
    "s_add_u32 s71, s79, 4\n\t"
    "s_and_b32 s71, s71, 7\n\t"
    "s_lshl_b32 s71, s71, 12\n\t"
    "s_add_u32 s71, s71, s76\n\t"
    "buffer_load_dwordx4 v[24:27], v6, s[40:43], s71 offen offset:2048 sc1\n\t"
    "s_waitcnt vmcnt(20) lgkmcnt(2)\n\t"
    "v_mfma_f32_32x32x16_bf16 v[112:127], v[104:107], a[200:203], v[112:127]\n\t"
    "ds_read_b64_tr_b16 v[100:101], v5 offset:9216\n\t"
    "ds_read_b64_tr_b16 v[102:103], v5 offset:9472\n\t"
    "v_cos_f32 v76, v70\n\t"
    "v_mfma_f32_32x32x16_bf16 v[128:143], v[104:107], a[204:207], v[128:143]\n\t"
    "v_add_f32 v70, v70, v72\n\t"
    "s_add_u32 s71, s79, 5\n\t"
    "s_and_b32 s71, s71, 7\n\t"
    "s_lshl_b32 s71, s71, 12\n\t"
    "s_add_u32 s71, s71, s76\n\t"
    "buffer_load_dwordx4 v[28:31], v6, s[40:43], s71 offen offset:2048 sc1\n\t"
    "s_add_u32 s71, s79, 6\n\t"
    "s_and_b32 s71, s71, 7\n\t"
    "s_lshl_b32 s71, s71, 12\n\t"
    "s_add_u32 s71, s71, s76\n\t"
    "buffer_load_dwordx4 v[32:35], v6, s[40:43], s71 offen offset:2048 sc1\n\t"
    "s_waitcnt vmcnt(21) lgkmcnt(2)\n\t"
    "v_mfma_f32_32x32x16_bf16 v[112:127], v[108:111], a[208:211], v[112:127]\n\t"
    "ds_read_b64_tr_b16 v[104:105], v5 offset:10240\n\t"
    "ds_read_b64_tr_b16 v[106:107], v5 offset:10496\n\t"
    "v_cos_f32 v77, v70\n\t"
    "v_mfma_f32_32x32x16_bf16 v[128:143], v[108:111], a[212:215], v[128:143]\n\t"
    "s_nop 0\n\t"
    "v_fma_f32 v78, v69, v77, -v76\n\t"
    "s_add_u32 s71, s79, 7\n\t"
    "s_and_b32 s71, s71, 7\n\t"
    "s_lshl_b32 s71, s71, 12\n\t"
    "s_add_u32 s71, s71, s76\n\t"
    "buffer_load_dwordx4 v[36:39], v6, s[40:43], s71 offen offset:2048 sc1\n\t"
    "s_add_u32 s71, s80, 2\n\t"
    "s_and_b32 s71, s71, 7\n\t"
    "s_lshl_b32 s71, s71, 12\n\t"
    "s_add_u32 s71, s71, s77\n\t"
    "buffer_load_dwordx4 v[40:43], v68, s[40:43], s71 offen sc1\n\t"
    "s_waitcnt vmcnt(22) lgkmcnt(2)\n\t"
    "v_mfma_f32_32x32x16_bf16 v[112:127], v[100:103], a[216:219], v[112:127]\n\t"
    "ds_read_b64_tr_b16 v[108:109], v5 offset:11264\n\t"
    "ds_read_b64_tr_b16 v[110:111], v5 offset:11520\n\t"
    "v_fma_f32 v79, v69, v78, -v77\n\t"
    "v_mfma_f32_32x32x16_bf16 v[128:143], v[100:103], a[220:223], v[128:143]\n\t"
    "v_cvt_pk_bf16_f32 v96, v76, v77\n\t"
    "v_fma_f32 v80, v69, v79, -v78\n\t"
    "s_add_u32 s71, s80, 3\n\t"
    "s_and_b32 s71, s71, 7\n\t"
    "s_lshl_b32 s71, s71, 12\n\t"
    "s_add_u32 s71, s71, s77\n\t"
    "buffer_load_dwordx4 v[44:47], v68, s[40:43], s71 offen sc1\n\t"
    "s_add_u32 s71, s80, 4\n\t"
    "s_and_b32 s71, s71, 7\n\t"
    "s_lshl_b32 s71, s71, 12\n\t"
    "s_add_u32 s71, s71, s77\n\t"
    "buffer_load_dwordx4 v[48:51], v68, s[40:43], s71 offen sc1\n\t"
    "s_waitcnt vmcnt(23) lgkmcnt(2)\n\t"
    "v_mfma_f32_32x32x16_bf16 v[112:127], v[104:107], a[224:227], v[112:127]\n\t"
    "ds_read_b64_tr_b16 v[100:101], v5 offset:0\n\t"
    "ds_read_b64_tr_b16 v[102:103], v5 offset:256\n\t"
    "v_fma_f32 v81, v69, v80, -v79\n\t"
    "v_mfma_f32_32x32x16_bf16 v[128:143], v[104:107], a[228:231], v[128:143]\n\t"
    "v_cvt_pk_bf16_f32 v97, v78, v79\n\t"
    "v_fma_f32 v82, v69, v81, -v80\n\t"
    "s_add_u32 s71, s79, 3\n\t"
    "s_and_b32 s71, s71, 7\n\t"
    "s_lshl_b32 s71, s71, 12\n\t"
    "s_add_u32 s71, s71, s76\n\t"
    "buffer_load_dwordx4 v[144:147], v6, s[40:43], s71 offen offset:3072 sc1\n\t"
    "s_add_u32 s71, s79, 4\n\t"
    "s_and_b32 s71, s71, 7\n\t"
    "s_lshl_b32 s71, s71, 12\n\t"
    "s_add_u32 s71, s71, s76\n\t"
    "buffer_load_dwordx4 v[148:151], v6, s[40:43], s71 offen offset:3072 sc1\n\t"
    "s_waitcnt vmcnt(24) lgkmcnt(2)\n\t"
    "v_mfma_f32_32x32x16_bf16 v[112:127], v[108:111], a[232:235], v[112:127]\n\t"
    "ds_read_b64_tr_b16 v[104:105], v5 offset:1024\n\t"
    "ds_read_b64_tr_b16 v[106:107], v5 offset:1280\n\t"
    "v_fma_f32 v83, v69, v82, -v81\n\t"
    "v_mfma_f32_32x32x16_bf16 v[128:143], v[108:111], a[236:239], v[128:143]\n\t"
    "v_cvt_pk_bf16_f32 v98, v80, v81\n\t"
    "v_cvt_pk_bf16_f32 v99, v82, v83\n\t"
    "s_add_u32 s71, s79, 5\n\t"
    "s_and_b32 s71, s71, 7\n\t"
    "s_lshl_b32 s71, s71, 12\n\t"
    "s_add_u32 s71, s71, s76\n\t"
    "buffer_load_dwordx4 v[152:155], v6, s[40:43], s71 offen offset:3072 sc1\n\t"
    "s_add_u32 s71, s79, 6\n\t"
    "s_and_b32 s71, s71, 7\n\t"
    "s_lshl_b32 s71, s71, 12\n\t"
    "s_add_u32 s71, s71, s76\n\t"
    "buffer_load_dwordx4 v[156:159], v6, s[40:43], s71 offen offset:3072 sc1\n\t"
    "v_mfma_f32_32x32x16_bf16 v[112:127], v[96:99], v[208:211], v[112:127]\n\t"
    "v_mfma_f32_32x32x16_bf16 v[128:143], v[96:99], v[212:215], v[128:143]\n\t"
    "s_waitcnt vmcnt(24) lgkmcnt(2)\n\t"
    "v_mfma_f32_32x32x16_bf16 v[112:127], v[100:103], a[240:243], v[112:127]\n\t"
    "ds_read_b64_tr_b16 v[108:109], v5 offset:2048\n\t"
    "ds_read_b64_tr_b16 v[110:111], v5 offset:2304\n\t"
    "v_fmaak_f32 v70, v71, v75, 0x3e800000\n\t"
    "v_mfma_f32_32x32x16_bf16 v[128:143], v[100:103], a[244:247], v[128:143]\n\t"
    "v_fract_f32 v70, v70\n\t"
    "s_add_u32 s71, s79, 7\n\t"
    "s_and_b32 s71, s71, 7\n\t"
    "s_lshl_b32 s71, s71, 12\n\t"
    "s_add_u32 s71, s71, s76\n\t"
    "buffer_load_dwordx4 v[160:163], v6, s[40:43], s71 offen offset:3072 sc1\n\t"
    "s_add_u32 s71, s80, 5\n\t"
    "s_and_b32 s71, s71, 7\n\t"
    "s_lshl_b32 s71, s71, 12\n\t"
    "s_add_u32 s71, s71, s77\n\t"
    "buffer_load_dwordx4 v[164:167], v68, s[40:43], s71 offen sc1\n\t"
    "s_waitcnt vmcnt(24) lgkmcnt(2)\n\t"
    "v_mfma_f32_32x32x16_bf16 v[112:127], v[104:107], a[248:251], v[112:127]\n\t"
    "ds_read_b64_tr_b16 v[100:101], v5 offset:3072\n\t"
    "ds_read_b64_tr_b16 v[102:103], v5 offset:3328\n\t"
    "v_cos_f32 v76, v70\n\t"
    "v_mfma_f32_32x32x16_bf16 v[128:143], v[104:107], a[252:255], v[128:143]\n\t"
    "v_add_f32 v70, v70, v72\n\t"
    "s_add_u32 s71, s80, 6\n\t"
    "s_and_b32 s71, s71, 7\n\t"
    "s_lshl_b32 s71, s71, 12\n\t"
    "s_add_u32 s71, s71, s77\n\t"
    "buffer_load_dwordx4 v[168:171], v68, s[40:43], s71 offen sc1\n\t"
    "s_add_u32 s71, s80, 7\n\t"
    "s_and_b32 s71, s71, 7\n\t"
    "s_lshl_b32 s71, s71, 12\n\t"
    "s_add_u32 s71, s71, s77\n\t"
    "buffer_load_dwordx4 v[172:175], v68, s[40:43], s71 offen sc1\n\t"
    "s_waitcnt vmcnt(24) lgkmcnt(2)\n\t"
    "v_mfma_f32_32x32x16_bf16 v[112:127], v[108:111], v[224:227], v[112:127]\n\t"
    "ds_read_b64_tr_b16 v[104:105], v5 offset:4096\n\t"
    "ds_read_b64_tr_b16 v[106:107], v5 offset:4352\n\t"
    "v_cos_f32 v77, v70\n\t"
    "v_mfma_f32_32x32x16_bf16 v[128:143], v[108:111], v[228:231], v[128:143]\n\t"
    "s_nop 0\n\t"
    "v_fma_f32 v78, v69, v77, -v76\n\t"
    "s_waitcnt vmcnt(22) lgkmcnt(2)\n\t"
    "s_bfe_u32 s70, s89, 327695\n\t"
    "s_lshl_b32 s70, s70, 6\n\t"
    "s_add_u32 s70, s70, s92\n\t"
    "buffer_store_dword v19, v7, s[40:43], s70 offen sc1\n\t"
    "v_mfma_f32_32x32x16_bf16 v[112:127], v[100:103], v[232:235], v[112:127]\n\t"
    "ds_read_b64_tr_b16 v[108:109], v5 offset:5120\n\t"
    "ds_read_b64_tr_b16 v[110:111], v5 offset:5376\n\t"
    "v_fma_f32 v79, v69, v78, -v77\n\t"
    "v_mfma_f32_32x32x16_bf16 v[128:143], v[100:103], v[236:239], v[128:143]\n\t"
    "v_cvt_pk_bf16_f32 v92, v76, v77\n\t"
    "v_fma_f32 v80, v69, v79, -v78\n\t"
    "s_waitcnt lgkmcnt(2)\n\t"
    "v_mfma_f32_32x32x16_bf16 v[112:127], v[104:107], v[240:243], v[112:127]\n\t"
    "v_fma_f32 v81, v69, v80, -v79\n\t"
    "v_mfma_f32_32x32x16_bf16 v[128:143], v[104:107], v[244:247], v[128:143]\n\t"
    "v_cvt_pk_bf16_f32 v93, v78, v79\n\t"
    "v_fma_f32 v82, v69, v81, -v80\n\t"
    "s_waitcnt lgkmcnt(0)\n\t"
    "v_mfma_f32_32x32x16_bf16 v[112:127], v[108:111], v[248:251], v[112:127]\n\t"
    "v_fma_f32 v83, v69, v82, -v81\n\t"
    "v_mfma_f32_32x32x16_bf16 v[128:143], v[108:111], v[252:255], v[128:143]\n\t"
    "v_cvt_pk_bf16_f32 v94, v80, v81\n\t"
    "v_cvt_pk_bf16_f32 v95, v82, v83\n\t"
    "s_nop 1\n\t"
    "v_mfma_f32_32x32x16_bf16 v[112:127], v[92:95], v[216:219], v[112:127]\n\t"
    "v_mfma_f32_32x32x16_bf16 v[128:143], v[92:95], v[220:223], v[128:143]\n\t"
    "s_mov_b32 s73, 0\n\t"
    "buffer_load_dword v11, v67, s[40:43], s73 offen sc1\n\t"
    "v_lshlrev_b32 v16, 4, v1\n\t"
    "s_xor_b32 s70, s64, 1\n\t"
    "s_mul_i32 s70, s70, 12288\n\t"
    "s_add_u32 s70, s70, 74752\n\t"
    "v_add_u32 v17, s70, v16\n\t"
    "s_mul_i32 s70, s64, 12288\n\t"
    "s_add_u32 s70, s70, 74752\n\t"
    "v_add_u32 v16, s70, v16\n\t"
    "s_nop 0\n\t"
    "ds_write_b128 v16, v[112:115] offset:0\n\t"
    "ds_write_b128 v16, v[116:119] offset:1024\n\t"
    "ds_write_b128 v16, v[120:123] offset:2048\n\t"
    "ds_write_b128 v16, v[124:127] offset:3072\n\t"
    "s_waitcnt lgkmcnt(0)\n\t"
    "s_barrier\n\t"
    "ds_read_b128 v[112:115], v17 offset:0\n\t"
    "ds_read_b128 v[116:119], v17 offset:1024\n\t"
    "ds_read_b128 v[120:123], v17 offset:2048\n\t"
    "ds_read_b128 v[124:127], v17 offset:3072\n\t"
    "v_mov_b32 v76, 0\n\t"
    "v_mov_b32 v77, 0\n\t"
    "v_mov_b32 v78, 0\n\t"
    "v_mov_b32 v79, 0\n\t"
    "v_mov_b32 v80, 0\n\t"
    "v_mov_b32 v81, 0\n\t"
    "v_mov_b32 v82, 0\n\t"
    "v_mov_b32 v83, 0\n\t"
    "s_mov_b32 s74, 0x00003f80\n\t"
    "s_mov_b32 s75, 0x3f800000\n\t"
    "s_waitcnt lgkmcnt(3)\n\t"
    "v_add_f32 v128, v112, v128\n\t"
    "v_add_f32 v129, v113, v129\n\t"
    "v_add_f32 v130, v114, v130\n\t"
    "v_add_f32 v131, v115, v131\n\t"
    "s_waitcnt lgkmcnt(2)\n\t"
    "v_add_f32 v132, v116, v132\n\t"
    "v_add_f32 v133, v117, v133\n\t"
    "v_add_f32 v134, v118, v134\n\t"
    "v_add_f32 v135, v119, v135\n\t"
    "s_waitcnt lgkmcnt(1)\n\t"
    "v_add_f32 v136, v120, v136\n\t"
    "v_add_f32 v137, v121, v137\n\t"
    "v_add_f32 v138, v122, v138\n\t"
    "v_add_f32 v139, v123, v139\n\t"
    "s_waitcnt lgkmcnt(0)\n\t"
    "v_add_f32 v140, v124, v140\n\t"
    "v_add_f32 v141, v125, v141\n\t"
    "v_add_f32 v142, v126, v142\n\t"
    "v_add_f32 v143, v127, v143\n\t"
    "s_waitcnt vmcnt(17)\n\t"
    "v_dot2c_f32_bf16 v128, s74, v20\n\t"
    "v_dot2c_f32_bf16 v129, s75, v20\n\t"
    "v_dot2c_f32_bf16 v130, s74, v21\n\t"
    "v_dot2c_f32_bf16 v131, s75, v21\n\t"
    "v_dot2c_f32_bf16 v132, s74, v22\n\t"
    "v_dot2c_f32_bf16 v133, s75, v22\n\t"
    "v_dot2c_f32_bf16 v134, s74, v23\n\t"
    "v_dot2c_f32_bf16 v135, s75, v23\n\t"
    "s_waitcnt vmcnt(16)\n\t"
    "v_dot2c_f32_bf16 v128, s74, v24\n\t"
    "v_dot2c_f32_bf16 v129, s75, v24\n\t"
    "v_dot2c_f32_bf16 v130, s74, v25\n\t"
    "v_dot2c_f32_bf16 v131, s75, v25\n\t"
    "v_dot2c_f32_bf16 v132, s74, v26\n\t"
    "v_dot2c_f32_bf16 v133, s75, v26\n\t"
    "v_dot2c_f32_bf16 v134, s74, v27\n\t"
    "v_dot2c_f32_bf16 v135, s75, v27\n\t"
    "s_waitcnt vmcnt(15)\n\t"
    "v_dot2c_f32_bf16 v128, s74, v28\n\t"
    "v_dot2c_f32_bf16 v129, s75, v28\n\t"
    "v_dot2c_f32_bf16 v130, s74, v29\n\t"
    "v_dot2c_f32_bf16 v131, s75, v29\n\t"
    "v_dot2c_f32_bf16 v132, s74, v30\n\t"
    "v_dot2c_f32_bf16 v133, s75, v30\n\t"
    "v_dot2c_f32_bf16 v134, s74, v31\n\t"
    "v_dot2c_f32_bf16 v135, s75, v31\n\t"
    "s_waitcnt vmcnt(14)\n\t"
    "v_dot2c_f32_bf16 v128, s74, v32\n\t"
    "v_dot2c_f32_bf16 v129, s75, v32\n\t"
    "v_dot2c_f32_bf16 v130, s74, v33\n\t"
    "v_dot2c_f32_bf16 v131, s75, v33\n\t"
    "v_dot2c_f32_bf16 v132, s74, v34\n\t"
    "v_dot2c_f32_bf16 v133, s75, v34\n\t"
    "v_dot2c_f32_bf16 v134, s74, v35\n\t"
    "v_dot2c_f32_bf16 v135, s75, v35\n\t"
    "s_waitcnt vmcnt(13)\n\t"
    "v_dot2c_f32_bf16 v128, s74, v36\n\t"
    "v_dot2c_f32_bf16 v129, s75, v36\n\t"
    "v_dot2c_f32_bf16 v130, s74, v37\n\t"
    "v_dot2c_f32_bf16 v131, s75, v37\n\t"
    "v_dot2c_f32_bf16 v132, s74, v38\n\t"
    "v_dot2c_f32_bf16 v133, s75, v38\n\t"
    "v_dot2c_f32_bf16 v134, s74, v39\n\t"
    "v_dot2c_f32_bf16 v135, s75, v39\n\t"
    "s_waitcnt vmcnt(9)\n\t"
    "v_dot2c_f32_bf16 v136, s74, v144\n\t"
    "v_dot2c_f32_bf16 v137, s75, v144\n\t"
    "v_dot2c_f32_bf16 v138, s74, v145\n\t"
    "v_dot2c_f32_bf16 v139, s75, v145\n\t"
    "v_dot2c_f32_bf16 v140, s74, v146\n\t"
    "v_dot2c_f32_bf16 v141, s75, v146\n\t"
    "v_dot2c_f32_bf16 v142, s74, v147\n\t"
    "v_dot2c_f32_bf16 v143, s75, v147\n\t"
    "s_waitcnt vmcnt(8)\n\t"
    "v_dot2c_f32_bf16 v136, s74, v148\n\t"
    "v_dot2c_f32_bf16 v137, s75, v148\n\t"
    "v_dot2c_f32_bf16 v138, s74, v149\n\t"
    "v_dot2c_f32_bf16 v139, s75, v149\n\t"
    "v_dot2c_f32_bf16 v140, s74, v150\n\t"
    "v_dot2c_f32_bf16 v141, s75, v150\n\t"
    "v_dot2c_f32_bf16 v142, s74, v151\n\t"
    "v_dot2c_f32_bf16 v143, s75, v151\n\t"
    "s_waitcnt vmcnt(7)\n\t"
    "v_dot2c_f32_bf16 v136, s74, v152\n\t"
    "v_dot2c_f32_bf16 v137, s75, v152\n\t"
    "v_dot2c_f32_bf16 v138, s74, v153\n\t"
    "v_dot2c_f32_bf16 v139, s75, v153\n\t"
    "v_dot2c_f32_bf16 v140, s74, v154\n\t"
    "v_dot2c_f32_bf16 v141, s75, v154\n\t"
    "v_dot2c_f32_bf16 v142, s74, v155\n\t"
    "v_dot2c_f32_bf16 v143, s75, v155\n\t"
    "s_waitcnt vmcnt(6)\n\t"
    "v_dot2c_f32_bf16 v136, s74, v156\n\t"
    "v_dot2c_f32_bf16 v137, s75, v156\n\t"
    "v_dot2c_f32_bf16 v138, s74, v157\n\t"
    "v_dot2c_f32_bf16 v139, s75, v157\n\t"
    "v_dot2c_f32_bf16 v140, s74, v158\n\t"
    "v_dot2c_f32_bf16 v141, s75, v158\n\t"
    "v_dot2c_f32_bf16 v142, s74, v159\n\t"
    "v_dot2c_f32_bf16 v143, s75, v159\n\t"
    "s_waitcnt vmcnt(5)\n\t"
    "v_dot2c_f32_bf16 v136, s74, v160\n\t"
    "v_dot2c_f32_bf16 v137, s75, v160\n\t"
    "v_dot2c_f32_bf16 v138, s74, v161\n\t"
    "v_dot2c_f32_bf16 v139, s75, v161\n\t"
    "v_dot2c_f32_bf16 v140, s74, v162\n\t"
    "v_dot2c_f32_bf16 v141, s75, v162\n\t"
    "v_dot2c_f32_bf16 v142, s74, v163\n\t"
    "v_dot2c_f32_bf16 v143, s75, v163\n\t"
    "v_dot2c_f32_bf16 v76, s74, v40\n\t"
    "v_dot2c_f32_bf16 v77, s75, v40\n\t"
    "v_dot2c_f32_bf16 v78, s74, v41\n\t"
    "v_dot2c_f32_bf16 v79, s75, v41\n\t"
    "v_dot2c_f32_bf16 v80, s74, v42\n\t"
    "v_dot2c_f32_bf16 v81, s75, v42\n\t"
    "v_dot2c_f32_bf16 v82, s74, v43\n\t"
    "v_dot2c_f32_bf16 v83, s75, v43\n\t"
    "v_dot2c_f32_bf16 v76, s74, v44\n\t"
    "v_dot2c_f32_bf16 v77, s75, v44\n\t"
    "v_dot2c_f32_bf16 v78, s74, v45\n\t"
    "v_dot2c_f32_bf16 v79, s75, v45\n\t"
    "v_dot2c_f32_bf16 v80, s74, v46\n\t"
    "v_dot2c_f32_bf16 v81, s75, v46\n\t"
    "v_dot2c_f32_bf16 v82, s74, v47\n\t"
    "v_dot2c_f32_bf16 v83, s75, v47\n\t"
    "v_dot2c_f32_bf16 v76, s74, v48\n\t"
    "v_dot2c_f32_bf16 v77, s75, v48\n\t"
    "v_dot2c_f32_bf16 v78, s74, v49\n\t"
    "v_dot2c_f32_bf16 v79, s75, v49\n\t"
    "v_dot2c_f32_bf16 v80, s74, v50\n\t"
    "v_dot2c_f32_bf16 v81, s75, v50\n\t"
    "v_dot2c_f32_bf16 v82, s74, v51\n\t"
    "v_dot2c_f32_bf16 v83, s75, v51\n\t"
    "s_waitcnt vmcnt(4)\n\t"
    "v_dot2c_f32_bf16 v76, s74, v164\n\t"
    "v_dot2c_f32_bf16 v77, s75, v164\n\t"
    "v_dot2c_f32_bf16 v78, s74, v165\n\t"
    "v_dot2c_f32_bf16 v79, s75, v165\n\t"
    "v_dot2c_f32_bf16 v80, s74, v166\n\t"
    "v_dot2c_f32_bf16 v81, s75, v166\n\t"
    "v_dot2c_f32_bf16 v82, s74, v167\n\t"
    "v_dot2c_f32_bf16 v83, s75, v167\n\t"
    "s_waitcnt vmcnt(3)\n\t"
    "v_dot2c_f32_bf16 v76, s74, v168\n\t"
    "v_dot2c_f32_bf16 v77, s75, v168\n\t"
    "v_dot2c_f32_bf16 v78, s74, v169\n\t"
    "v_dot2c_f32_bf16 v79, s75, v169\n\t"
    "v_dot2c_f32_bf16 v80, s74, v170\n\t"
    "v_dot2c_f32_bf16 v81, s75, v170\n\t"
    "v_dot2c_f32_bf16 v82, s74, v171\n\t"
    "v_dot2c_f32_bf16 v83, s75, v171\n\t"
    "s_waitcnt vmcnt(2)\n\t"
    "v_dot2c_f32_bf16 v76, s74, v172\n\t"
    "v_dot2c_f32_bf16 v77, s75, v172\n\t"
    "v_dot2c_f32_bf16 v78, s74, v173\n\t"
    "v_dot2c_f32_bf16 v79, s75, v173\n\t"
    "v_dot2c_f32_bf16 v80, s74, v174\n\t"
    "v_dot2c_f32_bf16 v81, s75, v174\n\t"
    "v_dot2c_f32_bf16 v82, s74, v175\n\t"
    "v_dot2c_f32_bf16 v83, s75, v175\n\t"
    "s_waitcnt vmcnt(0)\n\t"
    "v_cmp_ne_u32 vcc, s67, v11\n\t"
    "s_mov_b32 s94, 0x40000\n\t"
    "s_cbranch_vccz L_ok_30%=\n\t"
    "L_spin_31%=:\n\t"
    "s_sleep 1\n\t"
    "buffer_load_dword v11, v67, s[40:43], s73 offen sc1\n\t"
    "s_waitcnt vmcnt(0)\n\t"
    "v_cmp_ne_u32 vcc, s67, v11\n\t"
    "s_sub_u32 s94, s94, 1\n\t"
    "s_nop 3\n\t"
    "s_cbranch_vccz L_ok_30%=\n\t"
    "s_cmp_lg_u32 s94, 0\n\t"
    "s_cbranch_scc1 L_spin_31%=\n\t"
    "L_ok_30%=:\n\t"
    "s_add_u32 s71, s79, 1\n\t"
    "s_and_b32 s71, s71, 7\n\t"
    "s_lshl_b32 s71, s71, 12\n\t"
    "s_add_u32 s71, s71, s76\n\t"
    "buffer_load_dwordx4 v[176:179], v6, s[40:43], s71 offen offset:2048 sc1\n\t"
    "s_add_u32 s71, s79, 1\n\t"
    "s_and_b32 s71, s71, 7\n\t"
    "s_lshl_b32 s71, s71, 12\n\t"
    "s_add_u32 s71, s71, s76\n\t"
    "buffer_load_dwordx4 v[184:187], v6, s[40:43], s71 offen offset:3072 sc1\n\t"
    "s_add_u32 s71, s80, 0\n\t"
    "s_and_b32 s71, s71, 7\n\t"
    "s_lshl_b32 s71, s71, 12\n\t"
    "s_add_u32 s71, s71, s77\n\t"
    "buffer_load_dwordx4 v[192:195], v68, s[40:43], s71 offen sc1\n\t"
    "s_add_u32 s71, s79, 2\n\t"
    "s_and_b32 s71, s71, 7\n\t"
    "s_lshl_b32 s71, s71, 12\n\t"
    "s_add_u32 s71, s71, s76\n\t"
    "buffer_load_dwordx4 v[180:183], v6, s[40:43], s71 offen offset:2048 sc1\n\t"
    "s_add_u32 s71, s79, 2\n\t"
    "s_and_b32 s71, s71, 7\n\t"
    "s_lshl_b32 s71, s71, 12\n\t"
    "s_add_u32 s71, s71, s76\n\t"
    "buffer_load_dwordx4 v[188:191], v6, s[40:43], s71 offen offset:3072 sc1\n\t"
    "s_add_u32 s71, s80, 1\n\t"
    "s_and_b32 s71, s71, 7\n\t"
    "s_lshl_b32 s71, s71, 12\n\t"
    "s_add_u32 s71, s71, s77\n\t"
    "buffer_load_dwordx4 v[196:199], v68, s[40:43], s71 offen sc1\n\t"
    "s_mov_b32 s73, 0x3f317218\n\t"
    "s_waitcnt vmcnt(5)\n\t"
    "v_dot2c_f32_bf16 v128, s74, v176\n\t"
    "v_dot2c_f32_bf16 v129, s75, v176\n\t"
    "v_dot2c_f32_bf16 v130, s74, v177\n\t"
    "v_dot2c_f32_bf16 v131, s75, v177\n\t"
    "v_dot2c_f32_bf16 v132, s74, v178\n\t"
    "v_dot2c_f32_bf16 v133, s75, v178\n\t"
    "v_dot2c_f32_bf16 v134, s74, v179\n\t"
    "v_dot2c_f32_bf16 v135, s75, v179\n\t"
    "s_waitcnt vmcnt(2)\n\t"
    "v_dot2c_f32_bf16 v128, s74, v180\n\t"
    "v_dot2c_f32_bf16 v129, s75, v180\n\t"
    "v_dot2c_f32_bf16 v130, s74, v181\n\t"
    "v_dot2c_f32_bf16 v131, s75, v181\n\t"
    "v_dot2c_f32_bf16 v132, s74, v182\n\t"
    "v_dot2c_f32_bf16 v133, s75, v182\n\t"
    "v_dot2c_f32_bf16 v134, s74, v183\n\t"
    "v_dot2c_f32_bf16 v135, s75, v183\n\t"
    "v_lshlrev_b32 v88, 16, v52\n\t"
    "v_and_b32 v89, 0xffff0000, v52\n\t"
    "v_fma_f32 v88, v128, s73, v88\n\t"
    "v_fma_f32 v89, v129, s73, v89\n\t"
    "v_cvt_pk_bf16_f32 v84, v88, v89\n\t"
    "v_lshlrev_b32 v88, 16, v53\n\t"
    "v_and_b32 v89, 0xffff0000, v53\n\t"
    "v_fma_f32 v88, v130, s73, v88\n\t"
    "v_fma_f32 v89, v131, s73, v89\n\t"
    "v_cvt_pk_bf16_f32 v85, v88, v89\n\t"
    "v_lshlrev_b32 v88, 16, v54\n\t"
    "v_and_b32 v89, 0xffff0000, v54\n\t"
    "v_fma_f32 v88, v132, s73, v88\n\t"
    "v_fma_f32 v89, v133, s73, v89\n\t"
    "v_cvt_pk_bf16_f32 v86, v88, v89\n\t"
    "v_lshlrev_b32 v88, 16, v55\n\t"
    "v_and_b32 v89, 0xffff0000, v55\n\t"
    "v_fma_f32 v88, v134, s73, v88\n\t"
    "v_fma_f32 v89, v135, s73, v89\n\t"
    "v_cvt_pk_bf16_f32 v87, v88, v89\n\t"
    "buffer_store_dwordx2 v[84:85], v64, s[60:63], 0 offen\n\t"
    "buffer_store_dwordx2 v[86:87], v64, s[60:63], 0 offen offset:16\n\t"
    "v_dot2c_f32_bf16 v136, s74, v184\n\t"
    "v_dot2c_f32_bf16 v137, s75, v184\n\t"
    "v_dot2c_f32_bf16 v138, s74, v185\n\t"
    "v_dot2c_f32_bf16 v139, s75, v185\n\t"
    "v_dot2c_f32_bf16 v140, s74, v186\n\t"
    "v_dot2c_f32_bf16 v141, s75, v186\n\t"
    "v_dot2c_f32_bf16 v142, s74, v187\n\t"
    "v_dot2c_f32_bf16 v143, s75, v187\n\t"
    "s_waitcnt vmcnt(3)\n\t"
    "v_dot2c_f32_bf16 v136, s74, v188\n\t"
    "v_dot2c_f32_bf16 v137, s75, v188\n\t"
    "v_dot2c_f32_bf16 v138, s74, v189\n\t"
    "v_dot2c_f32_bf16 v139, s75, v189\n\t"
    "v_dot2c_f32_bf16 v140, s74, v190\n\t"
    "v_dot2c_f32_bf16 v141, s75, v190\n\t"
    "v_dot2c_f32_bf16 v142, s74, v191\n\t"
    "v_dot2c_f32_bf16 v143, s75, v191\n\t"
    "v_lshlrev_b32 v88, 16, v56\n\t"
    "v_and_b32 v89, 0xffff0000, v56\n\t"
    "v_fma_f32 v88, v136, s73, v88\n\t"
    "v_fma_f32 v89, v137, s73, v89\n\t"
    "v_cvt_pk_bf16_f32 v84, v88, v89\n\t"
    "v_lshlrev_b32 v88, 16, v57\n\t"
    "v_and_b32 v89, 0xffff0000, v57\n\t"
    "v_fma_f32 v88, v138, s73, v88\n\t"
    "v_fma_f32 v89, v139, s73, v89\n\t"
    "v_cvt_pk_bf16_f32 v85, v88, v89\n\t"
    "v_lshlrev_b32 v88, 16, v58\n\t"
    "v_and_b32 v89, 0xffff0000, v58\n\t"
    "v_fma_f32 v88, v140, s73, v88\n\t"
    "v_fma_f32 v89, v141, s73, v89\n\t"
    "v_cvt_pk_bf16_f32 v86, v88, v89\n\t"
    "v_lshlrev_b32 v88, 16, v59\n\t"
    "v_and_b32 v89, 0xffff0000, v59\n\t"
    "v_fma_f32 v88, v142, s73, v88\n\t"
    "v_fma_f32 v89, v143, s73, v89\n\t"
    "v_cvt_pk_bf16_f32 v87, v88, v89\n\t"
    "buffer_store_dwordx2 v[84:85], v65, s[60:63], 0 offen\n\t"
    "buffer_store_dwordx2 v[86:87], v65, s[60:63], 0 offen offset:16\n\t"
    "v_dot2c_f32_bf16 v76, s74, v192\n\t"
    "v_dot2c_f32_bf16 v77, s75, v192\n\t"
    "v_dot2c_f32_bf16 v78, s74, v193\n\t"
    "v_dot2c_f32_bf16 v79, s75, v193\n\t"
    "v_dot2c_f32_bf16 v80, s74, v194\n\t"
    "v_dot2c_f32_bf16 v81, s75, v194\n\t"
    "v_dot2c_f32_bf16 v82, s74, v195\n\t"
    "v_dot2c_f32_bf16 v83, s75, v195\n\t"
    "s_waitcnt vmcnt(4)\n\t"
    "v_dot2c_f32_bf16 v76, s74, v196\n\t"
    "v_dot2c_f32_bf16 v77, s75, v196\n\t"
    "v_dot2c_f32_bf16 v78, s74, v197\n\t"
    "v_dot2c_f32_bf16 v79, s75, v197\n\t"
    "v_dot2c_f32_bf16 v80, s74, v198\n\t"
    "v_dot2c_f32_bf16 v81, s75, v198\n\t"
    "v_dot2c_f32_bf16 v82, s74, v199\n\t"
    "v_dot2c_f32_bf16 v83, s75, v199\n\t"
    "v_lshlrev_b32 v88, 16, v60\n\t"
    "v_and_b32 v89, 0xffff0000, v60\n\t"
    "v_fma_f32 v88, v76, s73, v88\n\t"
    "v_fma_f32 v89, v77, s73, v89\n\t"
    "v_cvt_pk_bf16_f32 v84, v88, v89\n\t"
    "v_lshlrev_b32 v88, 16, v61\n\t"
    "v_and_b32 v89, 0xffff0000, v61\n\t"
    "v_fma_f32 v88, v78, s73, v88\n\t"
    "v_fma_f32 v89, v79, s73, v89\n\t"
    "v_cvt_pk_bf16_f32 v85, v88, v89\n\t"
    "v_lshlrev_b32 v88, 16, v62\n\t"
    "v_and_b32 v89, 0xffff0000, v62\n\t"
    "v_fma_f32 v88, v80, s73, v88\n\t"
    "v_fma_f32 v89, v81, s73, v89\n\t"
    "v_cvt_pk_bf16_f32 v86, v88, v89\n\t"
    "v_lshlrev_b32 v88, 16, v63\n\t"
    "v_and_b32 v89, 0xffff0000, v63\n\t"
    "v_fma_f32 v88, v82, s73, v88\n\t"
    "v_fma_f32 v89, v83, s73, v89\n\t"
    "v_cvt_pk_bf16_f32 v87, v88, v89\n\t"
    "buffer_store_dwordx2 v[84:85], v66, s[60:63], 0 offen\n\t"
    "buffer_store_dwordx2 v[86:87], v66, s[60:63], 0 offen offset:16\n\t"
    "L_p2end_23%=:\n\t"
    "s_or_b32 s70, %8, s64\n\t"
    "s_cmp_eq_u32 s70, 0\n\t"
    "s_cselect_b64 vcc, -1, 0\n\t"
    "v_mov_b32 v15, 2146435072\n\t"
    "v_cndmask_b32 v15, v15, v10, vcc\n\t"
    "v_mov_b32 v16, s67\n\t"
    "buffer_store_dword v16, v15, s[40:43], 0 offen sc1\n\t"
    "L_end%=:\n\t"
    :
    : "s"(a.x), "s"(a.wr), "s"(a.wi), "s"(a.w1), "s"(a.b1), "s"(a.w2), "s"(a.out), "s"(a.ws), "s"(blockIdx.x), "v"(threadIdx.x)
    : "memory", "scc", "vcc", "v1", "v2", "v3", "v4", "v5", "v6", "v7", "v8", "v9", "v10", "v11", "v12", "v13", "v14", "v15", "v16", "v17", "v18", "v19", "v20", "v21", "v22", "v23", "v24", "v25", "v26", "v27", "v28", "v29", "v30", "v31", "v32", "v33", "v34", "v35", "v36", "v37", "v38", "v39", "v40", "v41", "v42", "v43", "v44", "v45", "v46", "v47", "v48", "v49", "v50", "v51", "v52", "v53", "v54", "v55", "v56", "v57", "v58", "v59", "v60", "v61", "v62", "v63", "v64", "v65", "v66", "v67", "v68", "v69", "v70", "v71", "v72", "v73", "v74", "v75", "v76", "v77", "v78", "v79", "v80", "v81", "v82", "v83", "v84", "v85", "v86", "v87", "v88", "v89", "v90", "v91", "v92", "v93", "v94", "v95", "v96", "v97", "v98", "v99", "v100", "v101", "v102", "v103", "v104", "v105", "v106", "v107", "v108", "v109", "v110", "v111", "v112", "v113", "v114", "v115", "v116", "v117", "v118", "v119", "v120", "v121", "v122", "v123", "v124", "v125", "v126", "v127", "v128", "v129", "v130", "v131", "v132", "v133", "v134", "v135", "v136", "v137", "v138", "v139", "v140", "v141", "v142", "v143", "v144", "v145", "v146", "v147", "v148", "v149", "v150", "v151", "v152", "v153", "v154", "v155", "v156", "v157", "v158", "v159", "v160", "v161", "v162", "v163", "v164", "v165", "v166", "v167", "v168", "v169", "v170", "v171", "v172", "v173", "v174", "v175", "v176", "v177", "v178", "v179", "v180", "v181", "v182", "v183", "v184", "v185", "v186", "v187", "v188", "v189", "v190", "v191", "v192", "v193", "v194", "v195", "v196", "v197", "v198", "v199", "v200", "v201", "v202", "v203", "v204", "v205", "v206", "v207", "v208", "v209", "v210", "v211", "v212", "v213", "v214", "v215", "v216", "v217", "v218", "v219", "v220", "v221", "v222", "v223", "v224", "v225", "v226", "v227", "v228", "v229", "v230", "v231", "v232", "v233", "v234", "v235", "v236", "v237", "v238", "v239", "v240", "v241", "v242", "v243", "v244", "v245", "v246", "v247", "v248", "v249", "v250", "v251", "v252", "v253", "v254", "v255", "a0", "a1", "a2", "a3", "a4", "a5", "a6", "a7", "a8", "a9", "a10", "a11", "a12", "a13", "a14", "a15", "a16", "a17", "a18", "a19", "a20", "a21", "a22", "a23", "a24", "a25", "a26", "a27", "a28", "a29", "a30", "a31", "a32", "a33", "a34", "a35", "a36", "a37", "a38", "a39", "a40", "a41", "a42", "a43", "a44", "a45", "a46", "a47", "a48", "a49", "a50", "a51", "a52", "a53", "a54", "a55", "a56", "a57", "a58", "a59", "a60", "a61", "a62", "a63", "a64", "a65", "a66", "a67", "a68", "a69", "a70", "a71", "a72", "a73", "a74", "a75", "a76", "a77", "a78", "a79", "a80", "a81", "a82", "a83", "a84", "a85", "a86", "a87", "a88", "a89", "a90", "a91", "a92", "a93", "a94", "a95", "a96", "a97", "a98", "a99", "a100", "a101", "a102", "a103", "a104", "a105", "a106", "a107", "a108", "a109", "a110", "a111", "a112", "a113", "a114", "a115", "a116", "a117", "a118", "a119", "a120", "a121", "a122", "a123", "a124", "a125", "a126", "a127", "a128", "a129", "a130", "a131", "a132", "a133", "a134", "a135", "a136", "a137", "a138", "a139", "a140", "a141", "a142", "a143", "a144", "a145", "a146", "a147", "a148", "a149", "a150", "a151", "a152", "a153", "a154", "a155", "a156", "a157", "a158", "a159", "a160", "a161", "a162", "a163", "a164", "a165", "a166", "a167", "a168", "a169", "a170", "a171", "a172", "a173", "a174", "a175", "a176", "a177", "a178", "a179", "a180", "a181", "a182", "a183", "a184", "a185", "a186", "a187", "a188", "a189", "a190", "a191", "a192", "a193", "a194", "a195", "a196", "a197", "a198", "a199", "a200", "a201", "a202", "a203", "a204", "a205", "a206", "a207", "a208", "a209", "a210", "a211", "a212", "a213", "a214", "a215", "a216", "a217", "a218", "a219", "a220", "a221", "a222", "a223", "a224", "a225", "a226", "a227", "a228", "a229", "a230", "a231", "a232", "a233", "a234", "a235", "a236", "a237", "a238", "a239", "a240", "a241", "a242", "a243", "a244", "a245", "a246", "a247", "a248", "a249", "a250", "a251", "a252", "a253", "a254", "a255", "s36", "s37", "s38", "s39", "s40", "s41", "s42", "s43", "s44", "s45", "s46", "s47", "s48", "s49", "s50", "s51", "s52", "s53", "s54", "s55", "s56", "s57", "s58", "s59", "s60", "s61", "s62", "s63", "s64", "s65", "s66", "s67", "s68", "s69", "s70", "s71", "s72", "s73", "s74", "s75", "s76", "s77", "s78", "s79", "s80", "s81", "s82", "s83", "s84", "s85", "s86", "s87", "s88", "s89", "s90", "s91", "s92", "s93", "s94", "s95", "s96", "s97", "s98", "s99");
}

extern "C" void kernel_launch(void* const* d_in, const int* in_sizes, int n_in, void* d_out, int out_size, void* d_ws, size_t ws_size,
                              hipStream_t stream) {
  Args a;
  a.x = d_in[0]; a.wr = d_in[1]; a.wi = d_in[2]; a.w1 = d_in[4]; a.b1 = d_in[5]; a.w2 = d_in[6]; a.out = d_out; a.ws = d_ws;
  (void)hipFuncSetAttribute((const void*)gfmlp_kernel, hipFuncAttributeMaxDynamicSharedMemorySize, 163840);
  gfmlp_kernel<<<256, 256, 163840, stream>>>(a);
}
```
